```python
import torch

def _softmax(x, axis=-1):
    return torch.softmax(x, dim=axis)
def _gelu(x):
    return torch.nn.functional.gelu(x, approximate="tanh")
def _silu(x):
    return torch.nn.functional.silu(x)
def _sigmoid(x):
    return torch.sigmoid(x)
def _layernorm(x, axis=-1, eps=1e-5):
    m = x.mean(dim=axis, keepdim=True)
    v = x.var(dim=axis, keepdim=True, unbiased=False)
    return (x - m) / torch.sqrt(v + eps)
def _scatter_add(tgt, idx, src, axis=0):
    return tgt.clone().index_add_(axis, idx, src)
def _segment_sum(src, seg, n):
    out = torch.zeros((n,) + src.shape[1:], dtype=src.dtype)
    return out.index_add_(0, seg, src)
def _pad(x, pw):
    flat = tuple(v for pair in reversed(pw) for v in pair)
    return torch.nn.functional.pad(x, flat)

kernel_name = 'concat_heads__gqa_block_cached__gqa_block_cached__near_ridge'

def setup_inputs():

    _w = (lambda a: (lambda t: t.to(torch.bfloat16) if t.is_floating_point() else t)(torch.as_tensor(a)))
    def _A_setup():
        B, Sq, D = 16, 80, 512
        Hq, Hkv, Sk, Dh = 32, 32, 80, 128
        assert Hq % Hkv == 0, (Hq, Hkv)
        return {
            "x": _w((lambda *s: torch.randn(*s, dtype=torch.bfloat16) * 0.8)(B, Sq, D)),
            "k_cache": _w((lambda *s: torch.randn(*s, dtype=torch.bfloat16) * 0.8)(B, Hkv, Sk, Dh)),
            "v_cache": _w((lambda *s: torch.randn(*s, dtype=torch.bfloat16) * 0.05)(B, Hkv, Sk, Dh)),
            "wq": _w((lambda *s: torch.randn(*s, dtype=torch.bfloat16) * 0.8)(D, Hq * Dh) * (D ** -0.5)),
            "wo": _w((lambda *s: torch.randn(*s, dtype=torch.bfloat16) * 0.05)(Hq * Dh, D) * ((Hq * Dh) ** -0.5)),
        }
    _A_in = _A_setup()

    def _B_setup():
        B, Sq, D = 16, 80, 512
        Hq, Hkv, Sk, Dh = 32, 32, 80, 128
        assert Hq % Hkv == 0, (Hq, Hkv)
        return {
            "x": _w((lambda *s: torch.randn(*s, dtype=torch.bfloat16) * 0.8)(B, Sq, D)),
            "k_cache": _w((lambda *s: torch.randn(*s, dtype=torch.bfloat16) * 0.8)(B, Hkv, Sk, Dh)),
            "v_cache": _w((lambda *s: torch.randn(*s, dtype=torch.bfloat16) * 0.05)(B, Hkv, Sk, Dh)),
            "wq": _w((lambda *s: torch.randn(*s, dtype=torch.bfloat16) * 0.8)(D, Hq * Dh) * (D ** -0.5)),
            "wo": _w((lambda *s: torch.randn(*s, dtype=torch.bfloat16) * 0.05)(Hq * Dh, D) * ((Hq * Dh) ** -0.5)),
        }
    _B_in = _B_setup()

    return {
        "A_x": _A_in["x"],
        "A_k_cache": _A_in["k_cache"],
        "A_v_cache": _A_in["v_cache"],
        "A_wq": _A_in["wq"],
        "A_wo": _A_in["wo"],
        "B_x": _B_in["x"],
        "B_k_cache": _B_in["k_cache"],
        "B_v_cache": _B_in["v_cache"],
        "B_wq": _B_in["wq"],
        "B_wo": _B_in["wo"],
    }

def reference(A_x, A_k_cache, A_v_cache, A_wq, A_wo, B_x, B_k_cache, B_v_cache, B_wq, B_wo):
    def _A_ref(x, k_cache, v_cache, wq, wo):
        B, Sq, D = x.shape
        Hq, Dh = 32, 128
        Hkv = k_cache.shape[1]
        rep = Hq // Hkv
        q = (lambda x, *d: x.permute(*d))((x @ wq).reshape(B, Sq, Hq, Dh), 0, 2, 1, 3)
        k = (lambda x, n, axis: torch.repeat_interleave(x, n, dim=axis))(k_cache, rep, axis=1)
        v = (lambda x, n, axis: torch.repeat_interleave(x, n, dim=axis))(v_cache, rep, axis=1)
        scores = torch.einsum("bhqd,bhkd->bhqk", q, k) * (Dh ** -0.5)
        p = _softmax(scores, axis=-1)
        o = torch.einsum("bhqk,bhkd->bhqd", p, v)
        return (lambda x, *d: x.permute(*d))(o, 0, 2, 1, 3).reshape(B, Sq, Hq * Dh) @ wo

    def _B_ref(x, k_cache, v_cache, wq, wo):
        B, Sq, D = x.shape
        Hq, Dh = 32, 128
        Hkv = k_cache.shape[1]
        rep = Hq // Hkv
        q = (lambda x, *d: x.permute(*d))((x @ wq).reshape(B, Sq, Hq, Dh), 0, 2, 1, 3)
        k = (lambda x, n, axis: torch.repeat_interleave(x, n, dim=axis))(k_cache, rep, axis=1)
        v = (lambda x, n, axis: torch.repeat_interleave(x, n, dim=axis))(v_cache, rep, axis=1)
        scores = torch.einsum("bhqd,bhkd->bhqk", q, k) * (Dh ** -0.5)
        p = _softmax(scores, axis=-1)
        o = torch.einsum("bhqk,bhkd->bhqd", p, v)
        return (lambda x, *d: x.permute(*d))(o, 0, 2, 1, 3).reshape(B, Sq, Hq * Dh) @ wo

    a = _A_ref(A_x, A_k_cache, A_v_cache, A_wq, A_wo)
    b = _B_ref(B_x, B_k_cache, B_v_cache, B_wq, B_wo)
    return torch.concatenate([a, b], axis=-1)
```

```cpp
#include <hip/hip_runtime.h>
#include <hip/hip_bf16.h>

typedef unsigned u32;
typedef unsigned short u16;
typedef u32 u32x4 __attribute__((ext_vector_type(4)));
typedef u32 u32x2 __attribute__((ext_vector_type(2)));
typedef float f32x4 __attribute__((ext_vector_type(4)));
typedef float f32x2 __attribute__((ext_vector_type(2)));
typedef short bf16x8 __attribute__((ext_vector_type(8)));
typedef short bf16x4 __attribute__((ext_vector_type(4)));
typedef __bf16 bf16x2 __attribute__((ext_vector_type(2)));
typedef __amdgpu_buffer_rsrc_t rsrc_t;
#define DI __device__ __forceinline__
#define LDS_AS __attribute__((address_space(3)))

#define RING_D 2

DI f32x4 mfma32(u32x4 a, u32x4 b, f32x4 c) {
  return __builtin_amdgcn_mfma_f32_16x16x32_bf16(__builtin_bit_cast(bf16x8, a), __builtin_bit_cast(bf16x8, b), c, 0, 0, 0);
}
DI f32x4 mfma16(u32x2 a, u32x2 b, f32x4 c) {
  return __builtin_amdgcn_mfma_f32_16x16x16bf16_1k(__builtin_bit_cast(bf16x4, a), __builtin_bit_cast(bf16x4, b), c, 0, 0, 0);
}
DI u32 pk(float lo, float hi) {
  f32x2 v = {lo, hi};
  return __builtin_bit_cast(u32, __builtin_convertvector(v, bf16x2));
}
DI u32 perm_lo(u32 hi_src, u32 lo_src) { return __builtin_amdgcn_perm(hi_src, lo_src, 0x05040100u); }
DI u32 perm_hi(u32 hi_src, u32 lo_src) { return __builtin_amdgcn_perm(hi_src, lo_src, 0x07060302u); }
DI rsrc_t mk_rsrc(const void* p, u32 bytes) { return __builtin_amdgcn_make_buffer_rsrc((void*)p, 0, bytes, 0x00020000); }
DI u32x4 bld(rsrc_t r, int voff, int soff) { return __builtin_amdgcn_raw_buffer_load_b128(r, voff, soff, 0); }
DI u32x4 lds_rd(u32 addr, int off) { return *(const LDS_AS u32x4*)((const LDS_AS char*)addr + off); }
DI void lds_wr(u32 addr, int off, u32x4 v) { *(LDS_AS u32x4*)((LDS_AS char*)addr + off) = v; }
DI float xmax16(float x) { auto r = __builtin_amdgcn_permlane16_swap(__builtin_bit_cast(u32, x), __builtin_bit_cast(u32, x), false, false);
  return fmaxf(__builtin_bit_cast(float, (u32)r[0]), __builtin_bit_cast(float, (u32)r[1])); }
DI float xmax32(float x) { auto r = __builtin_amdgcn_permlane32_swap(__builtin_bit_cast(u32, x), __builtin_bit_cast(u32, x), false, false);
  return fmaxf(__builtin_bit_cast(float, (u32)r[0]), __builtin_bit_cast(float, (u32)r[1])); }
DI float xsum16(float x) { auto r = __builtin_amdgcn_permlane16_swap(__builtin_bit_cast(u32, x), __builtin_bit_cast(u32, x), false, false);
  return __builtin_bit_cast(float, (u32)r[0]) + __builtin_bit_cast(float, (u32)r[1]); }
DI float xsum32(float x) { auto r = __builtin_amdgcn_permlane32_swap(__builtin_bit_cast(u32, x), __builtin_bit_cast(u32, x), false, false);
  return __builtin_bit_cast(float, (u32)r[0]) + __builtin_bit_cast(float, (u32)r[1]); }

DI void tr_pair(const u32x4& r0, const u32x4& r1, u32x4 (&A)[8], int p) {
#pragma unroll
  for (int q = 0; q < 4; ++q) { A[2 * q][p] = perm_lo(r1[q], r0[q]); A[2 * q + 1][p] = perm_hi(r1[q], r0[q]); }
}

constexpr int D = RING_D;
constexpr int KLAG = D + 1;

__global__ __launch_bounds__(256, 1) void fused_k(const u16* __restrict__ xA, const u16* __restrict__ kA, const u16* __restrict__ vA,
                                                  const u16* __restrict__ wqA, const u16* __restrict__ woA,
                                                  const u16* __restrict__ xB, const u16* __restrict__ kB, const u16* __restrict__ vB,
                                                  const u16* __restrict__ wqB, const u16* __restrict__ woB,
                                                  u32x4* __restrict__ part) {
  const int bid = blockIdx.x, xcd = bid & 7, idx = bid >> 3;
  const int br = xcd >> 2, hg = (xcd & 3) * 2 + (idx >> 4), b = idx & 15;
  const int tid = threadIdx.x, w = __builtin_amdgcn_readfirstlane(tid >> 6), l = tid & 63, g = l >> 4, c = l & 15;
  const int h = hg * 4 + w;
  const u16* x = br ? xB : xA; const u16* kc = br ? kB : kA; const u16* vc = br ? vB : vA;
  const u16* wq = br ? wqB : wqA; const u16* wo = br ? woB : woA;
  const rsrc_t rx = mk_rsrc(x + (size_t)b * 80 * 512, 81920);
  const rsrc_t rk = mk_rsrc(kc + (size_t)(b * 32 + h) * 10240, 20480);
  const rsrc_t rv = mk_rsrc(vc + (size_t)(b * 32 + h) * 10240, 20480);
  const rsrc_t rq = mk_rsrc(wq + h * 128, 4194304 - h * 256);
  const rsrc_t ro = mk_rsrc(wo + (size_t)hg * 512 * 512 + w * 128, 524288 - w * 256);
  const int rot = b;

  u32x4 xr[20];
#pragma unroll
  for (int i = 0; i < 20; ++i) xr[i] = bld(rx, l * 16, (w * 20 + i) * 1024);
  const int vq = g * 8 * 8192 + c * 16, vo = g * 8 * 1024 + c * 16;
  u32x4 ring[D][8];
#pragma unroll
  for (int s = 0; s < D; ++s) {
    const int ks = (s + rot) & 15;
#pragma unroll
    for (int j = 0; j < 8; ++j) ring[s][j] = bld(rq, vq, (ks * 32 + j) * 8192);
  }
#pragma unroll
  for (int i = 0; i < 20; ++i) { const int row = w * 20 + i; lds_wr(((row & 15) * 5 + (row >> 4)) * 1024 + ((l ^ (row & 15)) << 4), 0, xr[i]); }
  __syncthreads();

  u32 bbase = c * 5120 + ((g ^ (c & 3)) << 4) + ((c & 12) << 4);
  f32x4 acc[8][5];
#pragma unroll
  for (int i = 0; i < 8; ++i)
#pragma unroll
    for (int m = 0; m < 5; ++m) acc[i][m] = f32x4{0.f, 0.f, 0.f, 0.f};
  u32x4 Kt[20], Vr[20];
  const int vv = g * 1024 + c * 16;
  const u32 kb = 81920 + w * 20480;

  u32x4 A[2][8], Bf[2][5];
  {
#pragma unroll
    for (int p = 0; p < 4; ++p) tr_pair(ring[0][2 * p], ring[0][2 * p + 1], A[0], p);
    const u32 a = bbase ^ (u32)(rot * 64);
#pragma unroll
    for (int m = 0; m < 5; ++m) Bf[0][m] = lds_rd(a, m * 1024);
    const int ks = (D + rot) & 15;
#pragma unroll
    for (int j = 0; j < 8; ++j) ring[0][j] = bld(rq, vq, (ks * 32 + j) * 8192);
  }
  __builtin_amdgcn_sched_barrier(0);
#pragma unroll
  for (int s = 0; s < 16; ++s) {
    const int cur = s & 1, nxt = cur ^ 1, rs = (s + 1) % D;
    if (s < 15) {
      const u32 a = bbase ^ (u32)(((s + 1 + rot) & 15) * 64);
#pragma unroll
      for (int m = 0; m < 5; ++m) Bf[nxt][m] = lds_rd(a, m * 1024);
    }
#pragma unroll
    for (int p = 0; p < 4; ++p) {
#pragma unroll
      for (int i = 2 * p; i < 2 * p + 2; ++i)
#pragma unroll
        for (int m = 0; m < 5; ++m) acc[i][m] = mfma32(A[cur][i], Bf[cur][m], acc[i][m]);
      if (s < 15) {
        tr_pair(ring[rs][2 * p], ring[rs][2 * p + 1], A[nxt], p);
        const int n = s + 1 + D;
        const int ks = (n + rot) & 15;
        if (n < 16) {
          ring[rs][2 * p] = bld(rq, vq, (ks * 32 + 2 * p) * 8192);
          ring[rs][2 * p + 1] = bld(rq, vq, (ks * 32 + 2 * p + 1) * 8192);
        } else {
          ring[rs][2 * p] = bld(ro, vo, (ks * 32 + 2 * p) * 1024);
          ring[rs][2 * p + 1] = bld(ro, vo, (ks * 32 + 2 * p + 1) * 1024);
        }
      }
    }
#pragma unroll
    for (int q = s * 5 / 2; q < (s + 1) * 5 / 2; ++q) {
      if (q < 20) Kt[q] = bld(rk, l * 16, q * 1024);
      else { const int t = q - 20, sv = t >> 3, j = t & 7; Vr[t] = bld(rv, vv, (32 * sv + 16 * (j >> 2) + (j & 3)) * 256); }
    }
    if (s >= KLAG) {
#pragma unroll
      for (int q = (s - KLAG) * 5 / 2; q < (s - KLAG + 1) * 5 / 2; ++q)
        if (q < 20) lds_wr(kb + g * 256 + ((c ^ ((4 * (q & 3) + g + 12) & 15)) << 4), q * 1024, Kt[q]);
    }
    __builtin_amdgcn_sched_barrier(0);
  }
  __syncthreads();

  u32x4 Qf[5][4];
#pragma unroll
  for (int m = 0; m < 5; ++m)
#pragma unroll
    for (int r = 0; r < 4; ++r)
#pragma unroll
      for (int q = 0; q < 4; ++q) Qf[m][r][q] = pk(acc[2 * q][m][r], acc[2 * q + 1][m][r]);
  u32x4 Vf0[8], Vf1[8]; u32x2 Vf2[8];
#pragma unroll
  for (int p = 0; p < 4; ++p) { tr_pair(Vr[2 * p], Vr[2 * p + 1], Vf0, p); tr_pair(Vr[8 + 2 * p], Vr[8 + 2 * p + 1], Vf1, p); }
#pragma unroll
  for (int p = 0; p < 2; ++p)
#pragma unroll
    for (int q = 0; q < 4; ++q) { Vf2[2 * q][p] = perm_lo(Vr[16 + 2 * p + 1][q], Vr[16 + 2 * p][q]); Vf2[2 * q + 1][p] = perm_hi(Vr[16 + 2 * p + 1][q], Vr[16 + 2 * p][q]); }
  u32x4 Kf[20];
#pragma unroll
  for (int kt = 0; kt < 5; ++kt)
#pragma unroll
    for (int r = 0; r < 4; ++r) Kf[kt * 4 + r] = lds_rd(kb + c * 256 + (((4 * g + r) ^ ((c + 12) & 15)) << 4), kt * 4096);
  const float cs = 0.08838834764831845f * 1.4426950408889634f;
#pragma unroll
  for (int m = 0; m < 5; ++m) {
    f32x4 S[5];
#pragma unroll
    for (int kt = 0; kt < 5; ++kt) {
      S[kt] = f32x4{0.f, 0.f, 0.f, 0.f};
#pragma unroll
      for (int r = 0; r < 4; ++r) S[kt] = mfma32(Kf[kt * 4 + r], Qf[m][r], S[kt]);
    }
    float mx = S[0][0];
#pragma unroll
    for (int kt = 0; kt < 5; ++kt)
#pragma unroll
      for (int r = 0; r < 4; ++r) mx = fmaxf(mx, S[kt][r]);
    mx = xmax32(xmax16(mx));
    const float mc = mx * cs;
    float sum = 0.f;
#pragma unroll
    for (int kt = 0; kt < 5; ++kt)
#pragma unroll
      for (int r = 0; r < 4; ++r) { S[kt][r] = __builtin_amdgcn_exp2f(__builtin_fmaf(S[kt][r], cs, -mc)); sum += S[kt][r]; }
    sum = xsum32(xsum16(sum));
    const float inv = __builtin_amdgcn_rcpf(sum);
    u32x4 P0, P1; u32x2 P2;
    P0[0] = pk(S[0][0] * inv, S[0][1] * inv); P0[1] = pk(S[0][2] * inv, S[0][3] * inv);
    P0[2] = pk(S[1][0] * inv, S[1][1] * inv); P0[3] = pk(S[1][2] * inv, S[1][3] * inv);
    P1[0] = pk(S[2][0] * inv, S[2][1] * inv); P1[1] = pk(S[2][2] * inv, S[2][3] * inv);
    P1[2] = pk(S[3][0] * inv, S[3][1] * inv); P1[3] = pk(S[3][2] * inv, S[3][3] * inv);
    P2[0] = pk(S[4][0] * inv, S[4][1] * inv); P2[1] = pk(S[4][2] * inv, S[4][3] * inv);
    f32x4 O[8];
#pragma unroll
    for (int i = 0; i < 8; ++i) {
      O[i] = mfma32(Vf0[i], P0, f32x4{0.f, 0.f, 0.f, 0.f});
      O[i] = mfma32(Vf1[i], P1, O[i]);
      O[i] = mfma16(Vf2[i], P2, O[i]);
    }
#pragma unroll
    for (int r = 0; r < 4; ++r) {
      u32x4 o;
#pragma unroll
      for (int q = 0; q < 4; ++q) o[q] = pk(O[2 * q][r], O[2 * q + 1][r]);
      lds_wr(c * 5120 + (((w * 16 + 4 * g + r) ^ c) << 4), m * 1024, o);
    }
  }
  __syncthreads();

  asm volatile("" : "+v"(bbase));
#pragma unroll
  for (int i = 0; i < 8; ++i)
#pragma unroll
    for (int m = 0; m < 5; ++m) acc[i][m] = f32x4{0.f, 0.f, 0.f, 0.f};
  {
    constexpr int rs = 16 % D;
#pragma unroll
    for (int p = 0; p < 4; ++p) tr_pair(ring[rs][2 * p], ring[rs][2 * p + 1], A[0], p);
    const u32 a = bbase ^ (u32)(rot * 64);
#pragma unroll
    for (int m = 0; m < 5; ++m) Bf[0][m] = lds_rd(a, m * 1024);
    const int ks = (D + rot) & 15;
#pragma unroll
    for (int j = 0; j < 8; ++j) ring[rs][j] = bld(ro, vo, (ks * 32 + j) * 1024);
  }
  __builtin_amdgcn_sched_barrier(0);
#pragma unroll
  for (int s = 0; s < 16; ++s) {
    const int cur = s & 1, nxt = cur ^ 1, rs = (16 + s + 1) % D;
    if (s < 15) {
      const u32 a = bbase ^ (u32)(((s + 1 + rot) & 15) * 64);
#pragma unroll
      for (int m = 0; m < 5; ++m) Bf[nxt][m] = lds_rd(a, m * 1024);
    }
#pragma unroll
    for (int p = 0; p < 4; ++p) {
#pragma unroll
      for (int i = 2 * p; i < 2 * p + 2; ++i)
#pragma unroll
        for (int m = 0; m < 5; ++m) acc[i][m] = mfma32(A[cur][i], Bf[cur][m], acc[i][m]);
      if (s < 15) {
        tr_pair(ring[rs][2 * p], ring[rs][2 * p + 1], A[nxt], p);
        const int n = s + 1 + D;
        const int ks = (n + rot) & 15;
        if (n < 16) {
          ring[rs][2 * p] = bld(ro, vo, (ks * 32 + 2 * p) * 1024);
          ring[rs][2 * p + 1] = bld(ro, vo, (ks * 32 + 2 * p + 1) * 1024);
        }
      }
    }
    __builtin_amdgcn_sched_barrier(0);
  }
  u32x4* pp = part + ((size_t)(((br * 16 + b) * 8 + hg) * 4 + w) * 20) * 64 + l;
#pragma unroll
  for (int m = 0; m < 5; ++m)
#pragma unroll
    for (int r = 0; r < 4; ++r) {
      u32x4 o;
#pragma unroll
      for (int q = 0; q < 4; ++q) o[q] = pk(acc[2 * q][m][r], acc[2 * q + 1][m][r]);
      pp[(m * 4 + r) * 64] = o;
    }
}

__global__ __launch_bounds__(256) void reduce_k(const u32x4* __restrict__ part, u16* __restrict__ out) {
  const int t = blockIdx.x * 256 + threadIdx.x;
  const int bb = t / 5120, rem = t - bb * 5120;
  const u32x4* p = part + (size_t)bb * 8 * 5120 + rem;
  u32x4 v[8];
#pragma unroll
  for (int i = 0; i < 8; ++i) v[i] = p[i * 5120];
  float s[8];
#pragma unroll
  for (int q = 0; q < 4; ++q) {
    float lo = 0.f, hi = 0.f;
#pragma unroll
    for (int i = 0; i < 8; ++i) { lo += __builtin_bit_cast(float, v[i][q] << 16); hi += __builtin_bit_cast(float, v[i][q] & 0xffff0000u); }
    s[2 * q] = lo; s[2 * q + 1] = hi;
  }
  const int l = rem & 63, ch = rem >> 6, r = ch & 3, m = (ch >> 2) % 5, w = ch / 20;
  const int g = l >> 4, c = l & 15, br = bb >> 4, b = bb & 15;
  u32x4 o;
#pragma unroll
  for (int q = 0; q < 4; ++q) o[q] = pk(s[2 * q], s[2 * q + 1]);
  *(u32x4*)(out + (size_t)(b * 80 + m * 16 + c) * 1024 + br * 512 + w * 128 + 32 * g + 8 * r) = o;
}

extern "C" void kernel_launch(void* const* d_in, const int* in_sizes, int n_in,
                              void* d_out, int out_size, void* d_ws, size_t ws_size,
                              hipStream_t stream) {
  (void)hipFuncSetAttribute((const void*)fused_k, hipFuncAttributeMaxDynamicSharedMemorySize, 163840);
  fused_k<<<256, 256, 163840, stream>>>((const u16*)d_in[0], (const u16*)d_in[1], (const u16*)d_in[2], (const u16*)d_in[3], (const u16*)d_in[4],
                                       (const u16*)d_in[5], (const u16*)d_in[6], (const u16*)d_in[7], (const u16*)d_in[8], (const u16*)d_in[9],
                                       (u32x4*)d_ws);
  reduce_k<<<640, 256, 0, stream>>>((const u32x4*)d_ws, (u16*)d_out);
}
```

```cpp
#include <hip/hip_runtime.h>
#include <hip/hip_bf16.h>


typedef unsigned u32;


__global__ __launch_bounds__(256, 1) void fused_k(const char* __restrict__ xA, const char* __restrict__ kA, const char* __restrict__ vA,
                                                  const char* __restrict__ wqA, const char* __restrict__ woA,
                                                  const char* __restrict__ xB, const char* __restrict__ kB, const char* __restrict__ vB,
                                                  const char* __restrict__ wqB, const char* __restrict__ woB,
                                                  char* __restrict__ ws, char* __restrict__ out) {
  const u32 w = __builtin_amdgcn_readfirstlane(threadIdx.x >> 6);
  asm volatile(
"s_bfe_u32 s70, %1, 0x10002\n"
"s_mul_i32 s70, s70, 40\n"
"s_load_dwordx2 s[20:21], %0, s70 offset:24\n"
"s_load_dwordx2 s[8:9], %0, s70\n"
"s_load_dwordx2 s[12:13], %0, s70 offset:8\n"
"s_load_dwordx2 s[16:17], %0, s70 offset:16\n"
"s_load_dwordx2 s[24:25], %0, s70 offset:32\n"
"s_load_dwordx2 s[28:29], %0, 80\n"
"s_load_dwordx2 s[40:41], %0, 88\n"
"v_mbcnt_lo_u32_b32 v16, -1, 0\n"
"v_mbcnt_hi_u32_b32 v16, -1, v16\n"
"s_lshr_b32 s79, %1, 4\n"
"s_xor_b32 s79, s79, %1\n"
"s_bfe_u32 s79, s79, 0x10003\n"
"s_getpc_b64 s[44:45]\n"
"LPC%=:\n"
"s_mov_b32 s46, LEND%=-LPC%=\n"
"s_mov_b32 s47, 0x20000\n"
"s_lshr_b32 s71, %1, 4\n"
"s_lshl2_add_u32 s71, s71, %2\n"
"s_mul_i32 s71, s71, 640\n"
"s_mul_i32 s72, s79, LG1%=-LPC%=\n"
"s_add_u32 s71, s71, s72\n"
"v_lshlrev_b32 v19, 7, v16\n"
"s_mov_b64 exec, 31\n"
"buffer_load_dword v29, v19, s[44:47], s71 offen\n"
"s_mov_b64 exec, -1\n"
"s_bfe_u32 s71, %1, 0x40003\n"
"s_and_b32 s34, %1, 3\n"
"s_lshl_b32 s34, s34, 1\n"
"s_lshr_b32 s72, %1, 7\n"
"s_add_u32 s34, s34, s72\n"
"s_and_b32 s33, s71, 15\n"
"s_lshl2_add_u32 s72, s34, %2\n"
"s_lshl_b32 s73, s72, 8\n"
"s_sub_u32 s22, 4194304, s73\n"
"s_mov_b32 s23, 0x20000\n"
"v_lshrrev_b32 v17, 4, v16\n"
"v_and_b32 v18, 15, v16\n"
"v_lshlrev_b32 v22, 4, v18\n"
"v_lshl_add_u32 v0, v17, 16, v22\n"
"v_add_u32 v1, 8192, v0\n"
"v_add_u32 v2, 16384, v0\n"
"v_add_u32 v3, 24576, v0\n"
"v_add_u32 v4, 32768, v0\n"
"v_add_u32 v5, 40960, v0\n"
"v_add_u32 v6, 49152, v0\n"
"v_add_u32 v7, 57344, v0\n"
"s_lshl_b32 s54, s33, 18\n"
"s_xor_b32 s55, s54, 262144\n"
"s_xor_b32 s56, s54, 524288\n"
"s_xor_b32 s57, s54, 786432\n"
"s_mov_b32 s10, 81920\n"
"s_mov_b32 s11, 0x20000\n"
"s_mul_i32 s48, s71, 81920\n"
"s_lshl_b32 s49, s71, 5\n"
"s_add_u32 s49, s49, s72\n"
"s_mul_i32 s49, s49, 20480\n"
"s_lshl_b32 s75, %2, 12\n"
"s_add_u32 s75, s75, 81920\n"
"s_mov_b32 s14, 20480\n"
"s_mov_b32 s15, 0x20000\n"
"s_movk_i32 s50, 4096\n"
"s_movk_i32 s51, 8192\n"
"s_movk_i32 s52, 12288\n"
"s_movk_i32 s53, 16384\n"
"v_xor_b32 v12, v17, v18\n"
"v_lshlrev_b32 v12, 4, v12\n"
"v_lshl_add_u32 v12, v17, 8, v12\n"
"v_xor_b32 v13, 128, v12\n"
"s_lshl_b32 s74, %2, 2\n"
"v_add_u32 v22, s74, v17\n"
"v_xor_b32 v23, v22, v18\n"
"v_lshlrev_b32 v23, 4, v23\n"
"v_lshl_add_u32 v14, v22, 10, v23\n"
"s_mov_b32 s77, s71\n"
"s_mov_b32 s78, s72\n"
"s_cmp_lg_u32 s79, 0\n"
"s_waitcnt lgkmcnt(0)\n"
"s_cbranch_scc1 LG1%=\n"
"s_add_u32 s12, s12, s49\n"
"s_addc_u32 s13, s13, 0\n"
"s_add_u32 s20, s20, s73\n"
"s_addc_u32 s21, s21, 0\n"
"buffer_load_dwordx4 v[64:67], v0, s[20:23], s54 offen\n"
"buffer_load_dwordx4 v[68:71], v1, s[20:23], s54 offen\n"
"buffer_load_dwordx4 v[72:75], v2, s[20:23], s54 offen\n"
"buffer_load_dwordx4 v[76:79], v3, s[20:23], s54 offen\n"
"buffer_load_dwordx4 v[80:83], v4, s[20:23], s54 offen\n"
"buffer_load_dwordx4 v[84:87], v5, s[20:23], s54 offen\n"
"buffer_load_dwordx4 v[88:91], v6, s[20:23], s54 offen\n"
"buffer_load_dwordx4 v[92:95], v7, s[20:23], s54 offen\n"
"s_add_u32 s8, s8, s48\n"
"s_addc_u32 s9, s9, 0\n"
"s_add_u32 s16, s16, s49\n"
"s_addc_u32 s17, s17, 0\n"
"s_lshr_b32 s72, s54, 12\n"
"s_and_b32 s72, s72, 768\n"
"s_lshl_b32 s73, %2, 10\n"
"s_add_u32 m0, s73, 0\n"
"s_nop 0\n"
"buffer_load_dwordx4 v14, s[8:11], s72 offen lds\n"
"s_add_u32 s72, s72, 16384\n"
"s_add_u32 m0, s73, 4096\n"
"s_nop 0\n"
"buffer_load_dwordx4 v14, s[8:11], s72 offen lds\n"
"s_add_u32 s72, s72, 16384\n"
"s_add_u32 m0, s73, 8192\n"
"s_nop 0\n"
"buffer_load_dwordx4 v14, s[8:11], s72 offen lds\n"
"s_add_u32 s72, s72, 16384\n"
"s_add_u32 m0, s73, 12288\n"
"s_nop 0\n"
"buffer_load_dwordx4 v14, s[8:11], s72 offen lds\n"
"s_add_u32 s72, s72, 16384\n"
"s_add_u32 m0, s73, 16384\n"
"s_nop 0\n"
"buffer_load_dwordx4 v14, s[8:11], s72 offen lds\n"
"s_add_u32 m0, s75, 0\n"
"s_add_u32 s76, s33, 4\n"
"s_and_b32 s76, s76, 15\n"
"s_lshl_b32 s58, s76, 18\n"
"s_lshl_b32 s73, %2, 10\n"
"s_lshr_b32 s72, s58, 12\n"
"s_and_b32 s72, s72, 768\n"
"s_bfe_u32 s6, %1, 0x10002\n"
"s_mov_b32 s35, s77\n"
"s_lshl_b32 s70, s34, 19\n"
"s_lshl_b32 s6, s6, 4\n"
"s_add_u32 s76, s33, 8\n"
"s_add_u32 s24, s24, s70\n"
"s_addc_u32 s25, s25, 0\n"
"s_add_u32 s6, s6, s35\n"
"s_and_b32 s76, s76, 15\n"
"s_lshl2_add_u32 s70, s6, %2\n"
"s_lshl_b32 s62, s76, 18\n"
"s_lshl_b32 s70, s70, 11\n"
"s_add_u32 s76, s33, 12\n"
"s_add_u32 s70, s70, 20971520\n"
"s_and_b32 s76, s76, 15\n"
"s_add_u32 s36, s28, s70\n"
"s_addc_u32 s37, s29, 0\n"
"s_lshl_b32 s66, s76, 18\n"
"s_mul_i32 s70, s6, 655360\n"
"s_mul_i32 s71, %2, 20480\n"
"s_add_u32 s70, s70, s71\n"
"s_mul_i32 s76, s34, 81920\n"
"s_add_u32 s76, s76, s70\n"
"s_add_u32 s44, s28, s76\n"
"s_addc_u32 s45, s29, 0\n"
"s_sub_u32 s30, 655360, s71\n"
"s_add_u32 s28, s28, s70\n"
"s_addc_u32 s29, s29, 0\n"
"s_lshr_b32 s71, s6, 4\n"
"s_mul_i32 s70, s35, 163840\n"
"s_lshl_b32 s71, s71, 10\n"
"s_add_u32 s70, s70, s71\n"
"s_lshl2_add_u32 s71, s34, %2\n"
"s_lshl_b32 s71, s71, 5\n"
"s_add_u32 s70, s70, s71\n"
"v_xor_b32 v19, v17, v18\n"
"v_lshlrev_b32 v19, 4, v19\n"
"s_and_b32 s71, s33, 3\n"
"v_lshl_add_u32 v19, v18, 8, v19\n"
"s_lshl_b32 s71, s71, 6\n"
"v_xor_b32 v8, s71, v19\n"
"v_lshlrev_b32 v19, 1, v17\n"
"v_xor_b32 v19, v19, v18\n"
"s_add_u32 s40, s40, s70\n"
"s_addc_u32 s41, s41, 0\n"
"s_sub_u32 s42, 2621440, s70\n"
"v_lshlrev_b32 v19, 4, v19\n"
"s_add_u32 s70, s34, 7\n"
"s_mov_b32 s38, 2048\n"
"s_mov_b32 s39, 0x20000\n"
"s_xor_b32 s59, s58, 262144\n"
"buffer_load_dwordx4 v12, s[12:15], 0 offen nt lds\n"
"s_mov_b32 s48, 0x05040100\n"
"s_mov_b32 s49, 0x07060302\n"
"s_xor_b32 s60, s58, 524288\n"
"s_waitcnt vmcnt(12)\n"
"v_perm_b32 v36, v68, v64, s49\n"
"v_perm_b32 v40, v69, v65, s48\n"
"v_perm_b32 v44, v69, v65, s49\n"
"v_perm_b32 v48, v70, v66, s48\n"
"v_perm_b32 v52, v70, v66, s49\n"
"v_perm_b32 v56, v71, v67, s48\n"
"v_perm_b32 v60, v71, v67, s49\n"
"s_waitcnt vmcnt(10)\n"
"v_perm_b32 v33, v76, v72, s48\n"
"v_perm_b32 v37, v76, v72, s49\n"
"v_perm_b32 v41, v77, v73, s48\n"
"v_perm_b32 v45, v77, v73, s49\n"
"v_perm_b32 v49, v78, v74, s48\n"
"v_perm_b32 v53, v78, v74, s49\n"
"v_perm_b32 v57, v79, v75, s48\n"
"v_perm_b32 v61, v79, v75, s49\n"
"s_waitcnt vmcnt(8)\n"
"v_perm_b32 v34, v84, v80, s48\n"
"v_perm_b32 v38, v84, v80, s49\n"
"v_perm_b32 v42, v85, v81, s48\n"
"v_perm_b32 v46, v85, v81, s49\n"
"v_perm_b32 v50, v86, v82, s48\n"
"v_perm_b32 v54, v86, v82, s49\n"
"v_perm_b32 v58, v87, v83, s48\n"
"v_perm_b32 v62, v87, v83, s49\n"
"s_xor_b32 s61, s58, 786432\n"
"s_xor_b32 s63, s62, 262144\n"
"s_xor_b32 s64, s62, 524288\n"
"s_xor_b32 s65, s62, 786432\n"
"s_xor_b32 s67, s66, 262144\n"
"s_xor_b32 s68, s66, 524288\n"
"s_xor_b32 s69, s66, 786432\n"
"v_xor_b32 v9, 64, v8\n"
"v_xor_b32 v10, 128, v8\n"
"v_xor_b32 v11, 192, v8\n"
"v_and_b32 v26, 11, v18\n"
"v_lshlrev_b32 v23, 2, v17\n"
"v_xor_b32 v26, v26, v23\n"
"v_lshlrev_b32 v26, 4, v26\n"
"v_lshl_add_u32 v26, v18, 8, v26\n"
"v_add_u32 v23, s75, v26\n"
"v_xor_b32 v24, 16, v23\n"
"v_xor_b32 v25, 32, v23\n"
"v_xor_b32 v26, 48, v23\n"
"s_mov_b32 s18, 20480\n"
"s_mov_b32 s19, 0x20000\n"
"s_mov_b32 s26, 524288\n"
"s_mov_b32 s27, 0x20000\n"
"s_mov_b32 s74, 0x3e0293ee\n"
"s_lshl_b32 s77, %2, 8\n"
"s_mov_b32 s7, s78\n"
"s_xor_b32 s78, s35, 1\n"
"s_lshl_b32 s78, s78, 15\n"
"s_mov_b32 s46, 20480\n"
"s_mov_b32 s47, 0x20000\n"
"v_mov_b32 v15, -1\n"
"s_lshl_b32 s76, s34, 8\n"
"s_mov_b32 s31, 0x20000\n"
"s_mov_b32 s43, 0x20000\n"
"s_mov_b32 s79, 0\n"
"buffer_load_dwordx4 v12, s[12:15], 0 offen offset:1024 nt lds\n"
"buffer_load_dwordx4 v13, s[12:15], 0 offen offset:2048 nt lds\n"
"buffer_load_dwordx4 v13, s[12:15], 0 offen offset:3072 nt lds\n"
"s_add_u32 m0, s75, 16384\n"
"s_nop 0\n"
"buffer_load_dwordx4 v12, s[12:15], s50 offen nt lds\n"
"s_waitcnt vmcnt(10)\n"
"v_perm_b32 v32, v68, v64, s48\n"
"v_perm_b32 v35, v92, v88, s48\n"
"v_perm_b32 v39, v92, v88, s49\n"
"v_perm_b32 v43, v93, v89, s48\n"
"v_perm_b32 v47, v93, v89, s49\n"
"v_perm_b32 v51, v94, v90, s48\n"
"v_perm_b32 v55, v94, v90, s49\n"
"v_perm_b32 v59, v95, v91, s48\n"
"v_perm_b32 v63, v95, v91, s49\n"
"buffer_load_dwordx4 v12, s[12:15], s50 offen offset:1024 nt lds\n"
"buffer_load_dwordx4 v13, s[12:15], s50 offen offset:2048 nt lds\n"
"s_waitcnt vmcnt(7)\n"
"s_barrier\n"
"ds_read_b128 a[160:163], v8 offset:0\n"
"ds_read_b128 a[164:167], v8 offset:4096\n"
"ds_read_b128 a[168:171], v8 offset:8192\n"
"ds_read_b128 a[172:175], v8 offset:12288\n"
"ds_read_b128 a[176:179], v8 offset:16384\n"
"ds_read_b128 a[180:183], v9 offset:0\n"
"ds_read_b128 a[184:187], v9 offset:4096\n"
"ds_read_b128 a[188:191], v9 offset:8192\n"
"ds_read_b128 a[192:195], v9 offset:12288\n"
"ds_read_b128 a[196:199], v9 offset:16384\n"
"s_waitcnt lgkmcnt(9)\n"
"v_mfma_f32_16x16x32_bf16 a[0:3], v[32:35], a[160:163], 0\n"
"s_waitcnt lgkmcnt(8)\n"
"v_mfma_f32_16x16x32_bf16 a[32:35], v[32:35], a[164:167], 0\n"
"s_waitcnt lgkmcnt(7)\n"
"v_mfma_f32_16x16x32_bf16 a[64:67], v[32:35], a[168:171], 0\n"
"s_waitcnt lgkmcnt(6)\n"
"v_mfma_f32_16x16x32_bf16 a[96:99], v[32:35], a[172:175], 0\n"
"s_waitcnt lgkmcnt(5)\n"
"v_mfma_f32_16x16x32_bf16 a[128:131], v[32:35], a[176:179], 0\n"
"v_mfma_f32_16x16x32_bf16 a[4:7], v[36:39], a[160:163], 0\n"
"v_mfma_f32_16x16x32_bf16 a[36:39], v[36:39], a[164:167], 0\n"
"buffer_load_dwordx4 v13, s[12:15], s50 offen offset:3072 nt lds\n"
"v_mfma_f32_16x16x32_bf16 a[68:71], v[36:39], a[168:171], 0\n"
"s_add_u32 m0, s75, 32768\n"
"v_mfma_f32_16x16x32_bf16 a[100:103], v[36:39], a[172:175], 0\n"
"v_mfma_f32_16x16x32_bf16 a[132:135], v[36:39], a[176:179], 0\n"
"v_mfma_f32_16x16x32_bf16 a[8:11], v[40:43], a[160:163], 0\n"
"v_mfma_f32_16x16x32_bf16 a[40:43], v[40:43], a[164:167], 0\n"
"v_mfma_f32_16x16x32_bf16 a[72:75], v[40:43], a[168:171], 0\n"
"v_mfma_f32_16x16x32_bf16 a[104:107], v[40:43], a[172:175], 0\n"
"v_mfma_f32_16x16x32_bf16 a[136:139], v[40:43], a[176:179], 0\n"
"v_mfma_f32_16x16x32_bf16 a[12:15], v[44:47], a[160:163], 0\n"
"v_mfma_f32_16x16x32_bf16 a[44:47], v[44:47], a[164:167], 0\n"
"v_mfma_f32_16x16x32_bf16 a[76:79], v[44:47], a[168:171], 0\n"
"v_mfma_f32_16x16x32_bf16 a[108:111], v[44:47], a[172:175], 0\n"
"v_mfma_f32_16x16x32_bf16 a[140:143], v[44:47], a[176:179], 0\n"
"v_mfma_f32_16x16x32_bf16 a[16:19], v[48:51], a[160:163], 0\n"
"v_mfma_f32_16x16x32_bf16 a[48:51], v[48:51], a[164:167], 0\n"
"v_mfma_f32_16x16x32_bf16 a[80:83], v[48:51], a[168:171], 0\n"
"v_mfma_f32_16x16x32_bf16 a[112:115], v[48:51], a[172:175], 0\n"
"v_mfma_f32_16x16x32_bf16 a[144:147], v[48:51], a[176:179], 0\n"
"v_mfma_f32_16x16x32_bf16 a[20:23], v[52:55], a[160:163], 0\n"
"v_mfma_f32_16x16x32_bf16 a[52:55], v[52:55], a[164:167], 0\n"
"v_mfma_f32_16x16x32_bf16 a[84:87], v[52:55], a[168:171], 0\n"
"v_mfma_f32_16x16x32_bf16 a[116:119], v[52:55], a[172:175], 0\n"
"v_mfma_f32_16x16x32_bf16 a[148:151], v[52:55], a[176:179], 0\n"
"v_mfma_f32_16x16x32_bf16 a[24:27], v[56:59], a[160:163], 0\n"
"v_mfma_f32_16x16x32_bf16 a[56:59], v[56:59], a[164:167], 0\n"
"v_mfma_f32_16x16x32_bf16 a[88:91], v[56:59], a[168:171], 0\n"
"v_mfma_f32_16x16x32_bf16 a[120:123], v[56:59], a[172:175], 0\n"
"v_mfma_f32_16x16x32_bf16 a[152:155], v[56:59], a[176:179], 0\n"
"v_mfma_f32_16x16x32_bf16 a[28:31], v[60:63], a[160:163], 0\n"
"ds_read_b128 a[160:163], v10 offset:0\n"
"v_mfma_f32_16x16x32_bf16 a[60:63], v[60:63], a[164:167], 0\n"
"ds_read_b128 a[164:167], v10 offset:4096\n"
"v_mfma_f32_16x16x32_bf16 a[92:95], v[60:63], a[168:171], 0\n"
"ds_read_b128 a[168:171], v10 offset:8192\n"
"v_mfma_f32_16x16x32_bf16 a[124:127], v[60:63], a[172:175], 0\n"
"ds_read_b128 a[172:175], v10 offset:12288\n"
"v_mfma_f32_16x16x32_bf16 a[156:159], v[60:63], a[176:179], 0\n"
"ds_read_b128 a[176:179], v10 offset:16384\n"
"buffer_load_dwordx4 v12, s[12:15], s51 offen nt lds\n"
"buffer_load_dwordx4 v12, s[12:15], s51 offen offset:1024 nt lds\n"
"buffer_load_dwordx4 v13, s[12:15], s51 offen offset:2048 nt lds\n"
"buffer_load_dwordx4 v13, s[12:15], s51 offen offset:3072 nt lds\n"
"s_add_u32 m0, s75, 49152\n"
"s_nop 0\n"
"buffer_load_dwordx4 v12, s[12:15], s52 offen nt lds\n"
"buffer_load_dwordx4 v12, s[12:15], s52 offen offset:1024 nt lds\n"
"buffer_load_dwordx4 v13, s[12:15], s52 offen offset:2048 nt lds\n"
"buffer_load_dwordx4 v13, s[12:15], s52 offen offset:3072 nt lds\n"
"s_add_u32 m0, s75, 65536\n"
"s_nop 0\n"
"buffer_load_dwordx4 v12, s[12:15], s53 offen nt lds\n"
"buffer_load_dwordx4 v12, s[12:15], s53 offen offset:1024 nt lds\n"
"v_lshl_add_u32 v12, v17, 8, v19\n"
"v_sub_u32 v19, s70, v16\n"
"v_and_b32 v19, 7, v19\n"
"v_lshlrev_b32 v19, 8, v19\n"
"buffer_load_dwordx4 v13, s[12:15], s53 offen offset:2048 nt lds\n"
"buffer_load_dwordx4 v13, s[12:15], s53 offen offset:3072 nt lds\n"
"s_add_u32 m0, s73, 20480\n"
"v_xor_b32 v13, 128, v12\n"
"buffer_load_dwordx4 v[96:99], v0, s[20:23], s55 offen\n"
"buffer_load_dwordx4 v[100:103], v1, s[20:23], s55 offen\n"
"buffer_load_dwordx4 v[104:107], v2, s[20:23], s55 offen\n"
"buffer_load_dwordx4 v[108:111], v3, s[20:23], s55 offen\n"
"buffer_load_dwordx4 v[112:115], v4, s[20:23], s55 offen\n"
"buffer_load_dwordx4 v[116:119], v5, s[20:23], s55 offen\n"
"buffer_load_dwordx4 v[120:123], v6, s[20:23], s55 offen\n"
"buffer_load_dwordx4 v[124:127], v7, s[20:23], s55 offen\n"
"buffer_load_dwordx4 v[128:131], v0, s[20:23], s56 offen\n"
"buffer_load_dwordx4 v[132:135], v1, s[20:23], s56 offen\n"
"buffer_load_dwordx4 v[136:139], v2, s[20:23], s56 offen\n"
"buffer_load_dwordx4 v[140:143], v3, s[20:23], s56 offen\n"
"s_waitcnt vmcnt(10) lgkmcnt(0)\n"
"v_perm_b32 v36, v100, v96, s49\n"
"v_perm_b32 v40, v101, v97, s48\n"
"v_perm_b32 v44, v101, v97, s49\n"
"v_perm_b32 v48, v102, v98, s48\n"
"v_perm_b32 v52, v102, v98, s49\n"
"v_perm_b32 v56, v103, v99, s48\n"
"v_perm_b32 v60, v103, v99, s49\n"
"buffer_load_dwordx4 v[144:147], v4, s[20:23], s56 offen\n"
"buffer_load_dwordx4 v[148:151], v5, s[20:23], s56 offen\n"
"s_waitcnt vmcnt(10)\n"
"v_perm_b32 v33, v108, v104, s48\n"
"v_perm_b32 v37, v108, v104, s49\n"
"v_perm_b32 v41, v109, v105, s48\n"
"v_perm_b32 v45, v109, v105, s49\n"
"v_perm_b32 v49, v110, v106, s48\n"
"v_perm_b32 v53, v110, v106, s49\n"
"v_perm_b32 v57, v111, v107, s48\n"
"v_perm_b32 v61, v111, v107, s49\n"
"buffer_load_dwordx4 v[152:155], v6, s[20:23], s56 offen\n"
"buffer_load_dwordx4 v[156:159], v7, s[20:23], s56 offen\n"
"s_waitcnt vmcnt(10)\n"
"v_perm_b32 v34, v116, v112, s48\n"
"v_perm_b32 v38, v116, v112, s49\n"
"v_perm_b32 v42, v117, v113, s48\n"
"v_perm_b32 v46, v117, v113, s49\n"
"v_perm_b32 v50, v118, v114, s48\n"
"v_perm_b32 v54, v118, v114, s49\n"
"v_perm_b32 v58, v119, v115, s48\n"
"v_perm_b32 v62, v119, v115, s49\n"
"buffer_load_dwordx4 v14, s[8:11], s72 offen lds\n"
"s_add_u32 m0, s73, 24576\n"
"s_add_u32 s72, s72, 16384\n"
"buffer_load_dwordx4 v14, s[8:11], s72 offen lds\n"
"s_add_u32 m0, s73, 28672\n"
"s_add_u32 s72, s72, 16384\n"
"s_waitcnt vmcnt(10)\n"
"v_perm_b32 v32, v100, v96, s48\n"
"v_perm_b32 v35, v124, v120, s48\n"
"v_perm_b32 v39, v124, v120, s49\n"
"v_perm_b32 v43, v125, v121, s48\n"
"v_mfma_f32_16x16x32_bf16 a[0:3], v[32:35], a[180:183], a[0:3]\n"
"v_perm_b32 v47, v125, v121, s49\n"
"v_perm_b32 v51, v126, v122, s48\n"
"v_mfma_f32_16x16x32_bf16 a[32:35], v[32:35], a[184:187], a[32:35]\n"
"v_perm_b32 v55, v126, v122, s49\n"
"v_perm_b32 v59, v127, v123, s48\n"
"v_mfma_f32_16x16x32_bf16 a[64:67], v[32:35], a[188:191], a[64:67]\n"
"buffer_load_dwordx4 v14, s[8:11], s72 offen lds\n"
"v_mfma_f32_16x16x32_bf16 a[96:99], v[32:35], a[192:195], a[96:99]\n"
"s_add_u32 m0, s73, 32768\n"
"s_add_u32 s72, s72, 16384\n"
"v_perm_b32 v63, v127, v123, s49\n"
"v_mfma_f32_16x16x32_bf16 a[128:131], v[32:35], a[196:199], a[128:131]\n"
"v_mfma_f32_16x16x32_bf16 a[4:7], v[36:39], a[180:183], a[4:7]\n"
"v_mfma_f32_16x16x32_bf16 a[36:39], v[36:39], a[184:187], a[36:39]\n"
"buffer_load_dwordx4 v14, s[8:11], s72 offen lds\n"
"v_mfma_f32_16x16x32_bf16 a[68:71], v[36:39], a[188:191], a[68:71]\n"
"s_add_u32 m0, s73, 36864\n"
"s_add_u32 s72, s72, 16384\n"
"s_lshl_b32 s73, %2, 10\n"
"v_mfma_f32_16x16x32_bf16 a[100:103], v[36:39], a[192:195], a[100:103]\n"
"v_mfma_f32_16x16x32_bf16 a[132:135], v[36:39], a[196:199], a[132:135]\n"
"s_waitcnt vmcnt(10)\n"
"v_perm_b32 v36, v132, v128, s49\n"
"v_mfma_f32_16x16x32_bf16 a[8:11], v[40:43], a[180:183], a[8:11]\n"
"buffer_load_dwordx4 v14, s[8:11], s72 offen lds\n"
"v_mfma_f32_16x16x32_bf16 a[40:43], v[40:43], a[184:187], a[40:43]\n"
"s_lshr_b32 s72, s62, 12\n"
"s_add_u32 m0, s73, 40960\n"
"s_and_b32 s72, s72, 768\n"
"v_mfma_f32_16x16x32_bf16 a[72:75], v[40:43], a[188:191], a[72:75]\n"
"v_mfma_f32_16x16x32_bf16 a[104:107], v[40:43], a[192:195], a[104:107]\n"
"v_mfma_f32_16x16x32_bf16 a[136:139], v[40:43], a[196:199], a[136:139]\n"
"buffer_load_dword v31, v19, s[36:39], 0 offen sc1\n"
"v_mfma_f32_16x16x32_bf16 a[12:15], v[44:47], a[180:183], a[12:15]\n"
"s_waitcnt vmcnt(10)\n"
"v_perm_b32 v40, v133, v129, s48\n"
"v_perm_b32 v33, v140, v136, s48\n"
"v_mfma_f32_16x16x32_bf16 a[44:47], v[44:47], a[184:187], a[44:47]\n"
"v_perm_b32 v37, v140, v136, s49\n"
"v_perm_b32 v41, v141, v137, s48\n"
"v_mfma_f32_16x16x32_bf16 a[76:79], v[44:47], a[188:191], a[76:79]\n"
"v_mfma_f32_16x16x32_bf16 a[108:111], v[44:47], a[192:195], a[108:111]\n"
"v_mfma_f32_16x16x32_bf16 a[140:143], v[44:47], a[196:199], a[140:143]\n"
"buffer_load_dwordx4 v[160:163], v0, s[20:23], s57 offen\n"
"v_mfma_f32_16x16x32_bf16 a[16:19], v[48:51], a[180:183], a[16:19]\n"
"v_perm_b32 v44, v133, v129, s49\n"
"v_perm_b32 v45, v141, v137, s49\n"
"v_mfma_f32_16x16x32_bf16 a[48:51], v[48:51], a[184:187], a[48:51]\n"
"v_mfma_f32_16x16x32_bf16 a[80:83], v[48:51], a[188:191], a[80:83]\n"
"v_mfma_f32_16x16x32_bf16 a[112:115], v[48:51], a[192:195], a[112:115]\n"
"buffer_load_dwordx4 v[164:167], v1, s[20:23], s57 offen\n"
"v_mfma_f32_16x16x32_bf16 a[144:147], v[48:51], a[196:199], a[144:147]\n"
"v_perm_b32 v48, v134, v130, s48\n"
"v_perm_b32 v49, v142, v138, s48\n"
"v_mfma_f32_16x16x32_bf16 a[20:23], v[52:55], a[180:183], a[20:23]\n"
"s_waitcnt vmcnt(10)\n"
"v_perm_b32 v34, v148, v144, s48\n"
"v_perm_b32 v38, v148, v144, s49\n"
"v_mfma_f32_16x16x32_bf16 a[52:55], v[52:55], a[184:187], a[52:55]\n"
"v_perm_b32 v42, v149, v145, s48\n"
"v_perm_b32 v46, v149, v145, s49\n"
"v_mfma_f32_16x16x32_bf16 a[84:87], v[52:55], a[188:191], a[84:87]\n"
"buffer_load_dwordx4 v[168:171], v2, s[20:23], s57 offen\n"
"v_mfma_f32_16x16x32_bf16 a[116:119], v[52:55], a[192:195], a[116:119]\n"
"v_perm_b32 v50, v150, v146, s48\n"
"v_mfma_f32_16x16x32_bf16 a[148:151], v[52:55], a[196:199], a[148:151]\n"
"v_perm_b32 v52, v134, v130, s49\n"
"v_perm_b32 v53, v142, v138, s49\n"
"v_mfma_f32_16x16x32_bf16 a[24:27], v[56:59], a[180:183], a[24:27]\n"
"v_perm_b32 v54, v150, v146, s49\n"
"v_mfma_f32_16x16x32_bf16 a[56:59], v[56:59], a[184:187], a[56:59]\n"
"buffer_load_dwordx4 v[172:175], v3, s[20:23], s57 offen\n"
"v_mfma_f32_16x16x32_bf16 a[88:91], v[56:59], a[188:191], a[88:91]\n"
"s_waitcnt vmcnt(10)\n"
"v_perm_b32 v32, v132, v128, s48\n"
"v_perm_b32 v35, v156, v152, s48\n"
"v_mfma_f32_16x16x32_bf16 a[120:123], v[56:59], a[192:195], a[120:123]\n"
"v_perm_b32 v39, v156, v152, s49\n"
"v_perm_b32 v43, v157, v153, s48\n"
"v_mfma_f32_16x16x32_bf16 a[152:155], v[56:59], a[196:199], a[152:155]\n"
"v_perm_b32 v56, v135, v131, s48\n"
"v_perm_b32 v57, v143, v139, s48\n"
"v_mfma_f32_16x16x32_bf16 a[28:31], v[60:63], a[180:183], a[28:31]\n"
"buffer_load_dwordx4 v[176:179], v4, s[20:23], s57 offen\n"
"v_mfma_f32_16x16x32_bf16 a[60:63], v[60:63], a[184:187], a[60:63]\n"
"v_perm_b32 v58, v151, v147, s48\n"
"v_perm_b32 v47, v157, v153, s49\n"
"v_mfma_f32_16x16x32_bf16 a[92:95], v[60:63], a[188:191], a[92:95]\n"
"v_perm_b32 v51, v158, v154, s48\n"
"v_perm_b32 v55, v158, v154, s49\n"
"v_mfma_f32_16x16x32_bf16 a[124:127], v[60:63], a[192:195], a[124:127]\n"
"v_perm_b32 v59, v159, v155, s48\n"
"ds_read_b128 a[180:183], v11 offset:0\n"
"v_mfma_f32_16x16x32_bf16 a[156:159], v[60:63], a[196:199], a[156:159]\n"
"buffer_load_dwordx4 v[180:183], v5, s[20:23], s57 offen\n"
"v_mfma_f32_16x16x32_bf16 a[0:3], v[32:35], a[160:163], a[0:3]\n"
"v_perm_b32 v60, v135, v131, s49\n"
"v_perm_b32 v61, v143, v139, s49\n"
"v_mfma_f32_16x16x32_bf16 a[32:35], v[32:35], a[164:167], a[32:35]\n"
"v_perm_b32 v62, v151, v147, s49\n"
"v_perm_b32 v63, v159, v155, s49\n"
"v_mfma_f32_16x16x32_bf16 a[64:67], v[32:35], a[168:171], a[64:67]\n"
"ds_read_b128 a[184:187], v11 offset:4096\n"
"v_mfma_f32_16x16x32_bf16 a[96:99], v[32:35], a[172:175], a[96:99]\n"
"ds_read_b128 a[188:191], v11 offset:8192\n"
"v_mfma_f32_16x16x32_bf16 a[128:131], v[32:35], a[176:179], a[128:131]\n"
"ds_read_b128 a[192:195], v11 offset:12288\n"
"v_mfma_f32_16x16x32_bf16 a[4:7], v[36:39], a[160:163], a[4:7]\n"
"ds_read_b128 a[196:199], v11 offset:16384\n"
"v_mfma_f32_16x16x32_bf16 a[36:39], v[36:39], a[164:167], a[36:39]\n"
"v_mfma_f32_16x16x32_bf16 a[68:71], v[36:39], a[168:171], a[68:71]\n"
"v_mfma_f32_16x16x32_bf16 a[100:103], v[36:39], a[172:175], a[100:103]\n"
"v_mfma_f32_16x16x32_bf16 a[132:135], v[36:39], a[176:179], a[132:135]\n"
"v_mfma_f32_16x16x32_bf16 a[8:11], v[40:43], a[160:163], a[8:11]\n"
"v_mfma_f32_16x16x32_bf16 a[40:43], v[40:43], a[164:167], a[40:43]\n"
"v_mfma_f32_16x16x32_bf16 a[72:75], v[40:43], a[168:171], a[72:75]\n"
"v_mfma_f32_16x16x32_bf16 a[104:107], v[40:43], a[172:175], a[104:107]\n"
"v_mfma_f32_16x16x32_bf16 a[136:139], v[40:43], a[176:179], a[136:139]\n"
"v_mfma_f32_16x16x32_bf16 a[12:15], v[44:47], a[160:163], a[12:15]\n"
"v_mfma_f32_16x16x32_bf16 a[44:47], v[44:47], a[164:167], a[44:47]\n"
"v_mfma_f32_16x16x32_bf16 a[76:79], v[44:47], a[168:171], a[76:79]\n"
"v_mfma_f32_16x16x32_bf16 a[108:111], v[44:47], a[172:175], a[108:111]\n"
"v_mfma_f32_16x16x32_bf16 a[140:143], v[44:47], a[176:179], a[140:143]\n"
"v_mfma_f32_16x16x32_bf16 a[16:19], v[48:51], a[160:163], a[16:19]\n"
"v_mfma_f32_16x16x32_bf16 a[48:51], v[48:51], a[164:167], a[48:51]\n"
"v_mfma_f32_16x16x32_bf16 a[80:83], v[48:51], a[168:171], a[80:83]\n"
"v_mfma_f32_16x16x32_bf16 a[112:115], v[48:51], a[172:175], a[112:115]\n"
"v_mfma_f32_16x16x32_bf16 a[144:147], v[48:51], a[176:179], a[144:147]\n"
"v_mfma_f32_16x16x32_bf16 a[20:23], v[52:55], a[160:163], a[20:23]\n"
"v_mfma_f32_16x16x32_bf16 a[52:55], v[52:55], a[164:167], a[52:55]\n"
"v_mfma_f32_16x16x32_bf16 a[84:87], v[52:55], a[168:171], a[84:87]\n"
"v_mfma_f32_16x16x32_bf16 a[116:119], v[52:55], a[172:175], a[116:119]\n"
"v_mfma_f32_16x16x32_bf16 a[148:151], v[52:55], a[176:179], a[148:151]\n"
"v_mfma_f32_16x16x32_bf16 a[24:27], v[56:59], a[160:163], a[24:27]\n"
"v_mfma_f32_16x16x32_bf16 a[56:59], v[56:59], a[164:167], a[56:59]\n"
"v_mfma_f32_16x16x32_bf16 a[88:91], v[56:59], a[168:171], a[88:91]\n"
"v_mfma_f32_16x16x32_bf16 a[120:123], v[56:59], a[172:175], a[120:123]\n"
"v_mfma_f32_16x16x32_bf16 a[152:155], v[56:59], a[176:179], a[152:155]\n"
"v_mfma_f32_16x16x32_bf16 a[28:31], v[60:63], a[160:163], a[28:31]\n"
"v_mfma_f32_16x16x32_bf16 a[60:63], v[60:63], a[164:167], a[60:63]\n"
"v_mfma_f32_16x16x32_bf16 a[92:95], v[60:63], a[168:171], a[92:95]\n"
"v_mfma_f32_16x16x32_bf16 a[124:127], v[60:63], a[172:175], a[124:127]\n"
"v_mfma_f32_16x16x32_bf16 a[156:159], v[60:63], a[176:179], a[156:159]\n"
"buffer_load_dwordx4 v[184:187], v6, s[20:23], s57 offen\n"
"buffer_load_dwordx4 v[188:191], v7, s[20:23], s57 offen\n"
"buffer_load_dwordx4 v[192:195], v0, s[20:23], s58 offen\n"
"buffer_load_dwordx4 v[196:199], v1, s[20:23], s58 offen\n"
"s_waitcnt vmcnt(10) lgkmcnt(0)\n"
"v_xor_b32 v31, 0x5bd1e995, v31\n"
"buffer_load_dwordx4 v[200:203], v2, s[20:23], s58 offen\n"
"buffer_load_dwordx4 v[204:207], v3, s[20:23], s58 offen\n"
"s_waitcnt vmcnt(10)\n"
"v_perm_b32 v36, v164, v160, s49\n"
"v_perm_b32 v40, v165, v161, s48\n"
"v_perm_b32 v44, v165, v161, s49\n"
"v_perm_b32 v48, v166, v162, s48\n"
"v_perm_b32 v52, v166, v162, s49\n"
"v_perm_b32 v56, v167, v163, s48\n"
"v_perm_b32 v60, v167, v163, s49\n"
"buffer_load_dwordx4 v[208:211], v4, s[20:23], s58 offen\n"
"buffer_load_dwordx4 v[212:215], v5, s[20:23], s58 offen\n"
"s_waitcnt vmcnt(10)\n"
"v_perm_b32 v33, v172, v168, s48\n"
"v_perm_b32 v37, v172, v168, s49\n"
"v_perm_b32 v41, v173, v169, s48\n"
"v_perm_b32 v45, v173, v169, s49\n"
"v_perm_b32 v49, v174, v170, s48\n"
"v_perm_b32 v53, v174, v170, s49\n"
"v_perm_b32 v57, v175, v171, s48\n"
"v_perm_b32 v61, v175, v171, s49\n"
"buffer_load_dwordx4 v[216:219], v6, s[20:23], s58 offen\n"
"buffer_load_dwordx4 v[220:223], v7, s[20:23], s58 offen\n"
"s_waitcnt vmcnt(10)\n"
"v_perm_b32 v34, v180, v176, s48\n"
"v_perm_b32 v38, v180, v176, s49\n"
"v_perm_b32 v42, v181, v177, s48\n"
"v_perm_b32 v46, v181, v177, s49\n"
"v_perm_b32 v50, v182, v178, s48\n"
"v_perm_b32 v54, v182, v178, s49\n"
"v_perm_b32 v58, v183, v179, s48\n"
"v_perm_b32 v62, v183, v179, s49\n"
"buffer_load_dwordx4 v[224:227], v0, s[20:23], s59 offen\n"
"buffer_load_dwordx4 v[228:231], v1, s[20:23], s59 offen\n"
"s_waitcnt vmcnt(10)\n"
"v_perm_b32 v32, v164, v160, s48\n"
"v_perm_b32 v35, v188, v184, s48\n"
"v_perm_b32 v39, v188, v184, s49\n"
"v_perm_b32 v43, v189, v185, s48\n"
"v_mfma_f32_16x16x32_bf16 a[0:3], v[32:35], a[180:183], a[0:3]\n"
"v_perm_b32 v47, v189, v185, s49\n"
"v_perm_b32 v51, v190, v186, s48\n"
"v_mfma_f32_16x16x32_bf16 a[32:35], v[32:35], a[184:187], a[32:35]\n"
"v_perm_b32 v55, v190, v186, s49\n"
"v_perm_b32 v59, v191, v187, s48\n"
"v_mfma_f32_16x16x32_bf16 a[64:67], v[32:35], a[188:191], a[64:67]\n"
"v_perm_b32 v63, v191, v187, s49\n"
"v_mfma_f32_16x16x32_bf16 a[96:99], v[32:35], a[192:195], a[96:99]\n"
"buffer_load_dwordx4 v[232:235], v2, s[20:23], s59 offen\n"
"v_mfma_f32_16x16x32_bf16 a[128:131], v[32:35], a[196:199], a[128:131]\n"
"v_mfma_f32_16x16x32_bf16 a[4:7], v[36:39], a[180:183], a[4:7]\n"
"v_mfma_f32_16x16x32_bf16 a[36:39], v[36:39], a[184:187], a[36:39]\n"
"v_mfma_f32_16x16x32_bf16 a[68:71], v[36:39], a[188:191], a[68:71]\n"
"buffer_load_dwordx4 v[236:239], v3, s[20:23], s59 offen\n"
"v_mfma_f32_16x16x32_bf16 a[100:103], v[36:39], a[192:195], a[100:103]\n"
"v_mfma_f32_16x16x32_bf16 a[132:135], v[36:39], a[196:199], a[132:135]\n"
"s_waitcnt vmcnt(10)\n"
"v_perm_b32 v36, v196, v192, s49\n"
"v_mfma_f32_16x16x32_bf16 a[8:11], v[40:43], a[180:183], a[8:11]\n"
"v_mfma_f32_16x16x32_bf16 a[40:43], v[40:43], a[184:187], a[40:43]\n"
"buffer_load_dwordx4 v[240:243], v4, s[20:23], s59 offen\n"
"v_mfma_f32_16x16x32_bf16 a[72:75], v[40:43], a[188:191], a[72:75]\n"
"v_mfma_f32_16x16x32_bf16 a[104:107], v[40:43], a[192:195], a[104:107]\n"
"v_mfma_f32_16x16x32_bf16 a[136:139], v[40:43], a[196:199], a[136:139]\n"
"v_perm_b32 v40, v197, v193, s48\n"
"v_mfma_f32_16x16x32_bf16 a[12:15], v[44:47], a[180:183], a[12:15]\n"
"buffer_load_dwordx4 v[244:247], v5, s[20:23], s59 offen\n"
"v_mfma_f32_16x16x32_bf16 a[44:47], v[44:47], a[184:187], a[44:47]\n"
"s_waitcnt vmcnt(10)\n"
"v_perm_b32 v33, v204, v200, s48\n"
"v_perm_b32 v37, v204, v200, s49\n"
"v_mfma_f32_16x16x32_bf16 a[76:79], v[44:47], a[188:191], a[76:79]\n"
"v_perm_b32 v41, v205, v201, s48\n"
"v_mfma_f32_16x16x32_bf16 a[108:111], v[44:47], a[192:195], a[108:111]\n"
"v_mfma_f32_16x16x32_bf16 a[140:143], v[44:47], a[196:199], a[140:143]\n"
"buffer_load_dwordx4 v[248:251], v6, s[20:23], s59 offen\n"
"v_mfma_f32_16x16x32_bf16 a[16:19], v[48:51], a[180:183], a[16:19]\n"
"v_perm_b32 v44, v197, v193, s49\n"
"v_perm_b32 v45, v205, v201, s49\n"
"v_mfma_f32_16x16x32_bf16 a[48:51], v[48:51], a[184:187], a[48:51]\n"
"s_barrier\n"
"ds_read_b128 a[160:163], v8 offset:20480\n"
"v_mfma_f32_16x16x32_bf16 a[80:83], v[48:51], a[188:191], a[80:83]\n"
"ds_read_b128 a[164:167], v8 offset:24576\n"
"v_mfma_f32_16x16x32_bf16 a[112:115], v[48:51], a[192:195], a[112:115]\n"
"buffer_load_dwordx4 v[252:255], v7, s[20:23], s59 offen\n"
"v_mfma_f32_16x16x32_bf16 a[144:147], v[48:51], a[196:199], a[144:147]\n"
"v_perm_b32 v48, v198, v194, s48\n"
"v_perm_b32 v49, v206, v202, s48\n"
"v_mfma_f32_16x16x32_bf16 a[20:23], v[52:55], a[180:183], a[20:23]\n"
"s_waitcnt vmcnt(10)\n"
"v_perm_b32 v34, v212, v208, s48\n"
"v_perm_b32 v38, v212, v208, s49\n"
"v_mfma_f32_16x16x32_bf16 a[52:55], v[52:55], a[184:187], a[52:55]\n"
"v_perm_b32 v42, v213, v209, s48\n"
"v_perm_b32 v46, v213, v209, s49\n"
"v_mfma_f32_16x16x32_bf16 a[84:87], v[52:55], a[188:191], a[84:87]\n"
"buffer_load_dwordx4 v[64:67], v0, s[20:23], s60 offen\n"
"v_mfma_f32_16x16x32_bf16 a[116:119], v[52:55], a[192:195], a[116:119]\n"
"v_perm_b32 v50, v214, v210, s48\n"
"ds_read_b128 a[168:171], v8 offset:28672\n"
"v_mfma_f32_16x16x32_bf16 a[148:151], v[52:55], a[196:199], a[148:151]\n"
"v_perm_b32 v52, v198, v194, s49\n"
"v_perm_b32 v53, v206, v202, s49\n"
"v_mfma_f32_16x16x32_bf16 a[24:27], v[56:59], a[180:183], a[24:27]\n"
"v_perm_b32 v54, v214, v210, s49\n"
"ds_read_b128 a[172:175], v8 offset:32768\n"
"v_mfma_f32_16x16x32_bf16 a[56:59], v[56:59], a[184:187], a[56:59]\n"
"buffer_load_dwordx4 v[68:71], v1, s[20:23], s60 offen\n"
"v_mfma_f32_16x16x32_bf16 a[88:91], v[56:59], a[188:191], a[88:91]\n"
"s_waitcnt vmcnt(10)\n"
"v_perm_b32 v32, v196, v192, s48\n"
"v_perm_b32 v35, v220, v216, s48\n"
"v_mfma_f32_16x16x32_bf16 a[120:123], v[56:59], a[192:195], a[120:123]\n"
"v_perm_b32 v39, v220, v216, s49\n"
"v_perm_b32 v43, v221, v217, s48\n"
"v_mfma_f32_16x16x32_bf16 a[152:155], v[56:59], a[196:199], a[152:155]\n"
"v_perm_b32 v56, v199, v195, s48\n"
"v_perm_b32 v57, v207, v203, s48\n"
"v_mfma_f32_16x16x32_bf16 a[28:31], v[60:63], a[180:183], a[28:31]\n"
"v_perm_b32 v58, v215, v211, s48\n"
"v_perm_b32 v47, v221, v217, s49\n"
"v_mfma_f32_16x16x32_bf16 a[60:63], v[60:63], a[184:187], a[60:63]\n"
"buffer_load_dwordx4 v[72:75], v2, s[20:23], s60 offen\n"
"v_mfma_f32_16x16x32_bf16 a[92:95], v[60:63], a[188:191], a[92:95]\n"
"v_perm_b32 v51, v222, v218, s48\n"
"v_perm_b32 v55, v222, v218, s49\n"
"v_mfma_f32_16x16x32_bf16 a[124:127], v[60:63], a[192:195], a[124:127]\n"
"v_perm_b32 v59, v223, v219, s48\n"
"ds_read_b128 a[176:179], v8 offset:36864\n"
"v_mfma_f32_16x16x32_bf16 a[156:159], v[60:63], a[196:199], a[156:159]\n"
"s_waitcnt lgkmcnt(3)\n"
"v_perm_b32 v60, v199, v195, s49\n"
"v_perm_b32 v61, v207, v203, s49\n"
"v_mfma_f32_16x16x32_bf16 a[0:3], v[32:35], a[160:163], a[0:3]\n"
"buffer_load_dwordx4 v[76:79], v3, s[20:23], s60 offen\n"
"v_mfma_f32_16x16x32_bf16 a[32:35], v[32:35], a[164:167], a[32:35]\n"
"s_waitcnt lgkmcnt(2)\n"
"v_perm_b32 v62, v215, v211, s49\n"
"v_perm_b32 v63, v223, v219, s49\n"
"v_mfma_f32_16x16x32_bf16 a[64:67], v[32:35], a[168:171], a[64:67]\n"
"s_waitcnt lgkmcnt(1)\n"
"ds_read_b128 a[180:183], v9 offset:20480\n"
"v_add_u32 v8, 40960, v8\n"
"v_mfma_f32_16x16x32_bf16 a[96:99], v[32:35], a[172:175], a[96:99]\n"
"ds_read_b128 a[184:187], v9 offset:24576\n"
"ds_read_b128 a[188:191], v9 offset:28672\n"
"buffer_load_dwordx4 v[80:83], v4, s[20:23], s60 offen\n"
"ds_read_b128 a[192:195], v9 offset:32768\n"
"ds_read_b128 a[196:199], v9 offset:36864\n"
"v_add_u32 v9, 40960, v9\n"
"s_waitcnt lgkmcnt(5)\n"
"v_mfma_f32_16x16x32_bf16 a[128:131], v[32:35], a[176:179], a[128:131]\n"
"buffer_load_dwordx4 v[84:87], v5, s[20:23], s60 offen\n"
"v_mfma_f32_16x16x32_bf16 a[4:7], v[36:39], a[160:163], a[4:7]\n"
"s_waitcnt vmcnt(10)\n"
"v_perm_b32 v33, v236, v232, s48\n"
"v_mfma_f32_16x16x32_bf16 a[36:39], v[36:39], a[164:167], a[36:39]\n"
"v_mfma_f32_16x16x32_bf16 a[68:71], v[36:39], a[168:171], a[68:71]\n"
"v_mfma_f32_16x16x32_bf16 a[100:103], v[36:39], a[172:175], a[100:103]\n"
"v_mfma_f32_16x16x32_bf16 a[132:135], v[36:39], a[176:179], a[132:135]\n"
"buffer_load_dwordx4 v[88:91], v6, s[20:23], s60 offen\n"
"v_mfma_f32_16x16x32_bf16 a[8:11], v[40:43], a[160:163], a[8:11]\n"
"v_perm_b32 v36, v228, v224, s49\n"
"v_perm_b32 v37, v236, v232, s49\n"
"v_mfma_f32_16x16x32_bf16 a[40:43], v[40:43], a[164:167], a[40:43]\n"
"v_mfma_f32_16x16x32_bf16 a[72:75], v[40:43], a[168:171], a[72:75]\n"
"v_mfma_f32_16x16x32_bf16 a[104:107], v[40:43], a[172:175], a[104:107]\n"
"buffer_load_dwordx4 v[92:95], v7, s[20:23], s60 offen\n"
"v_mfma_f32_16x16x32_bf16 a[136:139], v[40:43], a[176:179], a[136:139]\n"
"v_perm_b32 v40, v229, v225, s48\n"
"v_perm_b32 v41, v237, v233, s48\n"
"v_mfma_f32_16x16x32_bf16 a[12:15], v[44:47], a[160:163], a[12:15]\n"
"s_waitcnt vmcnt(10) lgkmcnt(0)\n"
"v_perm_b32 v34, v244, v240, s48\n"
"v_perm_b32 v38, v244, v240, s49\n"
"v_mfma_f32_16x16x32_bf16 a[44:47], v[44:47], a[164:167], a[44:47]\n"
"v_perm_b32 v42, v245, v241, s48\n"
"v_mfma_f32_16x16x32_bf16 a[76:79], v[44:47], a[168:171], a[76:79]\n"
"buffer_load_dwordx4 v[96:99], v0, s[20:23], s61 offen\n"
"v_mfma_f32_16x16x32_bf16 a[108:111], v[44:47], a[172:175], a[108:111]\n"
"v_mfma_f32_16x16x32_bf16 a[140:143], v[44:47], a[176:179], a[140:143]\n"
"v_perm_b32 v44, v229, v225, s49\n"
"v_perm_b32 v45, v237, v233, s49\n"
"v_mfma_f32_16x16x32_bf16 a[16:19], v[48:51], a[160:163], a[16:19]\n"
"v_perm_b32 v46, v245, v241, s49\n"
"v_mfma_f32_16x16x32_bf16 a[48:51], v[48:51], a[164:167], a[48:51]\n"
"buffer_load_dwordx4 v[100:103], v1, s[20:23], s61 offen\n"
"v_mfma_f32_16x16x32_bf16 a[80:83], v[48:51], a[168:171], a[80:83]\n"
"s_waitcnt vmcnt(10)\n"
"v_perm_b32 v32, v228, v224, s48\n"
"v_perm_b32 v35, v252, v248, s48\n"
"v_mfma_f32_16x16x32_bf16 a[112:115], v[48:51], a[172:175], a[112:115]\n"
"v_perm_b32 v39, v252, v248, s49\n"
"v_perm_b32 v43, v253, v249, s48\n"
"v_mfma_f32_16x16x32_bf16 a[144:147], v[48:51], a[176:179], a[144:147]\n"
"v_perm_b32 v48, v230, v226, s48\n"
"v_perm_b32 v49, v238, v234, s48\n"
"v_mfma_f32_16x16x32_bf16 a[20:23], v[52:55], a[160:163], a[20:23]\n"
"buffer_load_dwordx4 v[104:107], v2, s[20:23], s61 offen\n"
"v_mfma_f32_16x16x32_bf16 a[52:55], v[52:55], a[164:167], a[52:55]\n"
"v_perm_b32 v50, v246, v242, s48\n"
"v_perm_b32 v47, v253, v249, s49\n"
"v_mfma_f32_16x16x32_bf16 a[84:87], v[52:55], a[168:171], a[84:87]\n"
"v_perm_b32 v51, v254, v250, s48\n"
"v_mfma_f32_16x16x32_bf16 a[116:119], v[52:55], a[172:175], a[116:119]\n"
"v_mfma_f32_16x16x32_bf16 a[148:151], v[52:55], a[176:179], a[148:151]\n"
"buffer_load_dwordx4 v[108:111], v3, s[20:23], s61 offen\n"
"v_mfma_f32_16x16x32_bf16 a[24:27], v[56:59], a[160:163], a[24:27]\n"
"v_perm_b32 v52, v230, v226, s49\n"
"v_perm_b32 v53, v238, v234, s49\n"
"v_mfma_f32_16x16x32_bf16 a[56:59], v[56:59], a[164:167], a[56:59]\n"
"v_perm_b32 v54, v246, v242, s49\n"
"v_perm_b32 v55, v254, v250, s49\n"
"v_mfma_f32_16x16x32_bf16 a[88:91], v[56:59], a[168:171], a[88:91]\n"
"v_mfma_f32_16x16x32_bf16 a[120:123], v[56:59], a[172:175], a[120:123]\n"
"buffer_load_dwordx4 v[112:115], v4, s[20:23], s61 offen\n"
"v_mfma_f32_16x16x32_bf16 a[152:155], v[56:59], a[176:179], a[152:155]\n"
"v_perm_b32 v56, v231, v227, s48\n"
"v_perm_b32 v57, v239, v235, s48\n"
"v_mfma_f32_16x16x32_bf16 a[28:31], v[60:63], a[160:163], a[28:31]\n"
"v_perm_b32 v58, v247, v243, s48\n"
"v_perm_b32 v59, v255, v251, s48\n"
"v_mfma_f32_16x16x32_bf16 a[60:63], v[60:63], a[164:167], a[60:63]\n"
"ds_read_b128 a[160:163], v10 offset:20480\n"
"v_mfma_f32_16x16x32_bf16 a[92:95], v[60:63], a[168:171], a[92:95]\n"
"buffer_load_dwordx4 v[116:119], v5, s[20:23], s61 offen\n"
"v_mfma_f32_16x16x32_bf16 a[124:127], v[60:63], a[172:175], a[124:127]\n"
"ds_read_b128 a[164:167], v10 offset:24576\n"
"v_mfma_f32_16x16x32_bf16 a[156:159], v[60:63], a[176:179], a[156:159]\n"
"v_perm_b32 v60, v231, v227, s49\n"
"v_perm_b32 v61, v239, v235, s49\n"
"v_mfma_f32_16x16x32_bf16 a[0:3], v[32:35], a[180:183], a[0:3]\n"
"v_perm_b32 v62, v247, v243, s49\n"
"v_perm_b32 v63, v255, v251, s49\n"
"v_mfma_f32_16x16x32_bf16 a[32:35], v[32:35], a[184:187], a[32:35]\n"
"ds_read_b128 a[168:171], v10 offset:28672\n"
"v_mfma_f32_16x16x32_bf16 a[64:67], v[32:35], a[188:191], a[64:67]\n"
"buffer_load_dwordx4 v[120:123], v6, s[20:23], s61 offen\n"
"v_mfma_f32_16x16x32_bf16 a[96:99], v[32:35], a[192:195], a[96:99]\n"
"ds_read_b128 a[172:175], v10 offset:32768\n"
"v_mfma_f32_16x16x32_bf16 a[128:131], v[32:35], a[196:199], a[128:131]\n"
"s_waitcnt vmcnt(11)\n"
"v_perm_b32 v33, v76, v72, s48\n"
"ds_read_b128 a[176:179], v10 offset:36864\n"
"v_mfma_f32_16x16x32_bf16 a[4:7], v[36:39], a[180:183], a[4:7]\n"
"v_add_u32 v10, 40960, v10\n"
"v_mfma_f32_16x16x32_bf16 a[36:39], v[36:39], a[184:187], a[36:39]\n"
"buffer_load_dwordx4 v[124:127], v7, s[20:23], s61 offen\n"
"v_mfma_f32_16x16x32_bf16 a[68:71], v[36:39], a[188:191], a[68:71]\n"
"s_lshl_b32 s61, s34, 7\n"
"s_waitcnt vmcnt(10)\n"
"v_perm_b32 v34, v84, v80, s48\n"
"s_add_u32 s54, s61, 128\n"
"v_mfma_f32_16x16x32_bf16 a[100:103], v[36:39], a[192:195], a[100:103]\n"
"s_and_b32 s54, s54, 896\n"
"s_add_u32 s55, s61, 256\n"
"s_and_b32 s55, s55, 896\n"
"v_mfma_f32_16x16x32_bf16 a[132:135], v[36:39], a[196:199], a[132:135]\n"
"s_add_u32 s56, s61, 384\n"
"v_perm_b32 v36, v68, v64, s49\n"
"v_perm_b32 v37, v76, v72, s49\n"
"v_mfma_f32_16x16x32_bf16 a[8:11], v[40:43], a[180:183], a[8:11]\n"
"s_and_b32 s56, s56, 896\n"
"buffer_load_dwordx4 v14, s[8:11], s72 offen lds\n"
"v_mfma_f32_16x16x32_bf16 a[40:43], v[40:43], a[184:187], a[40:43]\n"
"s_add_u32 m0, s73, 45056\n"
"s_add_u32 s72, s72, 16384\n"
"v_perm_b32 v38, v84, v80, s49\n"
"v_mfma_f32_16x16x32_bf16 a[72:75], v[40:43], a[188:191], a[72:75]\n"
"s_add_u32 s57, s61, 512\n"
"s_and_b32 s57, s57, 896\n"
"s_add_u32 s58, s61, 640\n"
"v_mfma_f32_16x16x32_bf16 a[104:107], v[40:43], a[192:195], a[104:107]\n"
"s_lshl_b32 s71, s54, 1\n"
"s_and_b32 s58, s58, 896\n"
"s_add_u32 s59, s61, 768\n"
"v_mfma_f32_16x16x32_bf16 a[136:139], v[40:43], a[196:199], a[136:139]\n"
"s_and_b32 s59, s59, 896\n"
"buffer_load_dwordx4 v14, s[8:11], s72 offen lds\n"
"v_mfma_f32_16x16x32_bf16 a[12:15], v[44:47], a[180:183], a[12:15]\n"
"s_add_u32 m0, s73, 49152\n"
"s_add_u32 s72, s72, 16384\n"
"s_waitcnt vmcnt(10) lgkmcnt(2)\n"
"v_perm_b32 v32, v68, v64, s48\n"
"v_mfma_f32_16x16x32_bf16 a[44:47], v[44:47], a[184:187], a[44:47]\n"
"s_add_u32 s60, s61, 896\n"
"v_perm_b32 v40, v69, v65, s48\n"
"v_perm_b32 v41, v77, v73, s48\n"
"v_mfma_f32_16x16x32_bf16 a[76:79], v[44:47], a[188:191], a[76:79]\n"
"s_and_b32 s60, s60, 896\n"
"v_perm_b32 v42, v85, v81, s48\n"
"v_perm_b32 v35, v92, v88, s48\n"
"v_mfma_f32_16x16x32_bf16 a[108:111], v[44:47], a[192:195], a[108:111]\n"
"buffer_load_dwordx4 v14, s[8:11], s72 offen lds\n"
"v_mfma_f32_16x16x32_bf16 a[140:143], v[44:47], a[196:199], a[140:143]\n"
"s_add_u32 m0, s73, 53248\n"
"s_add_u32 s72, s72, 16384\n"
"v_perm_b32 v44, v69, v65, s49\n"
"v_mfma_f32_16x16x32_bf16 a[16:19], v[48:51], a[180:183], a[16:19]\n"
"v_perm_b32 v45, v77, v73, s49\n"
"v_perm_b32 v46, v85, v81, s49\n"
"v_mfma_f32_16x16x32_bf16 a[48:51], v[48:51], a[184:187], a[48:51]\n"
"v_perm_b32 v39, v92, v88, s49\n"
"v_perm_b32 v43, v93, v89, s48\n"
"v_mfma_f32_16x16x32_bf16 a[80:83], v[48:51], a[188:191], a[80:83]\n"
"buffer_load_dwordx4 v14, s[8:11], s72 offen lds\n"
"v_mfma_f32_16x16x32_bf16 a[112:115], v[48:51], a[192:195], a[112:115]\n"
"s_add_u32 m0, s73, 57344\n"
"s_add_u32 s72, s72, 16384\n"
"s_lshl_b32 s73, %2, 10\n"
"v_mfma_f32_16x16x32_bf16 a[144:147], v[48:51], a[196:199], a[144:147]\n"
"v_perm_b32 v48, v70, v66, s48\n"
"v_perm_b32 v49, v78, v74, s48\n"
"v_mfma_f32_16x16x32_bf16 a[20:23], v[52:55], a[180:183], a[20:23]\n"
"v_perm_b32 v50, v86, v82, s48\n"
"v_perm_b32 v47, v93, v89, s49\n"
"v_mfma_f32_16x16x32_bf16 a[52:55], v[52:55], a[184:187], a[52:55]\n"
"buffer_load_dwordx4 v14, s[8:11], s72 offen lds\n"
"v_mfma_f32_16x16x32_bf16 a[84:87], v[52:55], a[188:191], a[84:87]\n"
"s_lshr_b32 s72, s66, 12\n"
"s_add_u32 m0, s73, 61440\n"
"s_and_b32 s72, s72, 768\n"
"v_mfma_f32_16x16x32_bf16 a[116:119], v[52:55], a[192:195], a[116:119]\n"
"v_perm_b32 v51, v94, v90, s48\n"
"v_mfma_f32_16x16x32_bf16 a[148:151], v[52:55], a[196:199], a[148:151]\n"
"v_perm_b32 v52, v70, v66, s49\n"
"v_perm_b32 v53, v78, v74, s49\n"
"v_mfma_f32_16x16x32_bf16 a[24:27], v[56:59], a[180:183], a[24:27]\n"
"buffer_load_dwordx4 v[128:131], v0, s[20:23], s62 offen\n"
"v_mfma_f32_16x16x32_bf16 a[56:59], v[56:59], a[184:187], a[56:59]\n"
"v_perm_b32 v54, v86, v82, s49\n"
"v_perm_b32 v55, v94, v90, s49\n"
"v_mfma_f32_16x16x32_bf16 a[88:91], v[56:59], a[188:191], a[88:91]\n"
"v_mfma_f32_16x16x32_bf16 a[120:123], v[56:59], a[192:195], a[120:123]\n"
"v_mfma_f32_16x16x32_bf16 a[152:155], v[56:59], a[196:199], a[152:155]\n"
"v_perm_b32 v56, v71, v67, s48\n"
"v_perm_b32 v57, v79, v75, s48\n"
"v_mfma_f32_16x16x32_bf16 a[28:31], v[60:63], a[180:183], a[28:31]\n"
"buffer_load_dwordx4 v[132:135], v1, s[20:23], s62 offen\n"
"v_mfma_f32_16x16x32_bf16 a[60:63], v[60:63], a[184:187], a[60:63]\n"
"v_perm_b32 v58, v87, v83, s48\n"
"v_perm_b32 v59, v95, v91, s48\n"
"v_mfma_f32_16x16x32_bf16 a[92:95], v[60:63], a[188:191], a[92:95]\n"
"ds_read_b128 a[180:183], v11 offset:20480\n"
"v_mfma_f32_16x16x32_bf16 a[124:127], v[60:63], a[192:195], a[124:127]\n"
"ds_read_b128 a[184:187], v11 offset:24576\n"
"v_mfma_f32_16x16x32_bf16 a[156:159], v[60:63], a[196:199], a[156:159]\n"
"buffer_load_dwordx4 v[136:139], v2, s[20:23], s62 offen\n"
"v_mfma_f32_16x16x32_bf16 a[0:3], v[32:35], a[160:163], a[0:3]\n"
"v_perm_b32 v60, v71, v67, s49\n"
"v_perm_b32 v61, v79, v75, s49\n"
"v_mfma_f32_16x16x32_bf16 a[32:35], v[32:35], a[164:167], a[32:35]\n"
"v_perm_b32 v62, v87, v83, s49\n"
"v_perm_b32 v63, v95, v91, s49\n"
"v_mfma_f32_16x16x32_bf16 a[64:67], v[32:35], a[168:171], a[64:67]\n"
"s_waitcnt lgkmcnt(2)\n"
"ds_read_b128 a[188:191], v11 offset:28672\n"
"v_mfma_f32_16x16x32_bf16 a[96:99], v[32:35], a[172:175], a[96:99]\n"
"buffer_load_dwordx4 v[140:143], v3, s[20:23], s62 offen\n"
"v_mfma_f32_16x16x32_bf16 a[128:131], v[32:35], a[176:179], a[128:131]\n"
"s_waitcnt vmcnt(13)\n"
"v_perm_b32 v33, v108, v104, s48\n"
"s_waitcnt vmcnt(11)\n"
"v_perm_b32 v34, v116, v112, s48\n"
"v_mfma_f32_16x16x32_bf16 a[4:7], v[36:39], a[160:163], a[4:7]\n"
"ds_read_b128 a[192:195], v11 offset:32768\n"
"v_mfma_f32_16x16x32_bf16 a[36:39], v[36:39], a[164:167], a[36:39]\n"
"ds_read_b128 a[196:199], v11 offset:36864\n"
"v_add_u32 v11, 40960, v11\n"
"v_mfma_f32_16x16x32_bf16 a[68:71], v[36:39], a[168:171], a[68:71]\n"
"buffer_load_dwordx4 v[144:147], v4, s[20:23], s62 offen\n"
"v_mfma_f32_16x16x32_bf16 a[100:103], v[36:39], a[172:175], a[100:103]\n"
"s_waitcnt vmcnt(10)\n"
"v_perm_b32 v32, v100, v96, s48\n"
"v_perm_b32 v35, v124, v120, s48\n"
"v_mfma_f32_16x16x32_bf16 a[132:135], v[36:39], a[176:179], a[132:135]\n"
"v_perm_b32 v36, v100, v96, s49\n"
"v_perm_b32 v37, v108, v104, s49\n"
"v_mfma_f32_16x16x32_bf16 a[8:11], v[40:43], a[160:163], a[8:11]\n"
"v_perm_b32 v38, v116, v112, s49\n"
"v_perm_b32 v39, v124, v120, s49\n"
"v_mfma_f32_16x16x32_bf16 a[40:43], v[40:43], a[164:167], a[40:43]\n"
"buffer_load_dwordx4 v[148:151], v5, s[20:23], s62 offen\n"
"v_mfma_f32_16x16x32_bf16 a[72:75], v[40:43], a[168:171], a[72:75]\n"
"ds_read_b128 a[200:203], v23 offset:0\n"
"v_mfma_f32_16x16x32_bf16 a[104:107], v[40:43], a[172:175], a[104:107]\n"
"ds_read_b128 a[204:207], v24 offset:0\n"
"v_mfma_f32_16x16x32_bf16 a[136:139], v[40:43], a[176:179], a[136:139]\n"
"v_perm_b32 v40, v101, v97, s48\n"
"v_perm_b32 v41, v109, v105, s48\n"
"v_mfma_f32_16x16x32_bf16 a[12:15], v[44:47], a[160:163], a[12:15]\n"
"buffer_load_dwordx4 v[152:155], v6, s[20:23], s62 offen\n"
"v_mfma_f32_16x16x32_bf16 a[44:47], v[44:47], a[164:167], a[44:47]\n"
"v_perm_b32 v42, v117, v113, s48\n"
"v_perm_b32 v43, v125, v121, s48\n"
"v_mfma_f32_16x16x32_bf16 a[76:79], v[44:47], a[168:171], a[76:79]\n"
"ds_read_b128 a[208:211], v25 offset:0\n"
"v_mfma_f32_16x16x32_bf16 a[108:111], v[44:47], a[172:175], a[108:111]\n"
"ds_read_b128 a[212:215], v26 offset:0\n"
"v_mfma_f32_16x16x32_bf16 a[140:143], v[44:47], a[176:179], a[140:143]\n"
"buffer_load_dwordx4 v[156:159], v7, s[20:23], s62 offen\n"
"v_mfma_f32_16x16x32_bf16 a[16:19], v[48:51], a[160:163], a[16:19]\n"
"s_mul_i32 s62, s54, 640\n"
"v_perm_b32 v44, v101, v97, s49\n"
"v_perm_b32 v45, v109, v105, s49\n"
"v_mfma_f32_16x16x32_bf16 a[48:51], v[48:51], a[164:167], a[48:51]\n"
"v_perm_b32 v46, v117, v113, s49\n"
"v_perm_b32 v47, v125, v121, s49\n"
"v_mfma_f32_16x16x32_bf16 a[80:83], v[48:51], a[168:171], a[80:83]\n"
"ds_read_b128 a[216:219], v23 offset:16384\n"
"v_mfma_f32_16x16x32_bf16 a[112:115], v[48:51], a[172:175], a[112:115]\n"
"buffer_load_dwordx4 v[160:163], v0, s[20:23], s63 offen\n"
"v_mfma_f32_16x16x32_bf16 a[144:147], v[48:51], a[176:179], a[144:147]\n"
"v_perm_b32 v48, v102, v98, s48\n"
"v_perm_b32 v49, v110, v106, s48\n"
"v_mfma_f32_16x16x32_bf16 a[20:23], v[52:55], a[160:163], a[20:23]\n"
"v_perm_b32 v50, v118, v114, s48\n"
"v_perm_b32 v51, v126, v122, s48\n"
"v_mfma_f32_16x16x32_bf16 a[52:55], v[52:55], a[164:167], a[52:55]\n"
"ds_read_b128 a[220:223], v24 offset:16384\n"
"v_mfma_f32_16x16x32_bf16 a[84:87], v[52:55], a[168:171], a[84:87]\n"
"ds_read_b128 a[224:227], v25 offset:16384\n"
"v_mfma_f32_16x16x32_bf16 a[116:119], v[52:55], a[172:175], a[116:119]\n"
"buffer_load_dwordx4 v[164:167], v1, s[20:23], s63 offen\n"
"v_mfma_f32_16x16x32_bf16 a[148:151], v[52:55], a[176:179], a[148:151]\n"
"v_perm_b32 v52, v102, v98, s49\n"
"v_perm_b32 v53, v110, v106, s49\n"
"v_mfma_f32_16x16x32_bf16 a[24:27], v[56:59], a[160:163], a[24:27]\n"
"v_perm_b32 v54, v118, v114, s49\n"
"v_perm_b32 v55, v126, v122, s49\n"
"v_mfma_f32_16x16x32_bf16 a[56:59], v[56:59], a[164:167], a[56:59]\n"
"ds_read_b128 a[228:231], v26 offset:16384\n"
"v_mfma_f32_16x16x32_bf16 a[88:91], v[56:59], a[168:171], a[88:91]\n"
"buffer_load_dwordx4 v[168:171], v2, s[20:23], s63 offen\n"
"v_mfma_f32_16x16x32_bf16 a[120:123], v[56:59], a[172:175], a[120:123]\n"
"ds_read_b128 a[232:235], v23 offset:32768\n"
"v_mfma_f32_16x16x32_bf16 a[152:155], v[56:59], a[176:179], a[152:155]\n"
"v_perm_b32 v56, v103, v99, s48\n"
"v_perm_b32 v57, v111, v107, s48\n"
"v_mfma_f32_16x16x32_bf16 a[28:31], v[60:63], a[160:163], a[28:31]\n"
"v_perm_b32 v58, v119, v115, s48\n"
"v_perm_b32 v59, v127, v123, s48\n"
"v_mfma_f32_16x16x32_bf16 a[60:63], v[60:63], a[164:167], a[60:63]\n"
"buffer_load_dwordx4 v[172:175], v3, s[20:23], s63 offen\n"
"v_mfma_f32_16x16x32_bf16 a[92:95], v[60:63], a[168:171], a[92:95]\n"
"ds_read_b128 a[236:239], v24 offset:32768\n"
"v_mfma_f32_16x16x32_bf16 a[124:127], v[60:63], a[172:175], a[124:127]\n"
"ds_read_b128 a[240:243], v25 offset:32768\n"
"v_mfma_f32_16x16x32_bf16 a[156:159], v[60:63], a[176:179], a[156:159]\n"
"s_waitcnt lgkmcnt(4)\n"
"v_perm_b32 v60, v103, v99, s49\n"
"v_perm_b32 v61, v111, v107, s49\n"
"v_mfma_f32_16x16x32_bf16 a[0:3], v[32:35], a[180:183], a[0:3]\n"
"buffer_load_dwordx4 v[176:179], v4, s[20:23], s63 offen\n"
"v_mfma_f32_16x16x32_bf16 a[32:35], v[32:35], a[184:187], a[32:35]\n"
"v_perm_b32 v62, v119, v115, s49\n"
"v_perm_b32 v63, v127, v123, s49\n"
"v_mfma_f32_16x16x32_bf16 a[64:67], v[32:35], a[188:191], a[64:67]\n"
"ds_read_b128 a[244:247], v26 offset:32768\n"
"v_mfma_f32_16x16x32_bf16 a[96:99], v[32:35], a[192:195], a[96:99]\n"
"ds_read_b128 a[248:251], v23 offset:49152\n"
"v_add_u32 v23, 16384, v23\n"
"v_mfma_f32_16x16x32_bf16 a[128:131], v[32:35], a[196:199], a[128:131]\n"
"buffer_load_dwordx4 v[180:183], v5, s[20:23], s63 offen\n"
"v_mfma_f32_16x16x32_bf16 a[4:7], v[36:39], a[180:183], a[4:7]\n"
"s_waitcnt vmcnt(10) lgkmcnt(5)\n"
"v_perm_b32 v33, v140, v136, s48\n"
"ds_read_b128 a[252:255], v24 offset:49152\n"
"v_mfma_f32_16x16x32_bf16 a[36:39], v[36:39], a[184:187], a[36:39]\n"
"ds_read_b128 v[224:227], v25 offset:49152\n"
"v_add_u32 v24, 16384, v24\n"
"v_mfma_f32_16x16x32_bf16 a[68:71], v[36:39], a[188:191], a[68:71]\n"
"ds_read_b128 v[228:231], v26 offset:49152\n"
"v_add_u32 v25, 16384, v25\n"
"v_mfma_f32_16x16x32_bf16 a[100:103], v[36:39], a[192:195], a[100:103]\n"
"buffer_load_dwordx4 v[184:187], v6, s[20:23], s63 offen\n"
"v_mfma_f32_16x16x32_bf16 a[132:135], v[36:39], a[196:199], a[132:135]\n"
"v_perm_b32 v36, v132, v128, s49\n"
"v_perm_b32 v37, v140, v136, s49\n"
"v_mfma_f32_16x16x32_bf16 a[8:11], v[40:43], a[180:183], a[8:11]\n"
"ds_read_b128 v[232:235], v23 offset:49152\n"
"v_add_u32 v26, 16384, v26\n"
"v_mfma_f32_16x16x32_bf16 a[40:43], v[40:43], a[184:187], a[40:43]\n"
"ds_read_b128 v[236:239], v24 offset:49152\n"
"v_mfma_f32_16x16x32_bf16 a[72:75], v[40:43], a[188:191], a[72:75]\n"
"buffer_load_dwordx4 v[188:191], v7, s[20:23], s63 offen\n"
"v_mfma_f32_16x16x32_bf16 a[104:107], v[40:43], a[192:195], a[104:107]\n"
"s_mul_i32 s63, s55, 640\n"
"s_waitcnt vmcnt(10) lgkmcnt(7)\n"
"v_perm_b32 v34, v148, v144, s48\n"
"v_perm_b32 v38, v148, v144, s49\n"
"v_mfma_f32_16x16x32_bf16 a[136:139], v[40:43], a[196:199], a[136:139]\n"
"s_add_u32 s63, s63, 2560\n"
"v_perm_b32 v40, v133, v129, s48\n"
"v_perm_b32 v41, v141, v137, s48\n"
"v_mfma_f32_16x16x32_bf16 a[12:15], v[44:47], a[180:183], a[12:15]\n"
"v_perm_b32 v42, v149, v145, s48\n"
"ds_read_b128 v[240:243], v25 offset:49152\n"
"v_mfma_f32_16x16x32_bf16 a[44:47], v[44:47], a[184:187], a[44:47]\n"
"buffer_load_dwordx4 v[192:195], v0, s[20:23], s64 offen\n"
"v_mfma_f32_16x16x32_bf16 a[76:79], v[44:47], a[188:191], a[76:79]\n"
"ds_read_b128 v[244:247], v26 offset:49152\n"
"v_mfma_f32_16x16x32_bf16 a[108:111], v[44:47], a[192:195], a[108:111]\n"
"v_mfma_f32_16x16x32_bf16 a[140:143], v[44:47], a[196:199], a[140:143]\n"
"v_perm_b32 v44, v133, v129, s49\n"
"v_perm_b32 v45, v141, v137, s49\n"
"v_mfma_f32_16x16x32_bf16 a[16:19], v[48:51], a[180:183], a[16:19]\n"
"s_waitcnt vmcnt(9) lgkmcnt(4)\n"
"v_perm_b32 v46, v149, v145, s49\n"
"v_perm_b32 v32, v132, v128, s48\n"
"v_mfma_f32_16x16x32_bf16 a[48:51], v[48:51], a[184:187], a[48:51]\n"
"buffer_load_dwordx4 v[196:199], v1, s[20:23], s64 offen\n"
"v_mfma_f32_16x16x32_bf16 a[80:83], v[48:51], a[188:191], a[80:83]\n"
"v_perm_b32 v35, v156, v152, s48\n"
"v_perm_b32 v39, v156, v152, s49\n"
"v_mfma_f32_16x16x32_bf16 a[112:115], v[48:51], a[192:195], a[112:115]\n"
"v_perm_b32 v43, v157, v153, s48\n"
"v_perm_b32 v47, v157, v153, s49\n"
"v_mfma_f32_16x16x32_bf16 a[144:147], v[48:51], a[196:199], a[144:147]\n"
"v_perm_b32 v48, v134, v130, s48\n"
"v_perm_b32 v49, v142, v138, s48\n"
"v_mfma_f32_16x16x32_bf16 a[20:23], v[52:55], a[180:183], a[20:23]\n"
"buffer_load_dwordx4 v[200:203], v2, s[20:23], s64 offen\n"
"v_mfma_f32_16x16x32_bf16 a[52:55], v[52:55], a[184:187], a[52:55]\n"
"v_perm_b32 v50, v150, v146, s48\n"
"v_perm_b32 v51, v158, v154, s48\n"
"v_mfma_f32_16x16x32_bf16 a[84:87], v[52:55], a[188:191], a[84:87]\n"
"s_waitcnt lgkmcnt(0)\n"
"s_barrier\n"
"ds_read_b128 a[160:163], v8 offset:0\n"
"v_mfma_f32_16x16x32_bf16 a[116:119], v[52:55], a[192:195], a[116:119]\n"
"ds_read_b128 a[164:167], v8 offset:4096\n"
"v_mfma_f32_16x16x32_bf16 a[148:151], v[52:55], a[196:199], a[148:151]\n"
"buffer_load_dwordx4 v[204:207], v3, s[20:23], s64 offen\n"
"v_mfma_f32_16x16x32_bf16 a[24:27], v[56:59], a[180:183], a[24:27]\n"
"v_perm_b32 v52, v134, v130, s49\n"
"v_perm_b32 v53, v142, v138, s49\n"
"v_mfma_f32_16x16x32_bf16 a[56:59], v[56:59], a[184:187], a[56:59]\n"
"v_perm_b32 v54, v150, v146, s49\n"
"v_perm_b32 v55, v158, v154, s49\n"
"v_mfma_f32_16x16x32_bf16 a[88:91], v[56:59], a[188:191], a[88:91]\n"
"ds_read_b128 a[168:171], v8 offset:8192\n"
"v_mfma_f32_16x16x32_bf16 a[120:123], v[56:59], a[192:195], a[120:123]\n"
"buffer_load_dwordx4 v[208:211], v4, s[20:23], s64 offen\n"
"v_mfma_f32_16x16x32_bf16 a[152:155], v[56:59], a[196:199], a[152:155]\n"
"v_perm_b32 v56, v135, v131, s48\n"
"v_perm_b32 v57, v143, v139, s48\n"
"v_mfma_f32_16x16x32_bf16 a[28:31], v[60:63], a[180:183], a[28:31]\n"
"v_perm_b32 v58, v151, v147, s48\n"
"v_perm_b32 v59, v159, v155, s48\n"
"v_mfma_f32_16x16x32_bf16 a[60:63], v[60:63], a[184:187], a[60:63]\n"
"ds_read_b128 a[172:175], v8 offset:12288\n"
"v_mfma_f32_16x16x32_bf16 a[92:95], v[60:63], a[188:191], a[92:95]\n"
"buffer_load_dwordx4 v[212:215], v5, s[20:23], s64 offen\n"
"v_mfma_f32_16x16x32_bf16 a[124:127], v[60:63], a[192:195], a[124:127]\n"
"ds_read_b128 a[176:179], v8 offset:16384\n"
"v_mfma_f32_16x16x32_bf16 a[156:159], v[60:63], a[196:199], a[156:159]\n"
"s_waitcnt lgkmcnt(4)\n"
"v_perm_b32 v60, v135, v131, s49\n"
"v_perm_b32 v61, v143, v139, s49\n"
"v_mfma_f32_16x16x32_bf16 a[0:3], v[32:35], a[160:163], a[0:3]\n"
"s_waitcnt lgkmcnt(3)\n"
"v_perm_b32 v62, v151, v147, s49\n"
"v_perm_b32 v63, v159, v155, s49\n"
"v_mfma_f32_16x16x32_bf16 a[32:35], v[32:35], a[164:167], a[32:35]\n"
"s_waitcnt lgkmcnt(2)\n"
"buffer_load_dwordx4 v[216:219], v6, s[20:23], s64 offen\n"
"v_mfma_f32_16x16x32_bf16 a[64:67], v[32:35], a[168:171], a[64:67]\n"
"ds_read_b128 a[180:183], v9 offset:0\n"
"ds_read_b128 a[184:187], v9 offset:4096\n"
"ds_read_b128 a[188:191], v9 offset:8192\n"
"ds_read_b128 a[192:195], v9 offset:12288\n"
"ds_read_b128 a[196:199], v9 offset:16384\n"
"s_waitcnt lgkmcnt(6)\n"
"v_mfma_f32_16x16x32_bf16 a[96:99], v[32:35], a[172:175], a[96:99]\n"
"buffer_load_dwordx4 v[220:223], v7, s[20:23], s64 offen\n"
"s_mul_i32 s64, s56, 640\n"
"s_add_u32 s64, s64, 5120\n"
"s_waitcnt lgkmcnt(5)\n"
"v_mfma_f32_16x16x32_bf16 a[128:131], v[32:35], a[176:179], a[128:131]\n"
"s_waitcnt vmcnt(12)\n"
"v_perm_b32 v33, v172, v168, s48\n"
"s_waitcnt vmcnt(10)\n"
"v_perm_b32 v34, v180, v176, s48\n"
"v_mfma_f32_16x16x32_bf16 a[4:7], v[36:39], a[160:163], a[4:7]\n"
"v_mfma_f32_16x16x32_bf16 a[36:39], v[36:39], a[164:167], a[36:39]\n"
"buffer_load_dwordx4 v14, s[8:11], s72 offen lds\n"
"v_mfma_f32_16x16x32_bf16 a[68:71], v[36:39], a[168:171], a[68:71]\n"
"s_add_u32 m0, s73, 65536\n"
"s_add_u32 s72, s72, 16384\n"
"v_mfma_f32_16x16x32_bf16 a[100:103], v[36:39], a[172:175], a[100:103]\n"
"v_mfma_f32_16x16x32_bf16 a[132:135], v[36:39], a[176:179], a[132:135]\n"
"v_perm_b32 v36, v164, v160, s49\n"
"v_perm_b32 v37, v172, v168, s49\n"
"v_mfma_f32_16x16x32_bf16 a[8:11], v[40:43], a[160:163], a[8:11]\n"
"buffer_load_dwordx4 v14, s[8:11], s72 offen lds\n"
"v_mfma_f32_16x16x32_bf16 a[40:43], v[40:43], a[164:167], a[40:43]\n"
"s_add_u32 m0, s73, 69632\n"
"s_add_u32 s72, s72, 16384\n"
"s_waitcnt vmcnt(10) lgkmcnt(4)\n"
"v_perm_b32 v32, v164, v160, s48\n"
"v_mfma_f32_16x16x32_bf16 a[72:75], v[40:43], a[168:171], a[72:75]\n"
"v_perm_b32 v38, v180, v176, s49\n"
"v_perm_b32 v35, v188, v184, s48\n"
"v_mfma_f32_16x16x32_bf16 a[104:107], v[40:43], a[172:175], a[104:107]\n"
"v_perm_b32 v39, v188, v184, s49\n"
"v_mfma_f32_16x16x32_bf16 a[136:139], v[40:43], a[176:179], a[136:139]\n"
"buffer_load_dwordx4 v14, s[8:11], s72 offen lds\n"
"v_mfma_f32_16x16x32_bf16 a[12:15], v[44:47], a[160:163], a[12:15]\n"
"s_add_u32 m0, s73, 73728\n"
"s_add_u32 s72, s72, 16384\n"
"v_perm_b32 v40, v165, v161, s48\n"
"v_mfma_f32_16x16x32_bf16 a[44:47], v[44:47], a[164:167], a[44:47]\n"
"v_perm_b32 v41, v173, v169, s48\n"
"v_perm_b32 v42, v181, v177, s48\n"
"v_mfma_f32_16x16x32_bf16 a[76:79], v[44:47], a[168:171], a[76:79]\n"
"v_perm_b32 v43, v189, v185, s48\n"
"v_mfma_f32_16x16x32_bf16 a[108:111], v[44:47], a[172:175], a[108:111]\n"
"v_mfma_f32_16x16x32_bf16 a[140:143], v[44:47], a[176:179], a[140:143]\n"
"buffer_load_dwordx4 v14, s[8:11], s72 offen lds\n"
"v_mfma_f32_16x16x32_bf16 a[16:19], v[48:51], a[160:163], a[16:19]\n"
"s_add_u32 m0, s73, 77824\n"
"s_add_u32 s72, s72, 16384\n"
"v_perm_b32 v44, v165, v161, s49\n"
"v_mfma_f32_16x16x32_bf16 a[48:51], v[48:51], a[164:167], a[48:51]\n"
"v_perm_b32 v45, v173, v169, s49\n"
"v_perm_b32 v46, v181, v177, s49\n"
"v_mfma_f32_16x16x32_bf16 a[80:83], v[48:51], a[168:171], a[80:83]\n"
"v_perm_b32 v47, v189, v185, s49\n"
"v_mfma_f32_16x16x32_bf16 a[112:115], v[48:51], a[172:175], a[112:115]\n"
"buffer_load_dwordx4 v14, s[8:11], s72 offen lds\n"
"v_mfma_f32_16x16x32_bf16 a[144:147], v[48:51], a[176:179], a[144:147]\n"
"s_add_u32 m0, s75, 0\n"
"v_perm_b32 v48, v166, v162, s48\n"
"v_perm_b32 v49, v174, v170, s48\n"
"v_mfma_f32_16x16x32_bf16 a[20:23], v[52:55], a[160:163], a[20:23]\n"
"s_add_u32 s72, s54, 32768\n"
"v_perm_b32 v50, v182, v178, s48\n"
"v_perm_b32 v51, v190, v186, s48\n"
"v_mfma_f32_16x16x32_bf16 a[52:55], v[52:55], a[164:167], a[52:55]\n"
"v_mov_b32 v14, -1\n"
"v_mfma_f32_16x16x32_bf16 a[84:87], v[52:55], a[168:171], a[84:87]\n"
"buffer_load_dwordx4 v[64:67], v0, s[20:23], s65 offen\n"
"v_mfma_f32_16x16x32_bf16 a[116:119], v[52:55], a[172:175], a[116:119]\n"
"v_mfma_f32_16x16x32_bf16 a[148:151], v[52:55], a[176:179], a[148:151]\n"
"v_perm_b32 v52, v166, v162, s49\n"
"v_perm_b32 v53, v174, v170, s49\n"
"v_mfma_f32_16x16x32_bf16 a[24:27], v[56:59], a[160:163], a[24:27]\n"
"v_perm_b32 v54, v182, v178, s49\n"
"v_perm_b32 v55, v190, v186, s49\n"
"v_mfma_f32_16x16x32_bf16 a[56:59], v[56:59], a[164:167], a[56:59]\n"
"buffer_load_dwordx4 v[68:71], v1, s[20:23], s65 offen\n"
"v_mfma_f32_16x16x32_bf16 a[88:91], v[56:59], a[168:171], a[88:91]\n"
"v_mfma_f32_16x16x32_bf16 a[120:123], v[56:59], a[172:175], a[120:123]\n"
"v_mfma_f32_16x16x32_bf16 a[152:155], v[56:59], a[176:179], a[152:155]\n"
"v_perm_b32 v56, v167, v163, s48\n"
"v_perm_b32 v57, v175, v171, s48\n"
"v_mfma_f32_16x16x32_bf16 a[28:31], v[60:63], a[160:163], a[28:31]\n"
"buffer_load_dwordx4 v[72:75], v2, s[20:23], s65 offen\n"
"v_mfma_f32_16x16x32_bf16 a[60:63], v[60:63], a[164:167], a[60:63]\n"
"v_perm_b32 v58, v183, v179, s48\n"
"v_perm_b32 v59, v191, v187, s48\n"
"v_mfma_f32_16x16x32_bf16 a[92:95], v[60:63], a[168:171], a[92:95]\n"
"ds_read_b128 a[160:163], v10 offset:0\n"
"v_mfma_f32_16x16x32_bf16 a[124:127], v[60:63], a[172:175], a[124:127]\n"
"ds_read_b128 a[164:167], v10 offset:4096\n"
"v_mfma_f32_16x16x32_bf16 a[156:159], v[60:63], a[176:179], a[156:159]\n"
"buffer_load_dwordx4 v[76:79], v3, s[20:23], s65 offen\n"
"v_mfma_f32_16x16x32_bf16 a[0:3], v[32:35], a[180:183], a[0:3]\n"
"s_waitcnt lgkmcnt(2)\n"
"v_perm_b32 v60, v167, v163, s49\n"
"v_perm_b32 v61, v175, v171, s49\n"
"v_mfma_f32_16x16x32_bf16 a[32:35], v[32:35], a[184:187], a[32:35]\n"
"v_perm_b32 v62, v183, v179, s49\n"
"v_perm_b32 v63, v191, v187, s49\n"
"v_mfma_f32_16x16x32_bf16 a[64:67], v[32:35], a[188:191], a[64:67]\n"
"ds_read_b128 a[168:171], v10 offset:8192\n"
"v_mfma_f32_16x16x32_bf16 a[96:99], v[32:35], a[192:195], a[96:99]\n"
"buffer_load_dwordx4 v[80:83], v4, s[20:23], s65 offen\n"
"v_mfma_f32_16x16x32_bf16 a[128:131], v[32:35], a[196:199], a[128:131]\n"
"s_waitcnt vmcnt(10)\n"
"v_perm_b32 v32, v196, v192, s48\n"
"v_perm_b32 v33, v204, v200, s48\n"
"v_mfma_f32_16x16x32_bf16 a[4:7], v[36:39], a[180:183], a[4:7]\n"
"v_perm_b32 v34, v212, v208, s48\n"
"v_perm_b32 v35, v220, v216, s48\n"
"v_mfma_f32_16x16x32_bf16 a[36:39], v[36:39], a[184:187], a[36:39]\n"
"ds_read_b128 a[172:175], v10 offset:12288\n"
"v_mfma_f32_16x16x32_bf16 a[68:71], v[36:39], a[188:191], a[68:71]\n"
"buffer_load_dwordx4 v[84:87], v5, s[20:23], s65 offen\n"
"v_mfma_f32_16x16x32_bf16 a[100:103], v[36:39], a[192:195], a[100:103]\n"
"ds_read_b128 a[176:179], v10 offset:16384\n"
"v_mfma_f32_16x16x32_bf16 a[132:135], v[36:39], a[196:199], a[132:135]\n"
"v_perm_b32 v36, v196, v192, s49\n"
"v_perm_b32 v37, v204, v200, s49\n"
"v_mfma_f32_16x16x32_bf16 a[8:11], v[40:43], a[180:183], a[8:11]\n"
"v_perm_b32 v38, v212, v208, s49\n"
"v_perm_b32 v39, v220, v216, s49\n"
"v_mfma_f32_16x16x32_bf16 a[40:43], v[40:43], a[184:187], a[40:43]\n"
"v_mfma_f32_16x16x32_bf16 a[72:75], v[40:43], a[188:191], a[72:75]\n"
"buffer_load_dwordx4 v[88:91], v6, s[20:23], s65 offen\n"
"v_mfma_f32_16x16x32_bf16 a[104:107], v[40:43], a[192:195], a[104:107]\n"
"v_mfma_f32_16x16x32_bf16 a[136:139], v[40:43], a[196:199], a[136:139]\n"
"v_perm_b32 v40, v197, v193, s48\n"
"v_perm_b32 v41, v205, v201, s48\n"
"v_mfma_f32_16x16x32_bf16 a[12:15], v[44:47], a[180:183], a[12:15]\n"
"v_perm_b32 v42, v213, v209, s48\n"
"v_perm_b32 v43, v221, v217, s48\n"
"v_mfma_f32_16x16x32_bf16 a[44:47], v[44:47], a[184:187], a[44:47]\n"
"buffer_load_dwordx4 v[92:95], v7, s[20:23], s65 offen\n"
"v_mfma_f32_16x16x32_bf16 a[76:79], v[44:47], a[188:191], a[76:79]\n"
"s_mul_i32 s65, s57, 640\n"
"s_add_u32 s65, s65, 7680\n"
"v_mfma_f32_16x16x32_bf16 a[108:111], v[44:47], a[192:195], a[108:111]\n"
"v_mfma_f32_16x16x32_bf16 a[140:143], v[44:47], a[196:199], a[140:143]\n"
"v_perm_b32 v44, v197, v193, s49\n"
"v_perm_b32 v45, v205, v201, s49\n"
"v_mfma_f32_16x16x32_bf16 a[16:19], v[48:51], a[180:183], a[16:19]\n"
"buffer_load_dwordx4 v[96:99], v0, s[20:23], s66 offen\n"
"v_mfma_f32_16x16x32_bf16 a[48:51], v[48:51], a[184:187], a[48:51]\n"
"v_perm_b32 v46, v213, v209, s49\n"
"v_perm_b32 v47, v221, v217, s49\n"
"v_mfma_f32_16x16x32_bf16 a[80:83], v[48:51], a[188:191], a[80:83]\n"
"v_mfma_f32_16x16x32_bf16 a[112:115], v[48:51], a[192:195], a[112:115]\n"
"v_mfma_f32_16x16x32_bf16 a[144:147], v[48:51], a[196:199], a[144:147]\n"
"buffer_load_dwordx4 v[100:103], v1, s[20:23], s66 offen\n"
"v_mfma_f32_16x16x32_bf16 a[20:23], v[52:55], a[180:183], a[20:23]\n"
"v_perm_b32 v48, v198, v194, s48\n"
"v_perm_b32 v49, v206, v202, s48\n"
"v_mfma_f32_16x16x32_bf16 a[52:55], v[52:55], a[184:187], a[52:55]\n"
"v_perm_b32 v50, v214, v210, s48\n"
"v_perm_b32 v51, v222, v218, s48\n"
"v_mfma_f32_16x16x32_bf16 a[84:87], v[52:55], a[188:191], a[84:87]\n"
"v_mfma_f32_16x16x32_bf16 a[116:119], v[52:55], a[192:195], a[116:119]\n"
"buffer_load_dwordx4 v[104:107], v2, s[20:23], s66 offen\n"
"v_mfma_f32_16x16x32_bf16 a[148:151], v[52:55], a[196:199], a[148:151]\n"
"v_perm_b32 v52, v198, v194, s49\n"
"v_perm_b32 v53, v206, v202, s49\n"
"v_mfma_f32_16x16x32_bf16 a[24:27], v[56:59], a[180:183], a[24:27]\n"
"v_perm_b32 v54, v214, v210, s49\n"
"v_perm_b32 v55, v222, v218, s49\n"
"v_mfma_f32_16x16x32_bf16 a[56:59], v[56:59], a[184:187], a[56:59]\n"
"v_mfma_f32_16x16x32_bf16 a[88:91], v[56:59], a[188:191], a[88:91]\n"
"buffer_load_dwordx4 v[108:111], v3, s[20:23], s66 offen\n"
"v_mfma_f32_16x16x32_bf16 a[120:123], v[56:59], a[192:195], a[120:123]\n"
"v_mfma_f32_16x16x32_bf16 a[152:155], v[56:59], a[196:199], a[152:155]\n"
"v_perm_b32 v56, v199, v195, s48\n"
"v_perm_b32 v57, v207, v203, s48\n"
"v_mfma_f32_16x16x32_bf16 a[28:31], v[60:63], a[180:183], a[28:31]\n"
"v_perm_b32 v58, v215, v211, s48\n"
"v_perm_b32 v59, v223, v219, s48\n"
"v_mfma_f32_16x16x32_bf16 a[60:63], v[60:63], a[184:187], a[60:63]\n"
"buffer_load_dwordx4 v[112:115], v4, s[20:23], s66 offen\n"
"v_mfma_f32_16x16x32_bf16 a[92:95], v[60:63], a[188:191], a[92:95]\n"
"ds_read_b128 a[180:183], v11 offset:0\n"
"v_mfma_f32_16x16x32_bf16 a[124:127], v[60:63], a[192:195], a[124:127]\n"
"ds_read_b128 a[184:187], v11 offset:4096\n"
"v_mfma_f32_16x16x32_bf16 a[156:159], v[60:63], a[196:199], a[156:159]\n"
"s_waitcnt lgkmcnt(2)\n"
"v_perm_b32 v60, v199, v195, s49\n"
"v_perm_b32 v61, v207, v203, s49\n"
"v_mfma_f32_16x16x32_bf16 a[0:3], v[32:35], a[160:163], a[0:3]\n"
"buffer_load_dwordx4 v[116:119], v5, s[20:23], s66 offen\n"
"v_mfma_f32_16x16x32_bf16 a[32:35], v[32:35], a[164:167], a[32:35]\n"
"v_perm_b32 v62, v215, v211, s49\n"
"v_perm_b32 v63, v223, v219, s49\n"
"v_mfma_f32_16x16x32_bf16 a[64:67], v[32:35], a[168:171], a[64:67]\n"
"ds_read_b128 a[188:191], v11 offset:8192\n"
"v_mfma_f32_16x16x32_bf16 a[96:99], v[32:35], a[172:175], a[96:99]\n"
"ds_read_b128 a[192:195], v11 offset:12288\n"
"v_mfma_f32_16x16x32_bf16 a[128:131], v[32:35], a[176:179], a[128:131]\n"
"s_waitcnt vmcnt(10)\n"
"ds_read_b128 a[196:199], v11 offset:16384\n"
"v_perm_b32 v33, v76, v72, s48\n"
"v_mfma_f32_16x16x32_bf16 a[4:7], v[36:39], a[160:163], a[4:7]\n"
"buffer_load_dwordx4 v[120:123], v6, s[20:23], s66 offen\n"
"v_mfma_f32_16x16x32_bf16 a[36:39], v[36:39], a[164:167], a[36:39]\n"
"v_mfma_f32_16x16x32_bf16 a[68:71], v[36:39], a[168:171], a[68:71]\n"
"v_mfma_f32_16x16x32_bf16 a[100:103], v[36:39], a[172:175], a[100:103]\n"
"v_mfma_f32_16x16x32_bf16 a[132:135], v[36:39], a[176:179], a[132:135]\n"
"buffer_load_dwordx4 v[124:127], v7, s[20:23], s66 offen\n"
"v_mfma_f32_16x16x32_bf16 a[8:11], v[40:43], a[160:163], a[8:11]\n"
"s_mul_i32 s66, s58, 640\n"
"v_perm_b32 v37, v76, v72, s49\n"
"s_waitcnt vmcnt(10) lgkmcnt(4)\n"
"v_perm_b32 v34, v84, v80, s48\n"
"v_mfma_f32_16x16x32_bf16 a[40:43], v[40:43], a[164:167], a[40:43]\n"
"s_add_u32 s66, s66, 10240\n"
"v_perm_b32 v38, v84, v80, s49\n"
"v_perm_b32 v36, v68, v64, s49\n"
"v_mfma_f32_16x16x32_bf16 a[72:75], v[40:43], a[168:171], a[72:75]\n"
"v_mfma_f32_16x16x32_bf16 a[104:107], v[40:43], a[172:175], a[104:107]\n"
"buffer_load_dwordx4 v[128:131], v0, s[20:23], s67 offen\n"
"v_mfma_f32_16x16x32_bf16 a[136:139], v[40:43], a[176:179], a[136:139]\n"
"v_perm_b32 v41, v77, v73, s48\n"
"v_perm_b32 v42, v85, v81, s48\n"
"v_mfma_f32_16x16x32_bf16 a[12:15], v[44:47], a[160:163], a[12:15]\n"
"v_perm_b32 v40, v69, v65, s48\n"
"v_mfma_f32_16x16x32_bf16 a[44:47], v[44:47], a[164:167], a[44:47]\n"
"v_mfma_f32_16x16x32_bf16 a[76:79], v[44:47], a[168:171], a[76:79]\n"
"buffer_load_dwordx4 v[132:135], v1, s[20:23], s67 offen\n"
"v_mfma_f32_16x16x32_bf16 a[108:111], v[44:47], a[172:175], a[108:111]\n"
"s_waitcnt vmcnt(10) lgkmcnt(0)\n"
"v_perm_b32 v43, v93, v89, s48\n"
"v_perm_b32 v32, v68, v64, s48\n"
"v_mfma_f32_16x16x32_bf16 a[140:143], v[44:47], a[176:179], a[140:143]\n"
"v_perm_b32 v47, v93, v89, s49\n"
"v_lshrrev_b32 v93, 3, v16\n"
"v_mfma_f32_16x16x32_bf16 a[16:19], v[48:51], a[160:163], a[16:19]\n"
"v_lshlrev_b32 v93, 12, v93\n"
"v_perm_b32 v45, v77, v73, s49\n"
"v_mfma_f32_16x16x32_bf16 a[48:51], v[48:51], a[164:167], a[48:51]\n"
"buffer_load_dwordx4 v[136:139], v2, s[20:23], s67 offen\n"
"v_mfma_f32_16x16x32_bf16 a[80:83], v[48:51], a[168:171], a[80:83]\n"
"v_perm_b32 v46, v85, v81, s49\n"
"v_perm_b32 v44, v69, v65, s49\n"
"v_mfma_f32_16x16x32_bf16 a[112:115], v[48:51], a[172:175], a[112:115]\n"
"v_perm_b32 v35, v92, v88, s48\n"
"v_perm_b32 v39, v92, v88, s49\n"
"v_mfma_f32_16x16x32_bf16 a[144:147], v[48:51], a[176:179], a[144:147]\n"
"v_perm_b32 v51, v94, v90, s48\n"
"v_perm_b32 v49, v78, v74, s48\n"
"v_mfma_f32_16x16x32_bf16 a[20:23], v[52:55], a[160:163], a[20:23]\n"
"buffer_load_dwordx4 v[140:143], v3, s[20:23], s67 offen\n"
"v_mfma_f32_16x16x32_bf16 a[52:55], v[52:55], a[164:167], a[52:55]\n"
"v_perm_b32 v50, v86, v82, s48\n"
"v_perm_b32 v48, v70, v66, s48\n"
"v_mfma_f32_16x16x32_bf16 a[84:87], v[52:55], a[168:171], a[84:87]\n"
"v_mfma_f32_16x16x32_bf16 a[116:119], v[52:55], a[172:175], a[116:119]\n"
"v_mfma_f32_16x16x32_bf16 a[148:151], v[52:55], a[176:179], a[148:151]\n"
"buffer_load_dwordx4 v[144:147], v4, s[20:23], s67 offen\n"
"v_mfma_f32_16x16x32_bf16 a[24:27], v[56:59], a[160:163], a[24:27]\n"
"v_perm_b32 v55, v94, v90, s49\n"
"v_and_b32 v94, 7, v16\n"
"v_mfma_f32_16x16x32_bf16 a[56:59], v[56:59], a[164:167], a[56:59]\n"
"v_perm_b32 v53, v78, v74, s49\n"
"v_perm_b32 v54, v86, v82, s49\n"
"v_mfma_f32_16x16x32_bf16 a[88:91], v[56:59], a[168:171], a[88:91]\n"
"v_perm_b32 v52, v70, v66, s49\n"
"v_mfma_f32_16x16x32_bf16 a[120:123], v[56:59], a[172:175], a[120:123]\n"
"buffer_load_dwordx4 v[148:151], v5, s[20:23], s67 offen\n"
"v_mfma_f32_16x16x32_bf16 a[152:155], v[56:59], a[176:179], a[152:155]\n"
"v_perm_b32 v57, v79, v75, s48\n"
"v_perm_b32 v58, v87, v83, s48\n"
"v_mfma_f32_16x16x32_bf16 a[28:31], v[60:63], a[160:163], a[28:31]\n"
"v_perm_b32 v56, v71, v67, s48\n"
"v_perm_b32 v59, v95, v91, s48\n"
"v_mfma_f32_16x16x32_bf16 a[60:63], v[60:63], a[164:167], a[60:63]\n"
"v_mfma_f32_16x16x32_bf16 a[92:95], v[60:63], a[168:171], a[92:95]\n"
"v_mfma_f32_16x16x32_bf16 a[124:127], v[60:63], a[172:175], a[124:127]\n"
"buffer_load_dwordx4 v[152:155], v6, s[20:23], s67 offen\n"
"v_mfma_f32_16x16x32_bf16 a[156:159], v[60:63], a[176:179], a[156:159]\n"
"v_perm_b32 v61, v79, v75, s49\n"
"v_perm_b32 v62, v87, v83, s49\n"
"v_mfma_f32_16x16x32_bf16 a[0:3], v[32:35], a[180:183], a[0:3]\n"
"v_perm_b32 v60, v71, v67, s49\n"
"v_perm_b32 v63, v95, v91, s49\n"
"v_mfma_f32_16x16x32_bf16 a[32:35], v[32:35], a[184:187], a[32:35]\n"
"v_mfma_f32_16x16x32_bf16 a[64:67], v[32:35], a[188:191], a[64:67]\n"
"buffer_load_dwordx4 v[156:159], v7, s[20:23], s67 offen\n"
"v_mfma_f32_16x16x32_bf16 a[96:99], v[32:35], a[192:195], a[96:99]\n"
"s_mul_i32 s67, s59, 640\n"
"s_add_u32 s67, s67, 12800\n"
"v_mfma_f32_16x16x32_bf16 a[128:131], v[32:35], a[196:199], a[128:131]\n"
"s_waitcnt vmcnt(12)\n"
"v_perm_b32 v33, v108, v104, s48\n"
"s_waitcnt vmcnt(10)\n"
"v_perm_b32 v34, v116, v112, s48\n"
"v_mfma_f32_16x16x32_bf16 a[4:7], v[36:39], a[180:183], a[4:7]\n"
"v_mfma_f32_16x16x32_bf16 a[36:39], v[36:39], a[184:187], a[36:39]\n"
"buffer_load_dwordx4 v[160:163], v0, s[20:23], s68 offen\n"
"v_mfma_f32_16x16x32_bf16 a[68:71], v[36:39], a[188:191], a[68:71]\n"
"v_mfma_f32_16x16x32_bf16 a[100:103], v[36:39], a[192:195], a[100:103]\n"
"v_mfma_f32_16x16x32_bf16 a[132:135], v[36:39], a[196:199], a[132:135]\n"
"v_perm_b32 v37, v108, v104, s49\n"
"v_perm_b32 v38, v116, v112, s49\n"
"v_mfma_f32_16x16x32_bf16 a[8:11], v[40:43], a[180:183], a[8:11]\n"
"buffer_load_dwordx4 v[164:167], v1, s[20:23], s68 offen\n"
"v_mfma_f32_16x16x32_bf16 a[40:43], v[40:43], a[184:187], a[40:43]\n"
"s_waitcnt vmcnt(10)\n"
"v_perm_b32 v32, v100, v96, s48\n"
"v_perm_b32 v36, v100, v96, s49\n"
"v_mfma_f32_16x16x32_bf16 a[72:75], v[40:43], a[188:191], a[72:75]\n"
"v_perm_b32 v35, v124, v120, s48\n"
"v_perm_b32 v39, v124, v120, s49\n"
"v_mfma_f32_16x16x32_bf16 a[104:107], v[40:43], a[192:195], a[104:107]\n"
"v_mfma_f32_16x16x32_bf16 a[136:139], v[40:43], a[196:199], a[136:139]\n"
"buffer_load_dwordx4 v[168:171], v2, s[20:23], s68 offen\n"
"v_mfma_f32_16x16x32_bf16 a[12:15], v[44:47], a[180:183], a[12:15]\n"
"v_perm_b32 v41, v109, v105, s48\n"
"v_perm_b32 v42, v117, v113, s48\n"
"v_mfma_f32_16x16x32_bf16 a[44:47], v[44:47], a[184:187], a[44:47]\n"
"v_perm_b32 v40, v101, v97, s48\n"
"v_perm_b32 v43, v125, v121, s48\n"
"v_mfma_f32_16x16x32_bf16 a[76:79], v[44:47], a[188:191], a[76:79]\n"
"v_mfma_f32_16x16x32_bf16 a[108:111], v[44:47], a[192:195], a[108:111]\n"
"buffer_load_dwordx4 v[172:175], v3, s[20:23], s68 offen\n"
"v_mfma_f32_16x16x32_bf16 a[140:143], v[44:47], a[196:199], a[140:143]\n"
"s_barrier\n"
"ds_read_b128 a[160:163], v8 offset:20480\n"
"v_perm_b32 v45, v109, v105, s49\n"
"v_mfma_f32_16x16x32_bf16 a[16:19], v[48:51], a[180:183], a[16:19]\n"
"ds_read_b128 a[164:167], v8 offset:24576\n"
"v_perm_b32 v46, v117, v113, s49\n"
"v_mfma_f32_16x16x32_bf16 a[48:51], v[48:51], a[184:187], a[48:51]\n"
"ds_read_b128 a[168:171], v8 offset:28672\n"
"v_perm_b32 v44, v101, v97, s49\n"
"v_mfma_f32_16x16x32_bf16 a[80:83], v[48:51], a[188:191], a[80:83]\n"
"buffer_load_dwordx4 v[176:179], v4, s[20:23], s68 offen\n"
"v_mfma_f32_16x16x32_bf16 a[112:115], v[48:51], a[192:195], a[112:115]\n"
"ds_read_b128 a[172:175], v8 offset:32768\n"
"v_perm_b32 v47, v125, v121, s49\n"
"v_mfma_f32_16x16x32_bf16 a[144:147], v[48:51], a[196:199], a[144:147]\n"
"ds_read_b128 a[176:179], v8 offset:36864\n"
"v_lshl_add_u32 v8, v94, 4, v93\n"
"v_mfma_f32_16x16x32_bf16 a[20:23], v[52:55], a[180:183], a[20:23]\n"
"v_perm_b32 v49, v110, v106, s48\n"
"v_perm_b32 v50, v118, v114, s48\n"
"v_mfma_f32_16x16x32_bf16 a[52:55], v[52:55], a[184:187], a[52:55]\n"
"buffer_load_dwordx4 v[180:183], v5, s[20:23], s68 offen\n"
"v_mfma_f32_16x16x32_bf16 a[84:87], v[52:55], a[188:191], a[84:87]\n"
"v_perm_b32 v48, v102, v98, s48\n"
"v_perm_b32 v51, v126, v122, s48\n"
"v_mfma_f32_16x16x32_bf16 a[116:119], v[52:55], a[192:195], a[116:119]\n"
"v_bfe_u32 v93, v16, 2, 2\n"
"v_xor_b32 v94, %2, v93\n"
"v_mfma_f32_16x16x32_bf16 a[148:151], v[52:55], a[196:199], a[148:151]\n"
"v_perm_b32 v53, v110, v106, s49\n"
"v_perm_b32 v54, v118, v114, s49\n"
"v_mfma_f32_16x16x32_bf16 a[24:27], v[56:59], a[180:183], a[24:27]\n"
"v_perm_b32 v52, v102, v98, s49\n"
"v_perm_b32 v55, v126, v122, s49\n"
"v_mfma_f32_16x16x32_bf16 a[56:59], v[56:59], a[184:187], a[56:59]\n"
"buffer_load_dwordx4 v[184:187], v6, s[20:23], s68 offen\n"
"v_mfma_f32_16x16x32_bf16 a[88:91], v[56:59], a[188:191], a[88:91]\n"
"v_lshlrev_b32 v93, 10, v93\n"
"v_lshl_add_u32 v93, v94, 5, v93\n"
"v_mfma_f32_16x16x32_bf16 a[120:123], v[56:59], a[192:195], a[120:123]\n"
"v_lshl_add_u32 v93, v17, 7, v93\n"
"v_and_b32 v94, 3, v16\n"
"v_mfma_f32_16x16x32_bf16 a[152:155], v[56:59], a[196:199], a[152:155]\n"
"v_perm_b32 v57, v111, v107, s48\n"
"v_perm_b32 v58, v119, v115, s48\n"
"v_mfma_f32_16x16x32_bf16 a[28:31], v[60:63], a[180:183], a[28:31]\n"
"buffer_load_dwordx4 v[188:191], v7, s[20:23], s68 offen\n"
"v_mfma_f32_16x16x32_bf16 a[60:63], v[60:63], a[184:187], a[60:63]\n"
"s_mul_i32 s68, s60, 640\n"
"ds_read_b128 a[180:183], v9 offset:20480\n"
"v_perm_b32 v56, v103, v99, s48\n"
"v_mfma_f32_16x16x32_bf16 a[92:95], v[60:63], a[188:191], a[92:95]\n"
"s_add_u32 s68, s68, 15360\n"
"ds_read_b128 a[184:187], v9 offset:24576\n"
"v_perm_b32 v59, v127, v123, s48\n"
"v_mfma_f32_16x16x32_bf16 a[124:127], v[60:63], a[192:195], a[124:127]\n"
"ds_read_b128 a[188:191], v9 offset:28672\n"
"v_lshl_add_u32 v93, v94, 3, v93\n"
"v_mfma_f32_16x16x32_bf16 a[156:159], v[60:63], a[196:199], a[156:159]\n"
"s_waitcnt lgkmcnt(3)\n"
"buffer_load_dwordx4 v[192:195], v0, s[20:23], s69 offen\n"
"v_mfma_f32_16x16x32_bf16 a[0:3], v[32:35], a[160:163], a[0:3]\n"
"ds_read_b128 a[192:195], v9 offset:32768\n"
"v_perm_b32 v61, v111, v107, s49\n"
"v_mfma_f32_16x16x32_bf16 a[32:35], v[32:35], a[164:167], a[32:35]\n"
"ds_read_b128 a[196:199], v9 offset:36864\n"
"v_perm_b32 v62, v119, v115, s49\n"
"v_mfma_f32_16x16x32_bf16 a[64:67], v[32:35], a[168:171], a[64:67]\n"
"v_perm_b32 v60, v103, v99, s49\n"
"v_lshlrev_b32 v0, 4, v16\n"
"v_mfma_f32_16x16x32_bf16 a[96:99], v[32:35], a[172:175], a[96:99]\n"
"buffer_load_dwordx4 v[196:199], v1, s[20:23], s69 offen\n"
"v_mfma_f32_16x16x32_bf16 a[128:131], v[32:35], a[176:179], a[128:131]\n"
"s_waitcnt vmcnt(14)\n"
"v_perm_b32 v33, v140, v136, s48\n"
"s_waitcnt vmcnt(12)\n"
"v_perm_b32 v34, v148, v144, s48\n"
"v_mfma_f32_16x16x32_bf16 a[4:7], v[36:39], a[160:163], a[4:7]\n"
"s_waitcnt vmcnt(10)\n"
"v_perm_b32 v32, v132, v128, s48\n"
"v_perm_b32 v35, v156, v152, s48\n"
"v_mfma_f32_16x16x32_bf16 a[36:39], v[36:39], a[164:167], a[36:39]\n"
"v_perm_b32 v63, v127, v123, s49\n"
"v_mfma_f32_16x16x32_bf16 a[68:71], v[36:39], a[168:171], a[68:71]\n"
"buffer_load_dwordx4 v[200:203], v2, s[20:23], s69 offen\n"
"v_mfma_f32_16x16x32_bf16 a[100:103], v[36:39], a[172:175], a[100:103]\n"
"v_lshrrev_b32 v2, 1, v16\n"
"v_lshlrev_b32 v2, 10, v2\n"
"v_mfma_f32_16x16x32_bf16 a[132:135], v[36:39], a[176:179], a[132:135]\n"
"v_perm_b32 v37, v140, v136, s49\n"
"v_perm_b32 v38, v148, v144, s49\n"
"v_mfma_f32_16x16x32_bf16 a[8:11], v[40:43], a[160:163], a[8:11]\n"
"v_perm_b32 v36, v132, v128, s49\n"
"v_perm_b32 v39, v156, v152, s49\n"
"v_mfma_f32_16x16x32_bf16 a[40:43], v[40:43], a[164:167], a[40:43]\n"
"buffer_load_dwordx4 v[204:207], v3, s[20:23], s69 offen\n"
"v_mfma_f32_16x16x32_bf16 a[72:75], v[40:43], a[168:171], a[72:75]\n"
"v_and_b32 v3, 1, v16\n"
"v_lshl_add_u32 v2, v3, 7, v2\n"
"v_mfma_f32_16x16x32_bf16 a[104:107], v[40:43], a[172:175], a[104:107]\n"
"v_add_u32 v2, s77, v2\n"
"s_xor_b32 s77, s35, 0\n"
"v_mfma_f32_16x16x32_bf16 a[136:139], v[40:43], a[176:179], a[136:139]\n"
"s_lshl_b32 s77, s77, 15\n"
"v_perm_b32 v41, v141, v137, s48\n"
"v_perm_b32 v42, v149, v145, s48\n"
"v_mfma_f32_16x16x32_bf16 a[12:15], v[44:47], a[160:163], a[12:15]\n"
"buffer_load_dwordx4 v[208:211], v4, s[20:23], s69 offen\n"
"v_mfma_f32_16x16x32_bf16 a[44:47], v[44:47], a[164:167], a[44:47]\n"
"v_perm_b32 v40, v133, v129, s48\n"
"v_perm_b32 v43, v157, v153, s48\n"
"v_mfma_f32_16x16x32_bf16 a[76:79], v[44:47], a[168:171], a[76:79]\n"
"v_mfma_f32_16x16x32_bf16 a[108:111], v[44:47], a[172:175], a[108:111]\n"
"v_mfma_f32_16x16x32_bf16 a[140:143], v[44:47], a[176:179], a[140:143]\n"
"buffer_load_dwordx4 v[212:215], v5, s[20:23], s69 offen\n"
"v_mfma_f32_16x16x32_bf16 a[16:19], v[48:51], a[160:163], a[16:19]\n"
"v_perm_b32 v45, v141, v137, s49\n"
"v_perm_b32 v46, v149, v145, s49\n"
"v_mfma_f32_16x16x32_bf16 a[48:51], v[48:51], a[164:167], a[48:51]\n"
"v_perm_b32 v44, v133, v129, s49\n"
"v_perm_b32 v47, v157, v153, s49\n"
"v_mfma_f32_16x16x32_bf16 a[80:83], v[48:51], a[168:171], a[80:83]\n"
"v_mfma_f32_16x16x32_bf16 a[112:115], v[48:51], a[172:175], a[112:115]\n"
"v_mfma_f32_16x16x32_bf16 a[144:147], v[48:51], a[176:179], a[144:147]\n"
"buffer_load_dwordx4 v[216:219], v6, s[20:23], s69 offen\n"
"v_mfma_f32_16x16x32_bf16 a[20:23], v[52:55], a[160:163], a[20:23]\n"
"v_perm_b32 v49, v142, v138, s48\n"
"v_perm_b32 v50, v150, v146, s48\n"
"v_mfma_f32_16x16x32_bf16 a[52:55], v[52:55], a[164:167], a[52:55]\n"
"v_perm_b32 v48, v134, v130, s48\n"
"v_perm_b32 v51, v158, v154, s48\n"
"v_mfma_f32_16x16x32_bf16 a[84:87], v[52:55], a[168:171], a[84:87]\n"
"v_mfma_f32_16x16x32_bf16 a[116:119], v[52:55], a[172:175], a[116:119]\n"
"buffer_load_dwordx4 v[220:223], v7, s[20:23], s69 offen\n"
"v_mfma_f32_16x16x32_bf16 a[148:151], v[52:55], a[176:179], a[148:151]\n"
"s_lshl_b32 s69, %2, 17\n"
"v_add_u32 v8, s69, v8\n"
"s_lshl_b32 s69, %2, 12\n"
"v_mfma_f32_16x16x32_bf16 a[24:27], v[56:59], a[160:163], a[24:27]\n"
"v_xor_b32 v9, 32, v8\n"
"v_perm_b32 v53, v142, v138, s49\n"
"v_mfma_f32_16x16x32_bf16 a[56:59], v[56:59], a[164:167], a[56:59]\n"
"v_perm_b32 v54, v150, v146, s49\n"
"v_lshrrev_b32 v7, 2, v16\n"
"v_mfma_f32_16x16x32_bf16 a[88:91], v[56:59], a[168:171], a[88:91]\n"
"buffer_load_dwordx4 v12, s[16:19], 0 offen nt lds\n"
"v_mfma_f32_16x16x32_bf16 a[120:123], v[56:59], a[172:175], a[120:123]\n"
"v_and_b32 v6, 7, v7\n"
"v_lshlrev_b32 v6, 5, v6\n"
"v_mfma_f32_16x16x32_bf16 a[152:155], v[56:59], a[176:179], a[152:155]\n"
"v_perm_b32 v57, v143, v139, s48\n"
"v_perm_b32 v58, v151, v147, s48\n"
"v_mfma_f32_16x16x32_bf16 a[28:31], v[60:63], a[160:163], a[28:31]\n"
"ds_read_b128 a[160:163], v10 offset:20480\n"
"v_and_b32 v7, 3, v7\n"
"v_mfma_f32_16x16x32_bf16 a[60:63], v[60:63], a[164:167], a[60:63]\n"
"buffer_load_dwordx4 v13, s[16:19], 0 offen offset:1024 nt lds\n"
"v_mfma_f32_16x16x32_bf16 a[92:95], v[60:63], a[168:171], a[92:95]\n"
"ds_read_b128 a[164:167], v10 offset:24576\n"
"v_lshl_add_u32 v6, v7, 8, v6\n"
"v_mfma_f32_16x16x32_bf16 a[124:127], v[60:63], a[172:175], a[124:127]\n"
"ds_read_b128 a[168:171], v10 offset:28672\n"
"v_lshl_add_u32 v6, v17, 10, v6\n"
"v_mfma_f32_16x16x32_bf16 a[156:159], v[60:63], a[176:179], a[156:159]\n"
"s_waitcnt lgkmcnt(3)\n"
"ds_read_b128 a[172:175], v10 offset:32768\n"
"v_perm_b32 v61, v143, v139, s49\n"
"v_mfma_f32_16x16x32_bf16 a[0:3], v[32:35], a[180:183], a[0:3]\n"
"buffer_load_dwordx4 v12, s[16:19], 0 offen offset:2048 nt lds\n"
"v_mfma_f32_16x16x32_bf16 a[32:35], v[32:35], a[184:187], a[32:35]\n"
"ds_read_b128 a[176:179], v10 offset:36864\n"
"v_xor_b32 v10, 64, v8\n"
"v_mfma_f32_16x16x32_bf16 a[64:67], v[32:35], a[188:191], a[64:67]\n"
"v_perm_b32 v62, v151, v147, s49\n"
"v_and_b32 v7, 3, v16\n"
"v_mfma_f32_16x16x32_bf16 a[96:99], v[32:35], a[192:195], a[96:99]\n"
"v_lshl_add_u32 v6, v7, 3, v6\n"
"v_add_u32 v6, s75, v6\n"
"v_mfma_f32_16x16x32_bf16 a[128:131], v[32:35], a[196:199], a[128:131]\n"
"buffer_load_dwordx4 v13, s[16:19], 0 offen offset:3072 nt lds\n"
"v_mfma_f32_16x16x32_bf16 a[4:7], v[36:39], a[180:183], a[4:7]\n"
"s_add_u32 m0, s75, 16384\n"
"s_waitcnt vmcnt(16)\n"
"v_perm_b32 v33, v172, v168, s48\n"
"s_waitcnt vmcnt(14)\n"
"v_perm_b32 v34, v180, v176, s48\n"
"v_mfma_f32_16x16x32_bf16 a[36:39], v[36:39], a[184:187], a[36:39]\n"
"s_waitcnt vmcnt(10)\n"
"v_perm_b32 v108, v197, v193, s49\n"
"v_perm_b32 v116, v198, v194, s49\n"
"v_mfma_f32_16x16x32_bf16 a[68:71], v[36:39], a[188:191], a[68:71]\n"
"v_perm_b32 v32, v164, v160, s48\n"
"v_add_u32 v7, 16384, v6\n"
"v_mfma_f32_16x16x32_bf16 a[100:103], v[36:39], a[192:195], a[100:103]\n"
"buffer_load_dwordx4 v12, s[16:19], s50 offen nt lds\n"
"v_mfma_f32_16x16x32_bf16 a[132:135], v[36:39], a[196:199], a[132:135]\n"
"v_perm_b32 v37, v172, v168, s49\n"
"v_perm_b32 v38, v180, v176, s49\n"
"v_mfma_f32_16x16x32_bf16 a[8:11], v[40:43], a[180:183], a[8:11]\n"
"v_perm_b32 v36, v164, v160, s49\n"
"v_perm_b32 v35, v188, v184, s48\n"
"v_mfma_f32_16x16x32_bf16 a[40:43], v[40:43], a[184:187], a[40:43]\n"
"v_perm_b32 v39, v188, v184, s49\n"
"v_perm_b32 v52, v134, v130, s49\n"
"v_mfma_f32_16x16x32_bf16 a[72:75], v[40:43], a[188:191], a[72:75]\n"
"buffer_load_dwordx4 v13, s[16:19], s50 offen offset:1024 nt lds\n"
"v_mfma_f32_16x16x32_bf16 a[104:107], v[40:43], a[192:195], a[104:107]\n"
"s_waitcnt vmcnt(10) lgkmcnt(1)\n"
"v_perm_b32 v97, v204, v200, s48\n"
"v_perm_b32 v109, v205, v201, s49\n"
"v_mfma_f32_16x16x32_bf16 a[136:139], v[40:43], a[196:199], a[136:139]\n"
"v_perm_b32 v41, v173, v169, s48\n"
"v_perm_b32 v42, v181, v177, s48\n"
"v_mfma_f32_16x16x32_bf16 a[12:15], v[44:47], a[180:183], a[12:15]\n"
"v_perm_b32 v117, v206, v202, s49\n"
"v_perm_b32 v40, v165, v161, s48\n"
"v_mfma_f32_16x16x32_bf16 a[44:47], v[44:47], a[184:187], a[44:47]\n"
"v_perm_b32 v125, v207, v203, s49\n"
"v_perm_b32 v43, v189, v185, s48\n"
"v_mfma_f32_16x16x32_bf16 a[76:79], v[44:47], a[188:191], a[76:79]\n"
"buffer_load_dwordx4 v12, s[16:19], s50 offen offset:2048 nt lds\n"
"v_mfma_f32_16x16x32_bf16 a[108:111], v[44:47], a[192:195], a[108:111]\n"
"v_perm_b32 v56, v135, v131, s48\n"
"v_perm_b32 v55, v158, v154, s49\n"
"v_mfma_f32_16x16x32_bf16 a[140:143], v[44:47], a[196:199], a[140:143]\n"
"v_perm_b32 v45, v173, v169, s49\n"
"v_perm_b32 v46, v181, v177, s49\n"
"v_mfma_f32_16x16x32_bf16 a[16:19], v[48:51], a[180:183], a[16:19]\n"
"v_perm_b32 v44, v165, v161, s49\n"
"v_perm_b32 v47, v189, v185, s49\n"
"v_mfma_f32_16x16x32_bf16 a[48:51], v[48:51], a[184:187], a[48:51]\n"
"s_waitcnt vmcnt(9) lgkmcnt(0)\n"
"v_perm_b32 v98, v212, v208, s48\n"
"v_perm_b32 v106, v213, v209, s48\n"
"v_mfma_f32_16x16x32_bf16 a[80:83], v[48:51], a[188:191], a[80:83]\n"
"v_perm_b32 v118, v214, v210, s49\n"
"v_perm_b32 v59, v159, v155, s48\n"
"v_mfma_f32_16x16x32_bf16 a[112:115], v[48:51], a[192:195], a[112:115]\n"
"v_perm_b32 v60, v135, v131, s49\n"
"v_perm_b32 v100, v196, v192, s49\n"
"v_mfma_f32_16x16x32_bf16 a[144:147], v[48:51], a[196:199], a[144:147]\n"
"v_perm_b32 v49, v174, v170, s48\n"
"v_perm_b32 v50, v182, v178, s48\n"
"v_mfma_f32_16x16x32_bf16 a[20:23], v[52:55], a[180:183], a[20:23]\n"
"v_perm_b32 v48, v166, v162, s48\n"
"v_perm_b32 v51, v190, v186, s48\n"
"v_mfma_f32_16x16x32_bf16 a[52:55], v[52:55], a[184:187], a[52:55]\n"
"v_perm_b32 v104, v197, v193, s48\n"
"v_perm_b32 v112, v198, v194, s48\n"
"v_mfma_f32_16x16x32_bf16 a[84:87], v[52:55], a[188:191], a[84:87]\n"
"v_perm_b32 v120, v199, v195, s48\n"
"v_perm_b32 v124, v199, v195, s49\n"
"v_mfma_f32_16x16x32_bf16 a[116:119], v[52:55], a[192:195], a[116:119]\n"
"v_perm_b32 v101, v204, v200, s49\n"
"v_perm_b32 v105, v205, v201, s48\n"
"v_mfma_f32_16x16x32_bf16 a[148:151], v[52:55], a[196:199], a[148:151]\n"
"v_perm_b32 v53, v174, v170, s49\n"
"v_perm_b32 v54, v182, v178, s49\n"
"v_mfma_f32_16x16x32_bf16 a[24:27], v[56:59], a[180:183], a[24:27]\n"
"s_waitcnt vmcnt(7)\n"
"v_perm_b32 v96, v196, v192, s48\n"
"v_perm_b32 v99, v220, v216, s48\n"
"v_mfma_f32_16x16x32_bf16 a[56:59], v[56:59], a[184:187], a[56:59]\n"
"v_perm_b32 v107, v221, v217, s48\n"
"v_perm_b32 v119, v222, v218, s49\n"
"v_mfma_f32_16x16x32_bf16 a[88:91], v[56:59], a[188:191], a[88:91]\n"
"v_perm_b32 v52, v166, v162, s49\n"
"v_perm_b32 v55, v190, v186, s49\n"
"v_mfma_f32_16x16x32_bf16 a[120:123], v[56:59], a[192:195], a[120:123]\n"
"v_perm_b32 v113, v206, v202, s48\n"
"v_perm_b32 v121, v207, v203, s48\n"
"v_mfma_f32_16x16x32_bf16 a[152:155], v[56:59], a[196:199], a[152:155]\n"
"v_perm_b32 v57, v175, v171, s48\n"
"v_perm_b32 v58, v183, v179, s48\n"
"v_perm_b32 v56, v167, v163, s48\n"
"v_perm_b32 v59, v191, v187, s48\n"
"v_perm_b32 v102, v212, v208, s49\n"
"v_perm_b32 v110, v213, v209, s49\n"
"v_perm_b32 v114, v214, v210, s48\n"
"v_perm_b32 v122, v215, v211, s48\n"
"v_perm_b32 v126, v215, v211, s49\n"
"v_perm_b32 v103, v220, v216, s49\n"
"v_perm_b32 v111, v221, v217, s49\n"
"v_perm_b32 v115, v222, v218, s48\n"
"v_perm_b32 v123, v223, v219, s48\n"
"v_perm_b32 v127, v223, v219, s49\n"
"v_perm_b32 v63, v159, v155, s49\n"
"v_add_u32 v92, s69, v93\n"
"v_xor_b32 v93, 4096, v92\n"
"v_mfma_f32_16x16x32_bf16 a[28:31], v[60:63], a[180:183], a[28:31]\n"
"ds_read_b128 a[180:183], v11 offset:20480\n"
"v_xor_b32 v94, 8192, v92\n"
"v_mfma_f32_16x16x32_bf16 a[60:63], v[60:63], a[184:187], a[60:63]\n"
"ds_read_b128 a[184:187], v11 offset:24576\n"
"v_xor_b32 v95, 12288, v92\n"
"v_mfma_f32_16x16x32_bf16 a[92:95], v[60:63], a[188:191], a[92:95]\n"
"ds_read_b128 a[188:191], v11 offset:28672\n"
"v_lshrrev_b32 v188, 5, v16\n"
"v_mfma_f32_16x16x32_bf16 a[124:127], v[60:63], a[192:195], a[124:127]\n"
"ds_read_b128 a[192:195], v11 offset:32768\n"
"v_lshl_add_u32 v1, v188, 24, v0\n"
"v_mfma_f32_16x16x32_bf16 a[156:159], v[60:63], a[196:199], a[156:159]\n"
"ds_read_b128 a[196:199], v11 offset:36864\n"
"v_xor_b32 v11, 96, v8\n"
"v_mfma_f32_16x16x32_bf16 a[0:3], v[32:35], a[160:163], a[0:3]\n"
"v_perm_b32 v61, v175, v171, s49\n"
"v_perm_b32 v62, v183, v179, s49\n"
"v_mfma_f32_16x16x32_bf16 a[32:35], v[32:35], a[164:167], a[32:35]\n"
"v_perm_b32 v60, v167, v163, s49\n"
"v_perm_b32 v63, v191, v187, s49\n"
"v_mfma_f32_16x16x32_bf16 a[64:67], v[32:35], a[168:171], a[64:67]\n"
"v_xor_b32 v191, 7, v16\n"
"v_lshlrev_b32 v191, 20, v191\n"
"v_mfma_f32_16x16x32_bf16 a[96:99], v[32:35], a[172:175], a[96:99]\n"
"v_lshrrev_b32 v190, 3, v16\n"
"v_lshlrev_b32 v190, 20, v190\n"
"v_mfma_f32_16x16x32_bf16 a[128:131], v[32:35], a[176:179], a[128:131]\n"
"v_lshl_add_u32 v190, v16, 2, v190\n"
"v_lshlrev_b32 v188, 3, v188\n"
"v_mfma_f32_16x16x32_bf16 a[4:7], v[36:39], a[160:163], a[4:7]\n"
"v_and_b32 v189, 31, v16\n"
"v_lshl_add_u32 v188, v189, 4, v188\n"
"v_mfma_f32_16x16x32_bf16 a[36:39], v[36:39], a[164:167], a[36:39]\n"
"v_lshlrev_b32 v189, 11, v18\n"
"v_lshl_add_u32 v189, v17, 3, v189\n"
"v_mfma_f32_16x16x32_bf16 a[68:71], v[36:39], a[168:171], a[68:71]\n"
"v_mfma_f32_16x16x32_bf16 a[100:103], v[36:39], a[172:175], a[100:103]\n"
"v_mfma_f32_16x16x32_bf16 a[132:135], v[36:39], a[176:179], a[132:135]\n"
"v_mfma_f32_16x16x32_bf16 a[8:11], v[40:43], a[160:163], a[8:11]\n"
"v_mfma_f32_16x16x32_bf16 a[40:43], v[40:43], a[164:167], a[40:43]\n"
"v_mfma_f32_16x16x32_bf16 a[72:75], v[40:43], a[168:171], a[72:75]\n"
"v_mfma_f32_16x16x32_bf16 a[104:107], v[40:43], a[172:175], a[104:107]\n"
"v_mfma_f32_16x16x32_bf16 a[136:139], v[40:43], a[176:179], a[136:139]\n"
"v_mfma_f32_16x16x32_bf16 a[12:15], v[44:47], a[160:163], a[12:15]\n"
"v_mfma_f32_16x16x32_bf16 a[44:47], v[44:47], a[164:167], a[44:47]\n"
"v_mfma_f32_16x16x32_bf16 a[76:79], v[44:47], a[168:171], a[76:79]\n"
"v_mfma_f32_16x16x32_bf16 a[108:111], v[44:47], a[172:175], a[108:111]\n"
"v_mfma_f32_16x16x32_bf16 a[140:143], v[44:47], a[176:179], a[140:143]\n"
"v_mfma_f32_16x16x32_bf16 a[16:19], v[48:51], a[160:163], a[16:19]\n"
"v_mfma_f32_16x16x32_bf16 a[48:51], v[48:51], a[164:167], a[48:51]\n"
"v_mfma_f32_16x16x32_bf16 a[80:83], v[48:51], a[168:171], a[80:83]\n"
"v_mfma_f32_16x16x32_bf16 a[112:115], v[48:51], a[172:175], a[112:115]\n"
"v_mfma_f32_16x16x32_bf16 a[144:147], v[48:51], a[176:179], a[144:147]\n"
"v_mfma_f32_16x16x32_bf16 a[20:23], v[52:55], a[160:163], a[20:23]\n"
"v_mfma_f32_16x16x32_bf16 a[52:55], v[52:55], a[164:167], a[52:55]\n"
"v_mfma_f32_16x16x32_bf16 a[84:87], v[52:55], a[168:171], a[84:87]\n"
"v_mfma_f32_16x16x32_bf16 a[116:119], v[52:55], a[172:175], a[116:119]\n"
"v_mfma_f32_16x16x32_bf16 a[148:151], v[52:55], a[176:179], a[148:151]\n"
"s_waitcnt lgkmcnt(0)\n"
"s_barrier\n"
"v_mfma_f32_16x16x32_bf16 a[24:27], v[56:59], a[160:163], a[24:27]\n"
"v_mfma_f32_16x16x32_bf16 a[56:59], v[56:59], a[164:167], a[56:59]\n"
"v_mfma_f32_16x16x32_bf16 a[88:91], v[56:59], a[168:171], a[88:91]\n"
"v_mfma_f32_16x16x32_bf16 a[120:123], v[56:59], a[172:175], a[120:123]\n"
"v_mfma_f32_16x16x32_bf16 a[152:155], v[56:59], a[176:179], a[152:155]\n"
"v_mfma_f32_16x16x32_bf16 a[28:31], v[60:63], a[160:163], a[28:31]\n"
"v_mfma_f32_16x16x32_bf16 a[60:63], v[60:63], a[164:167], a[60:63]\n"
"buffer_load_dwordx4 v13, s[16:19], s50 offen offset:3072 nt lds\n"
"v_mfma_f32_16x16x32_bf16 a[92:95], v[60:63], a[168:171], a[92:95]\n"
"s_add_u32 m0, s75, 32768\n"
"v_mfma_f32_16x16x32_bf16 a[124:127], v[60:63], a[172:175], a[124:127]\n"
"v_mfma_f32_16x16x32_bf16 a[156:159], v[60:63], a[176:179], a[156:159]\n"
"v_mfma_f32_16x16x32_bf16 a[0:3], v[96:99], a[180:183], a[0:3]\n"
"buffer_load_dwordx4 v12, s[16:19], s51 offen nt lds\n"
"v_mfma_f32_16x16x32_bf16 a[4:7], v[100:103], a[180:183], a[4:7]\n"
"v_mfma_f32_16x16x32_bf16 a[8:11], v[104:107], a[180:183], a[8:11]\n"
"v_mfma_f32_16x16x32_bf16 a[12:15], v[108:111], a[180:183], a[12:15]\n"
"v_mfma_f32_16x16x32_bf16 a[16:19], v[112:115], a[180:183], a[16:19]\n"
"buffer_load_dwordx4 v13, s[16:19], s51 offen offset:1024 nt lds\n"
"v_mfma_f32_16x16x32_bf16 a[20:23], v[116:119], a[180:183], a[20:23]\n"
"v_mfma_f32_16x16x32_bf16 a[24:27], v[120:123], a[180:183], a[24:27]\n"
"v_accvgpr_read_b32 v21, a0\n"
"v_mfma_f32_16x16x32_bf16 a[28:31], v[124:127], a[180:183], a[28:31]\n"
"v_accvgpr_read_b32 v22, a4\n"
"v_cvt_pk_bf16_f32 v32, v21, v22\n"
"v_mfma_f32_16x16x32_bf16 a[32:35], v[96:99], a[184:187], a[32:35]\n"
"v_accvgpr_read_b32 v21, a1\n"
"v_accvgpr_read_b32 v22, a5\n"
"v_mfma_f32_16x16x32_bf16 a[36:39], v[100:103], a[184:187], a[36:39]\n"
"buffer_load_dwordx4 v12, s[16:19], s51 offen offset:2048 nt lds\n"
"v_mfma_f32_16x16x32_bf16 a[40:43], v[104:107], a[184:187], a[40:43]\n"
"v_cvt_pk_bf16_f32 v36, v21, v22\n"
"v_accvgpr_read_b32 v21, a2\n"
"v_mfma_f32_16x16x32_bf16 a[44:47], v[108:111], a[184:187], a[44:47]\n"
"v_accvgpr_read_b32 v22, a6\n"
"v_accvgpr_read_b32 v23, a8\n"
"v_mfma_f32_16x16x32_bf16 a[48:51], v[112:115], a[184:187], a[48:51]\n"
"v_accvgpr_read_b32 v24, a12\n"
"v_cvt_pk_bf16_f32 v40, v21, v22\n"
"v_mfma_f32_16x16x32_bf16 a[52:55], v[116:119], a[184:187], a[52:55]\n"
"buffer_load_dwordx4 v13, s[16:19], s51 offen offset:3072 nt lds\n"
"v_mfma_f32_16x16x32_bf16 a[56:59], v[120:123], a[184:187], a[56:59]\n"
"s_add_u32 m0, s75, 49152\n"
"v_cvt_pk_bf16_f32 v33, v23, v24\n"
"v_accvgpr_read_b32 v21, a3\n"
"v_mfma_f32_16x16x32_bf16 a[60:63], v[124:127], a[184:187], a[60:63]\n"
"v_accvgpr_read_b32 v23, a9\n"
"v_accvgpr_read_b32 v24, a13\n"
"v_mfma_f32_16x16x32_bf16 a[64:67], v[96:99], a[188:191], a[64:67]\n"
"v_cvt_pk_bf16_f32 v37, v23, v24\n"
"v_accvgpr_read_b32 v22, a7\n"
"v_mfma_f32_16x16x32_bf16 a[68:71], v[100:103], a[188:191], a[68:71]\n"
"buffer_load_dwordx4 v12, s[16:19], s52 offen nt lds\n"
"v_mfma_f32_16x16x32_bf16 a[72:75], v[104:107], a[188:191], a[72:75]\n"
"v_accvgpr_read_b32 v23, a10\n"
"v_accvgpr_read_b32 v24, a14\n"
"v_mfma_f32_16x16x32_bf16 a[76:79], v[108:111], a[188:191], a[76:79]\n"
"v_accvgpr_read_b32 v25, a16\n"
"v_accvgpr_read_b32 v26, a20\n"
"v_mfma_f32_16x16x32_bf16 a[80:83], v[112:115], a[188:191], a[80:83]\n"
"v_cvt_pk_bf16_f32 v41, v23, v24\n"
"v_cvt_pk_bf16_f32 v34, v25, v26\n"
"v_mfma_f32_16x16x32_bf16 a[84:87], v[116:119], a[188:191], a[84:87]\n"
"buffer_load_dwordx4 v13, s[16:19], s52 offen offset:1024 nt lds\n"
"v_mfma_f32_16x16x32_bf16 a[88:91], v[120:123], a[188:191], a[88:91]\n"
"v_accvgpr_read_b32 v23, a11\n"
"v_accvgpr_read_b32 v25, a17\n"
"v_mfma_f32_16x16x32_bf16 a[92:95], v[124:127], a[188:191], a[92:95]\n"
"v_accvgpr_read_b32 v26, a21\n"
"v_cvt_pk_bf16_f32 v38, v25, v26\n"
"v_mfma_f32_16x16x32_bf16 a[96:99], v[96:99], a[192:195], a[96:99]\n"
"v_accvgpr_read_b32 v24, a15\n"
"v_accvgpr_read_b32 v25, a18\n"
"v_mfma_f32_16x16x32_bf16 a[100:103], v[100:103], a[192:195], a[100:103]\n"
"v_accvgpr_read_b32 v26, a22\n"
"v_accvgpr_read_b32 v27, a24\n"
"v_mfma_f32_16x16x32_bf16 a[104:107], v[104:107], a[192:195], a[104:107]\n"
"v_accvgpr_read_b32 v28, a28\n"
"v_cvt_pk_bf16_f32 v42, v25, v26\n"
"v_mfma_f32_16x16x32_bf16 a[108:111], v[108:111], a[192:195], a[108:111]\n"
"v_cvt_pk_bf16_f32 v35, v27, v28\n"
"v_accvgpr_read_b32 v25, a19\n"
"v_mfma_f32_16x16x32_bf16 a[112:115], v[112:115], a[192:195], a[112:115]\n"
"v_accvgpr_read_b32 v27, a25\n"
"v_accvgpr_read_b32 v28, a29\n"
"v_mfma_f32_16x16x32_bf16 a[116:119], v[116:119], a[192:195], a[116:119]\n"
"v_cvt_pk_bf16_f32 v39, v27, v28\n"
"v_accvgpr_read_b32 v26, a23\n"
"v_mfma_f32_16x16x32_bf16 a[120:123], v[120:123], a[192:195], a[120:123]\n"
"v_accvgpr_read_b32 v27, a26\n"
"v_accvgpr_read_b32 v28, a30\n"
"v_mfma_f32_16x16x32_bf16 a[124:127], v[124:127], a[192:195], a[124:127]\n"
"v_cvt_pk_bf16_f32 v44, v21, v22\n"
"v_cvt_pk_bf16_f32 v43, v27, v28\n"
"v_mfma_f32_16x16x32_bf16 a[128:131], v[96:99], a[196:199], a[128:131]\n"
"v_accvgpr_read_b32 v21, a32\n"
"v_accvgpr_read_b32 v22, a36\n"
"v_mfma_f32_16x16x32_bf16 a[132:135], v[100:103], a[196:199], a[132:135]\n"
"v_accvgpr_read_b32 v27, a27\n"
"v_cvt_pk_bf16_f32 v48, v21, v22\n"
"v_mfma_f32_16x16x32_bf16 a[136:139], v[104:107], a[196:199], a[136:139]\n"
"v_accvgpr_read_b32 v21, a33\n"
"v_accvgpr_read_b32 v22, a37\n"
"v_mfma_f32_16x16x32_bf16 a[140:143], v[108:111], a[196:199], a[140:143]\n"
"v_accvgpr_read_b32 v28, a31\n"
"v_cvt_pk_bf16_f32 v52, v21, v22\n"
"v_mfma_f32_16x16x32_bf16 a[144:147], v[112:115], a[196:199], a[144:147]\n"
"v_cvt_pk_bf16_f32 v45, v23, v24\n"
"v_accvgpr_read_b32 v21, a34\n"
"v_mfma_f32_16x16x32_bf16 a[148:151], v[116:119], a[196:199], a[148:151]\n"
"v_accvgpr_read_b32 v22, a38\n"
"v_accvgpr_read_b32 v23, a40\n"
"v_mfma_f32_16x16x32_bf16 a[152:155], v[120:123], a[196:199], a[152:155]\n"
"v_accvgpr_read_b32 v24, a44\n"
"v_cvt_pk_bf16_f32 v56, v21, v22\n"
"v_mfma_f32_16x16x32_bf16 a[156:159], v[124:127], a[196:199], a[156:159]\n"
"v_cvt_pk_bf16_f32 v49, v23, v24\n"
"v_accvgpr_read_b32 v21, a35\n"
"v_mfma_f32_16x16x32_bf16 v[128:131], a[200:203], v[32:35], 0\n"
"v_accvgpr_read_b32 v23, a41\n"
"v_accvgpr_read_b32 v24, a45\n"
"v_mfma_f32_16x16x32_bf16 v[132:135], a[216:219], v[32:35], 0\n"
"v_cvt_pk_bf16_f32 v53, v23, v24\n"
"v_accvgpr_read_b32 v22, a39\n"
"v_mfma_f32_16x16x32_bf16 v[136:139], a[232:235], v[32:35], 0\n"
"v_cvt_pk_bf16_f32 v46, v25, v26\n"
"v_accvgpr_read_b32 v23, a42\n"
"v_mfma_f32_16x16x32_bf16 v[140:143], a[248:251], v[32:35], 0\n"
"v_accvgpr_read_b32 v24, a46\n"
"v_accvgpr_read_b32 v25, a48\n"
"v_mfma_f32_16x16x32_bf16 v[144:147], v[232:235], v[32:35], 0\n"
"v_accvgpr_read_b32 v26, a52\n"
"v_cvt_pk_bf16_f32 v57, v23, v24\n"
"v_mfma_f32_16x16x32_bf16 v[128:131], a[204:207], v[36:39], v[128:131]\n"
"v_cvt_pk_bf16_f32 v50, v25, v26\n"
"v_accvgpr_read_b32 v23, a43\n"
"v_mfma_f32_16x16x32_bf16 v[132:135], a[220:223], v[36:39], v[132:135]\n"
"v_accvgpr_read_b32 v25, a49\n"
"v_accvgpr_read_b32 v26, a53\n"
"v_mfma_f32_16x16x32_bf16 v[136:139], a[236:239], v[36:39], v[136:139]\n"
"v_cvt_pk_bf16_f32 v54, v25, v26\n"
"v_accvgpr_read_b32 v24, a47\n"
"v_mfma_f32_16x16x32_bf16 v[140:143], a[252:255], v[36:39], v[140:143]\n"
"v_cvt_pk_bf16_f32 v47, v27, v28\n"
"v_accvgpr_read_b32 v25, a50\n"
"v_mfma_f32_16x16x32_bf16 v[144:147], v[236:239], v[36:39], v[144:147]\n"
"v_accvgpr_read_b32 v26, a54\n"
"v_accvgpr_read_b32 v27, a56\n"
"v_mfma_f32_16x16x32_bf16 v[128:131], a[208:211], v[40:43], v[128:131]\n"
"v_accvgpr_read_b32 v28, a60\n"
"v_cvt_pk_bf16_f32 v58, v25, v26\n"
"v_mfma_f32_16x16x32_bf16 v[132:135], a[224:227], v[40:43], v[132:135]\n"
"v_cvt_pk_bf16_f32 v51, v27, v28\n"
"v_accvgpr_read_b32 v25, a51\n"
"v_mfma_f32_16x16x32_bf16 v[136:139], a[240:243], v[40:43], v[136:139]\n"
"v_accvgpr_read_b32 v27, a57\n"
"v_accvgpr_read_b32 v28, a61\n"
"v_mfma_f32_16x16x32_bf16 v[140:143], v[224:227], v[40:43], v[140:143]\n"
"v_cvt_pk_bf16_f32 v55, v27, v28\n"
"v_accvgpr_read_b32 v26, a55\n"
"v_mfma_f32_16x16x32_bf16 v[144:147], v[240:243], v[40:43], v[144:147]\n"
"v_accvgpr_read_b32 v27, a58\n"
"v_accvgpr_read_b32 v28, a62\n"
"v_mfma_f32_16x16x32_bf16 v[128:131], a[212:215], v[44:47], v[128:131]\n"
"v_cvt_pk_bf16_f32 v59, v27, v28\n"
"v_accvgpr_read_b32 v27, a59\n"
"v_mfma_f32_16x16x32_bf16 v[132:135], a[228:231], v[44:47], v[132:135]\n"
"v_accvgpr_read_b32 v28, a63\n"
"v_cvt_pk_bf16_f32 v61, v23, v24\n"
"v_mfma_f32_16x16x32_bf16 v[136:139], a[244:247], v[44:47], v[136:139]\n"
"v_accvgpr_read_b32 v23, a72\n"
"v_accvgpr_read_b32 v24, a76\n"
"v_mfma_f32_16x16x32_bf16 v[140:143], v[228:231], v[44:47], v[140:143]\n"
"v_max3_f32 v19, v128, v129, v130\n"
"v_cvt_pk_bf16_f32 v33, v23, v24\n"
"v_mfma_f32_16x16x32_bf16 v[144:147], v[244:247], v[44:47], v[144:147]\n"
"v_max3_f32 v20, v131, v132, v133\n"
"v_max3_f32 v19, v19, v134, v135\n"
"v_mfma_f32_16x16x32_bf16 v[148:151], a[200:203], v[48:51], 0\n"
"v_max3_f32 v20, v20, v136, v137\n"
"v_max3_f32 v19, v19, v138, v139\n"
"v_mfma_f32_16x16x32_bf16 v[152:155], a[216:219], v[48:51], 0\n"
"v_max3_f32 v20, v20, v140, v141\n"
"v_max3_f32 v19, v19, v142, v143\n"
"v_mfma_f32_16x16x32_bf16 v[156:159], a[232:235], v[48:51], 0\n"
"v_max3_f32 v20, v20, v144, v145\n"
"v_max3_f32 v19, v19, v146, v147\n"
"v_mfma_f32_16x16x32_bf16 v[160:163], a[248:251], v[48:51], 0\n"
"v_max_f32 v19, v19, v20\n"
"v_mov_b32 v20, v19\n"
"v_mfma_f32_16x16x32_bf16 v[164:167], v[232:235], v[48:51], 0\n"
"v_accvgpr_read_b32 v23, a73\n"
"v_accvgpr_read_b32 v24, a77\n"
"v_mfma_f32_16x16x32_bf16 v[148:151], a[204:207], v[52:55], v[148:151]\n"
"v_permlane16_swap_b32 v19, v20\n"
"v_mfma_f32_16x16x32_bf16 v[152:155], a[220:223], v[52:55], v[152:155]\n"
"v_max_f32 v19, v19, v20\n"
"v_mov_b32 v20, v19\n"
"v_mfma_f32_16x16x32_bf16 v[156:159], a[236:239], v[52:55], v[156:159]\n"
"v_cvt_pk_bf16_f32 v37, v23, v24\n"
"v_accvgpr_read_b32 v23, a74\n"
"v_mfma_f32_16x16x32_bf16 v[160:163], a[252:255], v[52:55], v[160:163]\n"
"v_permlane32_swap_b32 v19, v20\n"
"v_mfma_f32_16x16x32_bf16 v[164:167], v[236:239], v[52:55], v[164:167]\n"
"v_max_f32 v19, v19, v20\n"
"v_mul_f32 v20, s74, v19\n"
"v_mfma_f32_16x16x32_bf16 v[148:151], a[208:211], v[56:59], v[148:151]\n"
"v_pk_fma_f32 v[128:129], v[128:129], s[74:75], v[20:21] op_sel_hi:[1,0,0] neg_lo:[0,0,1] neg_hi:[0,0,1]\n"
"v_pk_fma_f32 v[130:131], v[130:131], s[74:75], v[20:21] op_sel_hi:[1,0,0] neg_lo:[0,0,1] neg_hi:[0,0,1]\n"
"v_mfma_f32_16x16x32_bf16 v[152:155], a[224:227], v[56:59], v[152:155]\n"
"v_pk_fma_f32 v[132:133], v[132:133], s[74:75], v[20:21] op_sel_hi:[1,0,0] neg_lo:[0,0,1] neg_hi:[0,0,1]\n"
"v_pk_fma_f32 v[134:135], v[134:135], s[74:75], v[20:21] op_sel_hi:[1,0,0] neg_lo:[0,0,1] neg_hi:[0,0,1]\n"
"v_mfma_f32_16x16x32_bf16 v[156:159], a[240:243], v[56:59], v[156:159]\n"
"v_pk_fma_f32 v[136:137], v[136:137], s[74:75], v[20:21] op_sel_hi:[1,0,0] neg_lo:[0,0,1] neg_hi:[0,0,1]\n"
"v_pk_fma_f32 v[138:139], v[138:139], s[74:75], v[20:21] op_sel_hi:[1,0,0] neg_lo:[0,0,1] neg_hi:[0,0,1]\n"
"v_mfma_f32_16x16x32_bf16 v[160:163], v[224:227], v[56:59], v[160:163]\n"
"v_pk_fma_f32 v[140:141], v[140:141], s[74:75], v[20:21] op_sel_hi:[1,0,0] neg_lo:[0,0,1] neg_hi:[0,0,1]\n"
"v_pk_fma_f32 v[142:143], v[142:143], s[74:75], v[20:21] op_sel_hi:[1,0,0] neg_lo:[0,0,1] neg_hi:[0,0,1]\n"
"v_mfma_f32_16x16x32_bf16 v[164:167], v[240:243], v[56:59], v[164:167]\n"
"v_pk_fma_f32 v[144:145], v[144:145], s[74:75], v[20:21] op_sel_hi:[1,0,0] neg_lo:[0,0,1] neg_hi:[0,0,1]\n"
"v_pk_fma_f32 v[146:147], v[146:147], s[74:75], v[20:21] op_sel_hi:[1,0,0] neg_lo:[0,0,1] neg_hi:[0,0,1]\n"
"v_exp_f32 v128, v128\n"
"v_exp_f32 v129, v129\n"
"v_exp_f32 v130, v130\n"
"v_exp_f32 v131, v131\n"
"v_exp_f32 v132, v132\n"
"v_exp_f32 v133, v133\n"
"v_exp_f32 v134, v134\n"
"v_exp_f32 v135, v135\n"
"v_exp_f32 v136, v136\n"
"v_exp_f32 v137, v137\n"
"v_exp_f32 v138, v138\n"
"v_exp_f32 v139, v139\n"
"v_exp_f32 v140, v140\n"
"v_exp_f32 v141, v141\n"
"v_exp_f32 v142, v142\n"
"v_exp_f32 v143, v143\n"
"v_exp_f32 v144, v144\n"
"v_exp_f32 v145, v145\n"
"buffer_load_dwordx4 v12, s[16:19], s52 offen offset:2048 nt lds\n"
"v_exp_f32 v146, v146\n"
"v_exp_f32 v147, v147\n"
"v_pk_add_f32 v[168:169], v[128:129], v[130:131]\n"
"v_pk_add_f32 v[170:171], v[132:133], v[134:135]\n"
"v_pk_add_f32 v[168:169], v[168:169], v[136:137]\n"
"v_pk_add_f32 v[170:171], v[170:171], v[138:139]\n"
"v_pk_add_f32 v[168:169], v[168:169], v[140:141]\n"
"v_pk_add_f32 v[170:171], v[170:171], v[142:143]\n"
"v_pk_add_f32 v[168:169], v[168:169], v[144:145]\n"
"v_pk_add_f32 v[170:171], v[170:171], v[146:147]\n"
"buffer_load_dwordx4 v13, s[16:19], s52 offen offset:3072 nt lds\n"
"s_add_u32 m0, s75, 65536\n"
"v_pk_add_f32 v[168:169], v[168:169], v[170:171]\n"
"v_add_f32 v30, v168, v169\n"
"v_mov_b32 v29, v30\n"
"v_accvgpr_read_b32 v24, a78\n"
"v_cvt_pk_bf16_f32 v41, v23, v24\n"
"v_permlane16_swap_b32 v30, v29\n"
"v_add_f32 v30, v30, v29\n"
"v_mov_b32 v29, v30\n"
"v_accvgpr_read_b32 v23, a75\n"
"v_accvgpr_read_b32 v24, a79\n"
"buffer_load_dwordx4 v12, s[16:19], s53 offen nt lds\n"
"v_permlane32_swap_b32 v30, v29\n"
"v_add_f32 v30, v30, v29\n"
"v_rcp_f32 v30, v30\n"
"v_cvt_pk_bf16_f32 v45, v23, v24\n"
"v_pk_mul_f32 v[128:129], v[128:129], v[30:31] op_sel_hi:[1,0]\n"
"v_pk_mul_f32 v[130:131], v[130:131], v[30:31] op_sel_hi:[1,0]\n"
"v_pk_mul_f32 v[132:133], v[132:133], v[30:31] op_sel_hi:[1,0]\n"
"v_pk_mul_f32 v[134:135], v[134:135], v[30:31] op_sel_hi:[1,0]\n"
"v_pk_mul_f32 v[136:137], v[136:137], v[30:31] op_sel_hi:[1,0]\n"
"v_pk_mul_f32 v[138:139], v[138:139], v[30:31] op_sel_hi:[1,0]\n"
"v_pk_mul_f32 v[140:141], v[140:141], v[30:31] op_sel_hi:[1,0]\n"
"v_pk_mul_f32 v[142:143], v[142:143], v[30:31] op_sel_hi:[1,0]\n"
"v_pk_mul_f32 v[144:145], v[144:145], v[30:31] op_sel_hi:[1,0]\n"
"buffer_load_dwordx4 v13, s[16:19], s53 offen offset:1024 nt lds\n"
"v_pk_mul_f32 v[146:147], v[146:147], v[30:31] op_sel_hi:[1,0]\n"
"v_cvt_pk_bf16_f32 v60, v21, v22\n"
"v_accvgpr_read_b32 v23, a104\n"
"v_accvgpr_read_b32 v24, a108\n"
"v_accvgpr_read_b32 v21, a64\n"
"v_accvgpr_read_b32 v22, a68\n"
"v_cvt_pk_bf16_f32 v49, v23, v24\n"
"v_cvt_pk_bf16_f32 v32, v21, v22\n"
"v_accvgpr_read_b32 v23, a105\n"
"v_accvgpr_read_b32 v24, a109\n"
"v_accvgpr_read_b32 v21, a65\n"
"v_accvgpr_read_b32 v22, a69\n"
"v_cvt_pk_bf16_f32 v53, v23, v24\n"
"buffer_load_dwordx4 v12, s[16:19], s53 offen offset:2048 nt lds\n"
"v_mov_b32 v12, -1\n"
"v_cvt_pk_bf16_f32 v36, v21, v22\n"
"v_accvgpr_read_b32 v23, a106\n"
"v_accvgpr_read_b32 v24, a110\n"
"v_accvgpr_read_b32 v21, a66\n"
"v_accvgpr_read_b32 v22, a70\n"
"v_cvt_pk_bf16_f32 v57, v23, v24\n"
"v_cvt_pk_bf16_f32 v40, v21, v22\n"
"v_accvgpr_read_b32 v23, a107\n"
"v_accvgpr_read_b32 v24, a111\n"
"v_accvgpr_read_b32 v21, a67\n"
"v_accvgpr_read_b32 v22, a71\n"
"buffer_load_dwordx4 v13, s[16:19], s53 offen offset:3072 nt lds\n"
"s_add_u32 m0, s69, 0\n"
"v_mov_b32 v13, -1\n"
"v_cvt_pk_bf16_f32 v44, v21, v22\n"
"v_accvgpr_read_b32 v21, a96\n"
"v_accvgpr_read_b32 v22, a100\n"
"v_cvt_pk_bf16_f32 v48, v21, v22\n"
"v_accvgpr_read_b32 v21, a97\n"
"v_accvgpr_read_b32 v22, a101\n"
"v_cvt_pk_bf16_f32 v52, v21, v22\n"
"v_accvgpr_read_b32 v21, a98\n"
"v_accvgpr_read_b32 v22, a102\n"
"v_cvt_pk_bf16_f32 v56, v21, v22\n"
"v_accvgpr_read_b32 v21, a99\n"
"buffer_load_dwordx4 v8, s[24:27], s54 offen lds\n"
"v_accvgpr_read_b32 v22, a103\n"
"v_cvt_pk_bf16_f32 v62, v25, v26\n"
"v_accvgpr_read_b32 v25, a80\n"
"v_accvgpr_read_b32 v26, a84\n"
"v_cvt_pk_bf16_f32 v63, v27, v28\n"
"v_cvt_pk_bf16_f32 v34, v25, v26\n"
"v_accvgpr_read_b32 v27, a88\n"
"v_mfma_f32_16x16x32_bf16 v[148:151], a[212:215], v[60:63], v[148:151]\n"
"v_accvgpr_read_b32 v28, a92\n"
"v_accvgpr_read_b32 v25, a81\n"
"v_mfma_f32_16x16x32_bf16 v[152:155], a[228:231], v[60:63], v[152:155]\n"
"v_accvgpr_read_b32 v26, a85\n"
"v_cvt_pk_bf16_f32 v35, v27, v28\n"
"v_mfma_f32_16x16x32_bf16 v[156:159], a[244:247], v[60:63], v[156:159]\n"
"v_cvt_pk_bf16_f32 v38, v25, v26\n"
"v_accvgpr_read_b32 v27, a89\n"
"v_mfma_f32_16x16x32_bf16 v[160:163], v[228:231], v[60:63], v[160:163]\n"
"v_max3_f32 v19, v148, v149, v150\n"
"v_accvgpr_read_b32 v28, a93\n"
"v_mfma_f32_16x16x32_bf16 v[164:167], v[244:247], v[60:63], v[164:167]\n"
"v_max3_f32 v20, v151, v152, v153\n"
"v_max3_f32 v19, v19, v154, v155\n"
"v_max3_f32 v20, v20, v156, v157\n"
"v_max3_f32 v19, v19, v158, v159\n"
"v_cvt_pk_bf16_f32 v61, v23, v24\n"
"v_max3_f32 v20, v20, v160, v161\n"
"v_max3_f32 v19, v19, v162, v163\n"
"v_accvgpr_read_b32 v23, a136\n"
"v_max3_f32 v20, v20, v164, v165\n"
"v_max3_f32 v19, v19, v166, v167\n"
"v_max_f32 v19, v19, v20\n"
"v_mov_b32 v20, v19\n"
"v_accvgpr_read_b32 v24, a140\n"
"v_cvt_pk_bf16_f32 v60, v21, v22\n"
"v_permlane16_swap_b32 v19, v20\n"
"v_max_f32 v19, v19, v20\n"
"v_mov_b32 v20, v19\n"
"v_accvgpr_read_b32 v21, a128\n"
"v_accvgpr_read_b32 v22, a132\n"
"v_permlane32_swap_b32 v19, v20\n"
"v_max_f32 v19, v19, v20\n"
"v_mul_f32 v20, s74, v19\n"
"v_pk_fma_f32 v[148:149], v[148:149], s[74:75], v[20:21] op_sel_hi:[1,0,0] neg_lo:[0,0,1] neg_hi:[0,0,1]\n"
"v_pk_fma_f32 v[150:151], v[150:151], s[74:75], v[20:21] op_sel_hi:[1,0,0] neg_lo:[0,0,1] neg_hi:[0,0,1]\n"
"v_pk_fma_f32 v[152:153], v[152:153], s[74:75], v[20:21] op_sel_hi:[1,0,0] neg_lo:[0,0,1] neg_hi:[0,0,1]\n"
"v_pk_fma_f32 v[154:155], v[154:155], s[74:75], v[20:21] op_sel_hi:[1,0,0] neg_lo:[0,0,1] neg_hi:[0,0,1]\n"
"v_pk_fma_f32 v[156:157], v[156:157], s[74:75], v[20:21] op_sel_hi:[1,0,0] neg_lo:[0,0,1] neg_hi:[0,0,1]\n"
"v_pk_fma_f32 v[158:159], v[158:159], s[74:75], v[20:21] op_sel_hi:[1,0,0] neg_lo:[0,0,1] neg_hi:[0,0,1]\n"
"v_pk_fma_f32 v[160:161], v[160:161], s[74:75], v[20:21] op_sel_hi:[1,0,0] neg_lo:[0,0,1] neg_hi:[0,0,1]\n"
"v_pk_fma_f32 v[162:163], v[162:163], s[74:75], v[20:21] op_sel_hi:[1,0,0] neg_lo:[0,0,1] neg_hi:[0,0,1]\n"
"v_pk_fma_f32 v[164:165], v[164:165], s[74:75], v[20:21] op_sel_hi:[1,0,0] neg_lo:[0,0,1] neg_hi:[0,0,1]\n"
"v_pk_fma_f32 v[166:167], v[166:167], s[74:75], v[20:21] op_sel_hi:[1,0,0] neg_lo:[0,0,1] neg_hi:[0,0,1]\n"
"v_exp_f32 v148, v148\n"
"v_exp_f32 v149, v149\n"
"v_exp_f32 v150, v150\n"
"v_exp_f32 v151, v151\n"
"v_exp_f32 v152, v152\n"
"v_exp_f32 v153, v153\n"
"v_exp_f32 v154, v154\n"
"v_exp_f32 v155, v155\n"
"v_exp_f32 v156, v156\n"
"v_exp_f32 v157, v157\n"
"v_exp_f32 v158, v158\n"
"v_exp_f32 v159, v159\n"
"v_exp_f32 v160, v160\n"
"v_exp_f32 v161, v161\n"
"v_exp_f32 v162, v162\n"
"v_exp_f32 v163, v163\n"
"v_exp_f32 v164, v164\n"
"v_exp_f32 v165, v165\n"
"v_exp_f32 v166, v166\n"
"v_exp_f32 v167, v167\n"
"v_pk_add_f32 v[178:179], v[148:149], v[150:151]\n"
"v_pk_add_f32 v[180:181], v[152:153], v[154:155]\n"
"v_pk_add_f32 v[178:179], v[178:179], v[156:157]\n"
"v_pk_add_f32 v[180:181], v[180:181], v[158:159]\n"
"v_pk_add_f32 v[178:179], v[178:179], v[160:161]\n"
"v_pk_add_f32 v[180:181], v[180:181], v[162:163]\n"
"v_pk_add_f32 v[178:179], v[178:179], v[164:165]\n"
"v_pk_add_f32 v[180:181], v[180:181], v[166:167]\n"
"v_pk_add_f32 v[178:179], v[178:179], v[180:181]\n"
"v_add_f32 v30, v178, v179\n"
"v_mov_b32 v29, v30\n"
"v_accvgpr_read_b32 v25, a82\n"
"v_accvgpr_read_b32 v26, a86\n"
"v_permlane16_swap_b32 v30, v29\n"
"v_add_f32 v30, v30, v29\n"
"v_mov_b32 v29, v30\n"
"v_cvt_pk_bf16_f32 v39, v27, v28\n"
"v_cvt_pk_bf16_f32 v42, v25, v26\n"
"v_permlane32_swap_b32 v30, v29\n"
"v_add_f32 v30, v30, v29\n"
"v_rcp_f32 v30, v30\n"
"v_accvgpr_read_b32 v27, a90\n"
"v_pk_mul_f32 v[148:149], v[148:149], v[30:31] op_sel_hi:[1,0]\n"
"v_pk_mul_f32 v[150:151], v[150:151], v[30:31] op_sel_hi:[1,0]\n"
"v_pk_mul_f32 v[152:153], v[152:153], v[30:31] op_sel_hi:[1,0]\n"
"v_pk_mul_f32 v[154:155], v[154:155], v[30:31] op_sel_hi:[1,0]\n"
"v_pk_mul_f32 v[156:157], v[156:157], v[30:31] op_sel_hi:[1,0]\n"
"v_pk_mul_f32 v[158:159], v[158:159], v[30:31] op_sel_hi:[1,0]\n"
"v_pk_mul_f32 v[160:161], v[160:161], v[30:31] op_sel_hi:[1,0]\n"
"v_pk_mul_f32 v[162:163], v[162:163], v[30:31] op_sel_hi:[1,0]\n"
"v_pk_mul_f32 v[164:165], v[164:165], v[30:31] op_sel_hi:[1,0]\n"
"v_pk_mul_f32 v[166:167], v[166:167], v[30:31] op_sel_hi:[1,0]\n"
"v_accvgpr_read_b32 v28, a94\n"
"v_accvgpr_read_b32 v25, a83\n"
"v_accvgpr_read_b32 v26, a87\n"
"v_cvt_pk_bf16_f32 v43, v27, v28\n"
"v_cvt_pk_bf16_f32 v46, v25, v26\n"
"v_accvgpr_read_b32 v27, a91\n"
"v_accvgpr_read_b32 v28, a95\n"
"v_accvgpr_read_b32 v25, a112\n"
"v_accvgpr_read_b32 v26, a116\n"
"v_cvt_pk_bf16_f32 v47, v27, v28\n"
"v_cvt_pk_bf16_f32 v50, v25, v26\n"
"v_accvgpr_read_b32 v27, a120\n"
"v_accvgpr_read_b32 v28, a124\n"
"v_accvgpr_read_b32 v25, a113\n"
"v_accvgpr_read_b32 v26, a117\n"
"v_cvt_pk_bf16_f32 v51, v27, v28\n"
"v_cvt_pk_bf16_f32 v54, v25, v26\n"
"v_accvgpr_read_b32 v27, a121\n"
"v_accvgpr_read_b32 v28, a125\n"
"v_accvgpr_read_b32 v25, a114\n"
"v_accvgpr_read_b32 v26, a118\n"
"v_cvt_pk_bf16_f32 v55, v27, v28\n"
"v_cvt_pk_bf16_f32 v58, v25, v26\n"
"v_accvgpr_read_b32 v27, a122\n"
"v_accvgpr_read_b32 v28, a126\n"
"v_accvgpr_read_b32 v25, a115\n"
"v_accvgpr_read_b32 v26, a119\n"
"v_cvt_pk_bf16_f32 v59, v27, v28\n"
"v_cvt_pk_bf16_f32 v62, v25, v26\n"
"v_accvgpr_read_b32 v27, a123\n"
"v_accvgpr_read_b32 v28, a127\n"
"v_accvgpr_read_b32 v25, a144\n"
"v_accvgpr_read_b32 v26, a148\n"
"v_cvt_pk_bf16_f32 v63, v27, v28\n"
"v_accvgpr_read_b32 v27, a152\n"
"v_accvgpr_read_b32 v28, a156\n"
"v_cvt_pk_bf16_f32 v168, v128, v129\n"
"v_cvt_pk_bf16_f32 v169, v130, v131\n"
"v_mfma_f32_16x16x32_bf16 v[128:131], a[200:203], v[32:35], 0\n"
"v_cvt_pk_bf16_f32 v170, v132, v133\n"
"v_cvt_pk_bf16_f32 v171, v134, v135\n"
"v_mfma_f32_16x16x32_bf16 v[132:135], a[216:219], v[32:35], 0\n"
"v_cvt_pk_bf16_f32 v172, v136, v137\n"
"v_cvt_pk_bf16_f32 v173, v138, v139\n"
"v_mfma_f32_16x16x32_bf16 v[136:139], a[232:235], v[32:35], 0\n"
"v_cvt_pk_bf16_f32 v174, v140, v141\n"
"v_cvt_pk_bf16_f32 v175, v142, v143\n"
"v_mfma_f32_16x16x32_bf16 v[140:143], a[248:251], v[32:35], 0\n"
"v_cvt_pk_bf16_f32 v176, v144, v145\n"
"v_cvt_pk_bf16_f32 v177, v146, v147\n"
"v_mfma_f32_16x16x32_bf16 v[144:147], v[232:235], v[32:35], 0\n"
"v_cvt_pk_bf16_f32 v33, v23, v24\n"
"v_accvgpr_read_b32 v23, a137\n"
"v_mfma_f32_16x16x32_bf16 v[128:131], a[204:207], v[36:39], v[128:131]\n"
"v_accvgpr_read_b32 v24, a141\n"
"v_cvt_pk_bf16_f32 v32, v21, v22\n"
"v_mfma_f32_16x16x32_bf16 v[132:135], a[220:223], v[36:39], v[132:135]\n"
"v_accvgpr_read_b32 v21, a129\n"
"v_accvgpr_read_b32 v22, a133\n"
"v_mfma_f32_16x16x32_bf16 v[136:139], a[236:239], v[36:39], v[136:139]\n"
"v_cvt_pk_bf16_f32 v34, v25, v26\n"
"v_accvgpr_read_b32 v25, a145\n"
"v_mfma_f32_16x16x32_bf16 v[140:143], a[252:255], v[36:39], v[140:143]\n"
"v_accvgpr_read_b32 v26, a149\n"
"v_cvt_pk_bf16_f32 v35, v27, v28\n"
"v_mfma_f32_16x16x32_bf16 v[144:147], v[236:239], v[36:39], v[144:147]\n"
"v_cvt_pk_bf16_f32 v37, v23, v24\n"
"v_accvgpr_read_b32 v23, a138\n"
"v_mfma_f32_16x16x32_bf16 v[128:131], a[208:211], v[40:43], v[128:131]\n"
"v_accvgpr_read_b32 v24, a142\n"
"v_cvt_pk_bf16_f32 v36, v21, v22\n"
"v_mfma_f32_16x16x32_bf16 v[132:135], a[224:227], v[40:43], v[132:135]\n"
"v_accvgpr_read_b32 v21, a130\n"
"v_accvgpr_read_b32 v22, a134\n"
"v_mfma_f32_16x16x32_bf16 v[136:139], a[240:243], v[40:43], v[136:139]\n"
"v_cvt_pk_bf16_f32 v38, v25, v26\n"
"v_accvgpr_read_b32 v27, a153\n"
"v_mfma_f32_16x16x32_bf16 v[140:143], v[224:227], v[40:43], v[140:143]\n"
"v_accvgpr_read_b32 v28, a157\n"
"v_accvgpr_read_b32 v25, a146\n"
"v_mfma_f32_16x16x32_bf16 v[144:147], v[240:243], v[40:43], v[144:147]\n"
"v_cvt_pk_bf16_f32 v41, v23, v24\n"
"v_accvgpr_read_b32 v23, a139\n"
"v_mfma_f32_16x16x32_bf16 v[128:131], a[212:215], v[44:47], v[128:131]\n"
"v_accvgpr_read_b32 v24, a143\n"
"v_cvt_pk_bf16_f32 v40, v21, v22\n"
"v_mfma_f32_16x16x32_bf16 v[132:135], a[228:231], v[44:47], v[132:135]\n"
"v_accvgpr_read_b32 v21, a131\n"
"v_accvgpr_read_b32 v22, a135\n"
"v_mfma_f32_16x16x32_bf16 v[136:139], a[244:247], v[44:47], v[136:139]\n"
"v_accvgpr_read_b32 v26, a150\n"
"v_cvt_pk_bf16_f32 v39, v27, v28\n"
"v_mfma_f32_16x16x32_bf16 v[140:143], v[228:231], v[44:47], v[140:143]\n"
"v_max3_f32 v19, v128, v129, v130\n"
"v_cvt_pk_bf16_f32 v42, v25, v26\n"
"v_mfma_f32_16x16x32_bf16 v[144:147], v[244:247], v[44:47], v[144:147]\n"
"buffer_load_dwordx4 v9, s[24:27], s54 offen offset:1024 lds\n"
"v_max3_f32 v20, v131, v132, v133\n"
"v_max3_f32 v19, v19, v134, v135\n"
"v_max3_f32 v20, v20, v136, v137\n"
"v_max3_f32 v19, v19, v138, v139\n"
"v_max3_f32 v20, v20, v140, v141\n"
"v_max3_f32 v19, v19, v142, v143\n"
"v_cvt_pk_bf16_f32 v45, v23, v24\n"
"v_max3_f32 v20, v20, v144, v145\n"
"v_max3_f32 v19, v19, v146, v147\n"
"v_max_f32 v19, v19, v20\n"
"v_mov_b32 v20, v19\n"
"v_add_u32 v24, s75, v0\n"
"buffer_load_dwordx4 v10, s[24:27], s54 offen offset:2048 lds\n"
"v_permlane16_swap_b32 v19, v20\n"
"v_max_f32 v19, v19, v20\n"
"v_mov_b32 v20, v19\n"
"v_cvt_pk_bf16_f32 v44, v21, v22\n"
"v_xor_b32 v21, 4096, v24\n"
"v_permlane32_swap_b32 v19, v20\n"
"v_max_f32 v19, v19, v20\n"
"v_mul_f32 v20, s74, v19\n"
"v_pk_fma_f32 v[128:129], v[128:129], s[74:75], v[20:21] op_sel_hi:[1,0,0] neg_lo:[0,0,1] neg_hi:[0,0,1]\n"
"v_pk_fma_f32 v[130:131], v[130:131], s[74:75], v[20:21] op_sel_hi:[1,0,0] neg_lo:[0,0,1] neg_hi:[0,0,1]\n"
"buffer_load_dwordx4 v11, s[24:27], s54 offen offset:3072 lds\n"
"s_add_u32 m0, s69, 16384\n"
"v_pk_fma_f32 v[132:133], v[132:133], s[74:75], v[20:21] op_sel_hi:[1,0,0] neg_lo:[0,0,1] neg_hi:[0,0,1]\n"
"v_pk_fma_f32 v[134:135], v[134:135], s[74:75], v[20:21] op_sel_hi:[1,0,0] neg_lo:[0,0,1] neg_hi:[0,0,1]\n"
"v_pk_fma_f32 v[136:137], v[136:137], s[74:75], v[20:21] op_sel_hi:[1,0,0] neg_lo:[0,0,1] neg_hi:[0,0,1]\n"
"v_pk_fma_f32 v[138:139], v[138:139], s[74:75], v[20:21] op_sel_hi:[1,0,0] neg_lo:[0,0,1] neg_hi:[0,0,1]\n"
"v_pk_fma_f32 v[140:141], v[140:141], s[74:75], v[20:21] op_sel_hi:[1,0,0] neg_lo:[0,0,1] neg_hi:[0,0,1]\n"
"v_pk_fma_f32 v[142:143], v[142:143], s[74:75], v[20:21] op_sel_hi:[1,0,0] neg_lo:[0,0,1] neg_hi:[0,0,1]\n"
"v_pk_fma_f32 v[144:145], v[144:145], s[74:75], v[20:21] op_sel_hi:[1,0,0] neg_lo:[0,0,1] neg_hi:[0,0,1]\n"
"v_pk_fma_f32 v[146:147], v[146:147], s[74:75], v[20:21] op_sel_hi:[1,0,0] neg_lo:[0,0,1] neg_hi:[0,0,1]\n"
"v_exp_f32 v128, v128\n"
"v_exp_f32 v129, v129\n"
"v_exp_f32 v130, v130\n"
"buffer_load_dwordx4 v8, s[24:27], s72 offen lds\n"
"v_exp_f32 v131, v131\n"
"v_exp_f32 v132, v132\n"
"v_exp_f32 v133, v133\n"
"v_exp_f32 v134, v134\n"
"v_exp_f32 v135, v135\n"
"v_exp_f32 v136, v136\n"
"buffer_load_dwordx4 v9, s[24:27], s72 offen offset:1024 lds\n"
"v_exp_f32 v137, v137\n"
"v_exp_f32 v138, v138\n"
"v_exp_f32 v139, v139\n"
"v_exp_f32 v140, v140\n"
"v_exp_f32 v141, v141\n"
"v_exp_f32 v142, v142\n"
"v_exp_f32 v143, v143\n"
"buffer_load_dwordx4 v10, s[24:27], s72 offen offset:2048 lds\n"
"v_exp_f32 v144, v144\n"
"v_exp_f32 v145, v145\n"
"v_exp_f32 v146, v146\n"
"v_exp_f32 v147, v147\n"
"v_pk_add_f32 v[96:97], v[128:129], v[130:131]\n"
"v_pk_add_f32 v[98:99], v[132:133], v[134:135]\n"
"v_pk_add_f32 v[96:97], v[96:97], v[136:137]\n"
"buffer_load_dwordx4 v11, s[24:27], s72 offen offset:3072 lds\n"
"s_add_u32 m0, s69, 32768\n"
"s_add_u32 s72, s54, 65536\n"
"v_pk_add_f32 v[98:99], v[98:99], v[138:139]\n"
"v_pk_add_f32 v[96:97], v[96:97], v[140:141]\n"
"v_pk_add_f32 v[98:99], v[98:99], v[142:143]\n"
"v_pk_add_f32 v[96:97], v[96:97], v[144:145]\n"
"v_pk_add_f32 v[98:99], v[98:99], v[146:147]\n"
"v_pk_add_f32 v[96:97], v[96:97], v[98:99]\n"
"v_add_f32 v30, v96, v97\n"
"v_mov_b32 v29, v30\n"
"v_xor_b32 v22, 8192, v24\n"
"v_xor_b32 v23, 12288, v24\n"
"v_permlane16_swap_b32 v30, v29\n"
"buffer_load_dwordx4 v8, s[24:27], s72 offen lds\n"
"v_add_f32 v30, v30, v29\n"
"v_mov_b32 v29, v30\n"
"v_accvgpr_read_b32 v27, a154\n"
"v_accvgpr_read_b32 v28, a158\n"
"v_permlane32_swap_b32 v30, v29\n"
"v_add_f32 v30, v30, v29\n"
"v_rcp_f32 v30, v30\n"
"v_accvgpr_read_b32 v25, a147\n"
"v_pk_mul_f32 v[128:129], v[128:129], v[30:31] op_sel_hi:[1,0]\n"
"buffer_load_dwordx4 v9, s[24:27], s72 offen offset:1024 lds\n"
"v_pk_mul_f32 v[130:131], v[130:131], v[30:31] op_sel_hi:[1,0]\n"
"v_pk_mul_f32 v[132:133], v[132:133], v[30:31] op_sel_hi:[1,0]\n"
"v_pk_mul_f32 v[134:135], v[134:135], v[30:31] op_sel_hi:[1,0]\n"
"v_pk_mul_f32 v[136:137], v[136:137], v[30:31] op_sel_hi:[1,0]\n"
"v_pk_mul_f32 v[138:139], v[138:139], v[30:31] op_sel_hi:[1,0]\n"
"v_pk_mul_f32 v[140:141], v[140:141], v[30:31] op_sel_hi:[1,0]\n"
"v_pk_mul_f32 v[142:143], v[142:143], v[30:31] op_sel_hi:[1,0]\n"
"v_pk_mul_f32 v[144:145], v[144:145], v[30:31] op_sel_hi:[1,0]\n"
"v_pk_mul_f32 v[146:147], v[146:147], v[30:31] op_sel_hi:[1,0]\n"
"v_cvt_pk_bf16_f32 v43, v27, v28\n"
"v_accvgpr_read_b32 v26, a151\n"
"v_accvgpr_read_b32 v27, a155\n"
"v_accvgpr_read_b32 v28, a159\n"
"buffer_load_dwordx4 v10, s[24:27], s72 offen offset:2048 lds\n"
"v_cvt_pk_bf16_f32 v178, v148, v149\n"
"v_cvt_pk_bf16_f32 v179, v150, v151\n"
"v_mfma_f32_16x16x32_bf16 v[148:151], a[200:203], v[48:51], 0\n"
"v_cvt_pk_bf16_f32 v180, v152, v153\n"
"v_cvt_pk_bf16_f32 v181, v154, v155\n"
"v_mfma_f32_16x16x32_bf16 v[152:155], a[216:219], v[48:51], 0\n"
"v_cvt_pk_bf16_f32 v182, v156, v157\n"
"v_cvt_pk_bf16_f32 v183, v158, v159\n"
"v_mfma_f32_16x16x32_bf16 v[156:159], a[232:235], v[48:51], 0\n"
"buffer_load_dwordx4 v11, s[24:27], s72 offen offset:3072 lds\n"
"s_add_u32 m0, s69, 49152\n"
"s_add_u32 s72, s54, 98304\n"
"v_cvt_pk_bf16_f32 v104, v144, v145\n"
"v_cvt_pk_bf16_f32 v105, v146, v147\n"
"v_cvt_pk_bf16_f32 v96, v128, v129\n"
"v_cvt_pk_bf16_f32 v97, v130, v131\n"
"v_cvt_pk_bf16_f32 v98, v132, v133\n"
"v_cvt_pk_bf16_f32 v99, v134, v135\n"
"v_cvt_pk_bf16_f32 v100, v136, v137\n"
"v_cvt_pk_bf16_f32 v101, v138, v139\n"
"v_cvt_pk_bf16_f32 v102, v140, v141\n"
"v_cvt_pk_bf16_f32 v103, v142, v143\n"
"v_cvt_pk_bf16_f32 v184, v160, v161\n"
"v_cvt_pk_bf16_f32 v185, v162, v163\n"
"v_mfma_f32_16x16x32_bf16 v[160:163], a[248:251], v[48:51], 0\n"
"buffer_load_dwordx4 v8, s[24:27], s72 offen lds\n"
"v_cvt_pk_bf16_f32 v186, v164, v165\n"
"v_cvt_pk_bf16_f32 v187, v166, v167\n"
"v_mfma_f32_16x16x32_bf16 v[164:167], v[232:235], v[48:51], 0\n"
"v_cvt_pk_bf16_f32 v46, v25, v26\n"
"v_cvt_pk_bf16_f32 v47, v27, v28\n"
"v_mfma_f32_16x16x32_bf16 v[148:151], a[204:207], v[52:55], v[148:151]\n"
"v_mfma_f32_16x16x32_bf16 v[152:155], a[220:223], v[52:55], v[152:155]\n"
"v_mfma_f32_16x16x32_bf16 v[156:159], a[236:239], v[52:55], v[156:159]\n"
"buffer_load_dwordx4 v9, s[24:27], s72 offen offset:1024 lds\n"
"v_mfma_f32_16x16x32_bf16 v[160:163], a[252:255], v[52:55], v[160:163]\n"
"v_mfma_f32_16x16x32_bf16 v[164:167], v[236:239], v[52:55], v[164:167]\n"
"v_mfma_f32_16x16x32_bf16 v[148:151], a[208:211], v[56:59], v[148:151]\n"
"v_mfma_f32_16x16x32_bf16 v[152:155], a[224:227], v[56:59], v[152:155]\n"
"buffer_load_dwordx4 v10, s[24:27], s72 offen offset:2048 lds\n"
"v_mfma_f32_16x16x32_bf16 v[156:159], a[240:243], v[56:59], v[156:159]\n"
"v_mfma_f32_16x16x32_bf16 v[160:163], v[224:227], v[56:59], v[160:163]\n"
"v_mfma_f32_16x16x32_bf16 v[164:167], v[240:243], v[56:59], v[164:167]\n"
"v_mfma_f32_16x16x32_bf16 v[148:151], a[212:215], v[60:63], v[148:151]\n"
"buffer_load_dwordx4 v11, s[24:27], s72 offen offset:3072 lds\n"
"v_mfma_f32_16x16x32_bf16 v[152:155], a[228:231], v[60:63], v[152:155]\n"
"s_add_u32 m0, s69, 65536\n"
"s_add_u32 s72, s55, 32768\n"
"v_mfma_f32_16x16x32_bf16 v[156:159], a[244:247], v[60:63], v[156:159]\n"
"v_mfma_f32_16x16x32_bf16 v[160:163], v[228:231], v[60:63], v[160:163]\n"
"v_mfma_f32_16x16x32_bf16 v[164:167], v[244:247], v[60:63], v[164:167]\n"
"buffer_load_dwordx4 v8, s[24:27], s55 offen lds\n"
"v_mfma_f32_16x16x32_bf16 v[128:131], a[200:203], v[32:35], 0\n"
"v_max3_f32 v19, v148, v149, v150\n"
"v_max3_f32 v20, v151, v152, v153\n"
"v_mfma_f32_16x16x32_bf16 v[132:135], a[216:219], v[32:35], 0\n"
"v_max3_f32 v19, v19, v154, v155\n"
"v_max3_f32 v20, v20, v156, v157\n"
"v_mfma_f32_16x16x32_bf16 v[136:139], a[232:235], v[32:35], 0\n"
"v_max3_f32 v19, v19, v158, v159\n"
"v_max3_f32 v20, v20, v160, v161\n"
"v_mfma_f32_16x16x32_bf16 v[140:143], a[248:251], v[32:35], 0\n"
"buffer_load_dwordx4 v9, s[24:27], s55 offen offset:1024 lds\n"
"v_mfma_f32_16x16x32_bf16 v[144:147], v[232:235], v[32:35], 0\n"
"v_max3_f32 v19, v19, v162, v163\n"
"v_max3_f32 v20, v20, v164, v165\n"
"v_mfma_f32_16x16x32_bf16 v[128:131], a[204:207], v[36:39], v[128:131]\n"
"v_max3_f32 v19, v19, v166, v167\n"
"v_max_f32 v19, v19, v20\n"
"v_mfma_f32_16x16x32_bf16 v[132:135], a[220:223], v[36:39], v[132:135]\n"
"v_mov_b32 v20, v19\n"
"v_mfma_f32_16x16x32_bf16 v[136:139], a[236:239], v[36:39], v[136:139]\n"
"buffer_load_dwordx4 v10, s[24:27], s55 offen offset:2048 lds\n"
"v_mfma_f32_16x16x32_bf16 v[140:143], a[252:255], v[36:39], v[140:143]\n"
"v_permlane16_swap_b32 v19, v20\n"
"v_mfma_f32_16x16x32_bf16 v[144:147], v[236:239], v[36:39], v[144:147]\n"
"v_max_f32 v19, v19, v20\n"
"v_mov_b32 v20, v19\n"
"v_mfma_f32_16x16x32_bf16 v[128:131], a[208:211], v[40:43], v[128:131]\n"
"v_mfma_f32_16x16x32_bf16 v[132:135], a[224:227], v[40:43], v[132:135]\n"
"buffer_load_dwordx4 v11, s[24:27], s55 offen offset:3072 lds\n"
"v_mfma_f32_16x16x32_bf16 v[136:139], a[240:243], v[40:43], v[136:139]\n"
"s_add_u32 m0, s69, 131072\n"
"v_permlane32_swap_b32 v19, v20\n"
"v_mfma_f32_16x16x32_bf16 v[140:143], v[224:227], v[40:43], v[140:143]\n"
"v_max_f32 v19, v19, v20\n"
"v_mul_f32 v20, s74, v19\n"
"v_mfma_f32_16x16x32_bf16 v[144:147], v[240:243], v[40:43], v[144:147]\n"
"v_pk_fma_f32 v[148:149], v[148:149], s[74:75], v[20:21] op_sel_hi:[1,0,0] neg_lo:[0,0,1] neg_hi:[0,0,1]\n"
"v_pk_fma_f32 v[150:151], v[150:151], s[74:75], v[20:21] op_sel_hi:[1,0,0] neg_lo:[0,0,1] neg_hi:[0,0,1]\n"
"v_mfma_f32_16x16x32_bf16 v[128:131], a[212:215], v[44:47], v[128:131]\n"
"v_pk_fma_f32 v[152:153], v[152:153], s[74:75], v[20:21] op_sel_hi:[1,0,0] neg_lo:[0,0,1] neg_hi:[0,0,1]\n"
"v_pk_fma_f32 v[154:155], v[154:155], s[74:75], v[20:21] op_sel_hi:[1,0,0] neg_lo:[0,0,1] neg_hi:[0,0,1]\n"
"v_mfma_f32_16x16x32_bf16 v[132:135], a[228:231], v[44:47], v[132:135]\n"
"v_pk_fma_f32 v[156:157], v[156:157], s[74:75], v[20:21] op_sel_hi:[1,0,0] neg_lo:[0,0,1] neg_hi:[0,0,1]\n"
"v_pk_fma_f32 v[158:159], v[158:159], s[74:75], v[20:21] op_sel_hi:[1,0,0] neg_lo:[0,0,1] neg_hi:[0,0,1]\n"
"v_mfma_f32_16x16x32_bf16 v[136:139], a[244:247], v[44:47], v[136:139]\n"
"v_pk_fma_f32 v[160:161], v[160:161], s[74:75], v[20:21] op_sel_hi:[1,0,0] neg_lo:[0,0,1] neg_hi:[0,0,1]\n"
"v_pk_fma_f32 v[162:163], v[162:163], s[74:75], v[20:21] op_sel_hi:[1,0,0] neg_lo:[0,0,1] neg_hi:[0,0,1]\n"
"v_mfma_f32_16x16x32_bf16 v[140:143], v[228:231], v[44:47], v[140:143]\n"
"v_pk_fma_f32 v[164:165], v[164:165], s[74:75], v[20:21] op_sel_hi:[1,0,0] neg_lo:[0,0,1] neg_hi:[0,0,1]\n"
"v_pk_fma_f32 v[166:167], v[166:167], s[74:75], v[20:21] op_sel_hi:[1,0,0] neg_lo:[0,0,1] neg_hi:[0,0,1]\n"
"v_mfma_f32_16x16x32_bf16 v[144:147], v[244:247], v[44:47], v[144:147]\n"
"v_max3_f32 v19, v128, v129, v130\n"
"v_max3_f32 v20, v131, v132, v133\n"
"v_max3_f32 v19, v19, v134, v135\n"
"v_max3_f32 v20, v20, v136, v137\n"
"v_max3_f32 v19, v19, v138, v139\n"
"v_exp_f32 v148, v148\n"
"v_exp_f32 v149, v149\n"
"v_exp_f32 v150, v150\n"
"v_exp_f32 v151, v151\n"
"v_exp_f32 v152, v152\n"
"v_exp_f32 v153, v153\n"
"v_exp_f32 v154, v154\n"
"v_exp_f32 v155, v155\n"
"v_max3_f32 v20, v20, v140, v141\n"
"v_max3_f32 v19, v19, v142, v143\n"
"v_exp_f32 v156, v156\n"
"v_exp_f32 v157, v157\n"
"v_exp_f32 v158, v158\n"
"v_exp_f32 v159, v159\n"
"v_max3_f32 v20, v20, v144, v145\n"
"v_max3_f32 v19, v19, v146, v147\n"
"v_exp_f32 v160, v160\n"
"v_exp_f32 v161, v161\n"
"v_exp_f32 v162, v162\n"
"v_exp_f32 v163, v163\n"
"v_max_f32 v19, v19, v20\n"
"v_exp_f32 v164, v164\n"
"v_exp_f32 v165, v165\n"
"v_exp_f32 v166, v166\n"
"v_exp_f32 v167, v167\n"
"v_pk_add_f32 v[106:107], v[148:149], v[150:151]\n"
"v_pk_add_f32 v[108:109], v[152:153], v[154:155]\n"
"v_mov_b32 v20, v19\n"
"v_pk_add_f32 v[106:107], v[106:107], v[156:157]\n"
"v_pk_add_f32 v[108:109], v[108:109], v[158:159]\n"
"v_pk_add_f32 v[106:107], v[106:107], v[160:161]\n"
"v_pk_add_f32 v[108:109], v[108:109], v[162:163]\n"
"v_pk_add_f32 v[106:107], v[106:107], v[164:165]\n"
"v_pk_add_f32 v[108:109], v[108:109], v[166:167]\n"
"v_permlane16_swap_b32 v19, v20\n"
"v_pk_add_f32 v[106:107], v[106:107], v[108:109]\n"
"v_add_f32 v30, v106, v107\n"
"v_max_f32 v19, v19, v20\n"
"v_mov_b32 v29, v30\n"
"v_mov_b32 v20, v19\n"
"s_nop 0\n"
"v_permlane16_swap_b32 v30, v29\n"
"v_add_f32 v30, v30, v29\n"
"v_permlane32_swap_b32 v19, v20\n"
"v_max_f32 v19, v19, v20\n"
"v_mov_b32 v29, v30\n"
"v_mul_f32 v20, s74, v19\n"
"v_pk_fma_f32 v[128:129], v[128:129], s[74:75], v[20:21] op_sel_hi:[1,0,0] neg_lo:[0,0,1] neg_hi:[0,0,1]\n"
"v_pk_fma_f32 v[130:131], v[130:131], s[74:75], v[20:21] op_sel_hi:[1,0,0] neg_lo:[0,0,1] neg_hi:[0,0,1]\n"
"v_pk_fma_f32 v[132:133], v[132:133], s[74:75], v[20:21] op_sel_hi:[1,0,0] neg_lo:[0,0,1] neg_hi:[0,0,1]\n"
"v_pk_fma_f32 v[134:135], v[134:135], s[74:75], v[20:21] op_sel_hi:[1,0,0] neg_lo:[0,0,1] neg_hi:[0,0,1]\n"
"v_exp_f32 v128, v128\n"
"v_exp_f32 v129, v129\n"
"v_exp_f32 v130, v130\n"
"buffer_load_dword v3, v2, s[24:27], s77 offen\n"
"v_exp_f32 v131, v131\n"
"v_exp_f32 v132, v132\n"
"v_exp_f32 v133, v133\n"
"v_exp_f32 v134, v134\n"
"v_exp_f32 v135, v135\n"
"v_pk_fma_f32 v[136:137], v[136:137], s[74:75], v[20:21] op_sel_hi:[1,0,0] neg_lo:[0,0,1] neg_hi:[0,0,1]\n"
"v_pk_fma_f32 v[138:139], v[138:139], s[74:75], v[20:21] op_sel_hi:[1,0,0] neg_lo:[0,0,1] neg_hi:[0,0,1]\n"
"v_permlane32_swap_b32 v30, v29\n"
"v_exp_f32 v136, v136\n"
"v_exp_f32 v137, v137\n"
"v_exp_f32 v138, v138\n"
"v_exp_f32 v139, v139\n"
"v_pk_fma_f32 v[140:141], v[140:141], s[74:75], v[20:21] op_sel_hi:[1,0,0] neg_lo:[0,0,1] neg_hi:[0,0,1]\n"
"v_pk_fma_f32 v[142:143], v[142:143], s[74:75], v[20:21] op_sel_hi:[1,0,0] neg_lo:[0,0,1] neg_hi:[0,0,1]\n"
"v_exp_f32 v140, v140\n"
"v_exp_f32 v141, v141\n"
"v_exp_f32 v142, v142\n"
"v_exp_f32 v143, v143\n"
"v_pk_fma_f32 v[144:145], v[144:145], s[74:75], v[20:21] op_sel_hi:[1,0,0] neg_lo:[0,0,1] neg_hi:[0,0,1]\n"
"v_pk_fma_f32 v[146:147], v[146:147], s[74:75], v[20:21] op_sel_hi:[1,0,0] neg_lo:[0,0,1] neg_hi:[0,0,1]\n"
"v_add_f32 v30, v30, v29\n"
"v_exp_f32 v144, v144\n"
"v_exp_f32 v145, v145\n"
"v_exp_f32 v146, v146\n"
"v_exp_f32 v147, v147\n"
"v_pk_add_f32 v[116:117], v[128:129], v[130:131]\n"
"v_pk_add_f32 v[118:119], v[132:133], v[134:135]\n"
"v_rcp_f32 v30, v30\n"
"v_pk_add_f32 v[116:117], v[116:117], v[136:137]\n"
"v_pk_add_f32 v[118:119], v[118:119], v[138:139]\n"
"v_pk_add_f32 v[116:117], v[116:117], v[140:141]\n"
"v_pk_add_f32 v[118:119], v[118:119], v[142:143]\n"
"v_pk_add_f32 v[116:117], v[116:117], v[144:145]\n"
"v_pk_add_f32 v[118:119], v[118:119], v[146:147]\n"
"v_pk_mul_f32 v[148:149], v[148:149], v[30:31] op_sel_hi:[1,0]\n"
"v_pk_mul_f32 v[150:151], v[150:151], v[30:31] op_sel_hi:[1,0]\n"
"v_pk_mul_f32 v[152:153], v[152:153], v[30:31] op_sel_hi:[1,0]\n"
"v_pk_mul_f32 v[154:155], v[154:155], v[30:31] op_sel_hi:[1,0]\n"
"v_pk_mul_f32 v[156:157], v[156:157], v[30:31] op_sel_hi:[1,0]\n"
"v_pk_mul_f32 v[158:159], v[158:159], v[30:31] op_sel_hi:[1,0]\n"
"v_pk_mul_f32 v[160:161], v[160:161], v[30:31] op_sel_hi:[1,0]\n"
"v_pk_mul_f32 v[162:163], v[162:163], v[30:31] op_sel_hi:[1,0]\n"
"v_pk_mul_f32 v[164:165], v[164:165], v[30:31] op_sel_hi:[1,0]\n"
"v_pk_mul_f32 v[166:167], v[166:167], v[30:31] op_sel_hi:[1,0]\n"
"v_pk_add_f32 v[116:117], v[116:117], v[118:119]\n"
"v_add_f32 v30, v116, v117\n"
"v_mov_b32 v29, v30\n"
"v_cvt_pk_bf16_f32 v106, v148, v149\n"
"v_cvt_pk_bf16_f32 v107, v150, v151\n"
"v_permlane16_swap_b32 v30, v29\n"
"v_add_f32 v30, v30, v29\n"
"v_mov_b32 v29, v30\n"
"v_cvt_pk_bf16_f32 v108, v152, v153\n"
"v_cvt_pk_bf16_f32 v109, v154, v155\n"
"v_permlane32_swap_b32 v30, v29\n"
"v_add_f32 v30, v30, v29\n"
"v_rcp_f32 v30, v30\n"
"v_cvt_pk_bf16_f32 v110, v156, v157\n"
"v_pk_mul_f32 v[146:147], v[146:147], v[30:31] op_sel_hi:[1,0]\n"
"v_cvt_pk_bf16_f32 v125, v146, v147\n"
"s_waitcnt vmcnt(37)\n"
"ds_read_b64_tr_b16 a[0:1], v6 offset:0\n"
"s_waitcnt vmcnt(33)\n"
"ds_read_b64_tr_b16 a[2:3], v6 offset:16384\n"
"s_waitcnt vmcnt(29)\n"
"ds_read_b64_tr_b16 a[4:5], v6 offset:32768\n"
"s_waitcnt vmcnt(25)\n"
"ds_read_b64_tr_b16 a[6:7], v6 offset:49152\n"
"s_waitcnt vmcnt(21)\n"
"ds_read_b64_tr_b16 a[184:185], v7 offset:49152\n"
"v_xor_b32 v6, 32, v6\n"
"ds_read_b64_tr_b16 a[8:9], v6 offset:0\n"
"ds_read_b64_tr_b16 a[10:11], v6 offset:16384\n"
"ds_read_b64_tr_b16 a[12:13], v6 offset:32768\n"
"ds_read_b64_tr_b16 a[14:15], v6 offset:49152\n"
"v_add_u32 v7, 16384, v6\n"
"ds_read_b64_tr_b16 a[186:187], v7 offset:49152\n"
"v_xor_b32 v6, 96, v6\n"
"ds_read_b64_tr_b16 a[16:17], v6 offset:0\n"
"ds_read_b64_tr_b16 a[18:19], v6 offset:16384\n"
"ds_read_b64_tr_b16 a[20:21], v6 offset:32768\n"
"ds_read_b64_tr_b16 a[22:23], v6 offset:49152\n"
"v_add_u32 v7, 16384, v6\n"
"ds_read_b64_tr_b16 a[188:189], v7 offset:49152\n"
"v_xor_b32 v6, 32, v6\n"
"ds_read_b64_tr_b16 a[24:25], v6 offset:0\n"
"ds_read_b64_tr_b16 a[26:27], v6 offset:16384\n"
"ds_read_b64_tr_b16 a[28:29], v6 offset:32768\n"
"ds_read_b64_tr_b16 a[30:31], v6 offset:49152\n"
"v_add_u32 v7, 16384, v6\n"
"ds_read_b64_tr_b16 a[190:191], v7 offset:49152\n"
"v_xor_b32 v6, 224, v6\n"
"ds_read_b64_tr_b16 a[32:33], v6 offset:0\n"
"ds_read_b64_tr_b16 a[34:35], v6 offset:16384\n"
"ds_read_b64_tr_b16 a[36:37], v6 offset:32768\n"
"ds_read_b64_tr_b16 a[38:39], v6 offset:49152\n"
"v_add_u32 v7, 16384, v6\n"
"ds_read_b64_tr_b16 a[192:193], v7 offset:49152\n"
"v_xor_b32 v6, 32, v6\n"
"ds_read_b64_tr_b16 a[40:41], v6 offset:0\n"
"ds_read_b64_tr_b16 a[42:43], v6 offset:16384\n"
"s_waitcnt lgkmcnt(15)\n"
"v_mfma_f32_16x16x32_bf16 v[32:35], a[0:3], v[168:171], 0\n"
"ds_read_b64_tr_b16 a[44:45], v6 offset:32768\n"
"ds_read_b64_tr_b16 a[46:47], v6 offset:49152\n"
"v_add_u32 v7, 16384, v6\n"
"ds_read_b64_tr_b16 a[194:195], v7 offset:49152\n"
"v_mfma_f32_16x16x32_bf16 v[36:39], a[8:11], v[168:171], 0\n"
"v_xor_b32 v6, 96, v6\n"
"ds_read_b64_tr_b16 a[48:49], v6 offset:0\n"
"v_mfma_f32_16x16x32_bf16 v[32:35], a[4:7], v[172:175], v[32:35]\n"
"ds_read_b64_tr_b16 a[50:51], v6 offset:16384\n"
"ds_read_b64_tr_b16 a[52:53], v6 offset:32768\n"
"v_mfma_f32_16x16x32_bf16 v[36:39], a[12:15], v[172:175], v[36:39]\n"
"ds_read_b64_tr_b16 a[54:55], v6 offset:49152\n"
"v_add_u32 v7, 16384, v6\n"
"v_mfma_f32_16x16x16_bf16 v[32:35], a[184:185], v[176:177], v[32:35]\n"
"ds_read_b64_tr_b16 a[196:197], v7 offset:49152\n"
"v_xor_b32 v6, 32, v6\n"
"v_mfma_f32_16x16x16_bf16 v[36:39], a[186:187], v[176:177], v[36:39]\n"
"ds_read_b64_tr_b16 a[56:57], v6 offset:0\n"
"ds_read_b64_tr_b16 a[58:59], v6 offset:16384\n"
"v_mfma_f32_16x16x32_bf16 v[40:43], a[16:19], v[168:171], 0\n"
"s_waitcnt lgkmcnt(13)\n"
"ds_read_b64_tr_b16 a[60:61], v6 offset:32768\n"
"ds_read_b64_tr_b16 a[62:63], v6 offset:49152\n"
"v_mfma_f32_16x16x32_bf16 v[44:47], a[24:27], v[168:171], 0\n"
"v_add_u32 v7, 16384, v6\n"
"ds_read_b64_tr_b16 a[198:199], v7 offset:49152\n"
"v_mfma_f32_16x16x32_bf16 v[40:43], a[20:23], v[172:175], v[40:43]\n"
"v_cvt_pk_bf16_f32 v192, v32, v33\n"
"v_cvt_pk_bf16_f32 v193, v34, v35\n"
"v_mfma_f32_16x16x32_bf16 v[44:47], a[28:31], v[172:175], v[44:47]\n"
"v_cvt_pk_bf16_f32 v194, v36, v37\n"
"v_cvt_pk_bf16_f32 v195, v38, v39\n"
"v_mfma_f32_16x16x16_bf16 v[40:43], a[188:189], v[176:177], v[40:43]\n"
"ds_write_b128 v24, v[192:195]\n"
"v_mfma_f32_16x16x16_bf16 v[44:47], a[190:191], v[176:177], v[44:47]\n"
"v_cvt_pk_bf16_f32 v111, v158, v159\n"
"v_cvt_pk_bf16_f32 v112, v160, v161\n"
"v_mfma_f32_16x16x32_bf16 v[48:51], a[32:35], v[168:171], 0\n"
"s_waitcnt lgkmcnt(10)\n"
"v_cvt_pk_bf16_f32 v113, v162, v163\n"
"v_cvt_pk_bf16_f32 v114, v164, v165\n"
"v_mfma_f32_16x16x32_bf16 v[52:55], a[40:43], v[168:171], 0\n"
"v_cvt_pk_bf16_f32 v196, v40, v41\n"
"v_cvt_pk_bf16_f32 v197, v42, v43\n"
"v_mfma_f32_16x16x32_bf16 v[48:51], a[36:39], v[172:175], v[48:51]\n"
"v_cvt_pk_bf16_f32 v198, v44, v45\n"
"v_cvt_pk_bf16_f32 v199, v46, v47\n"
"v_mfma_f32_16x16x32_bf16 v[52:55], a[44:47], v[172:175], v[52:55]\n"
"ds_write_b128 v24, v[196:199] offset:1024\n"
"v_mfma_f32_16x16x16_bf16 v[48:51], a[192:193], v[176:177], v[48:51]\n"
"v_cvt_pk_bf16_f32 v115, v166, v167\n"
"v_pk_mul_f32 v[128:129], v[128:129], v[30:31] op_sel_hi:[1,0]\n"
"v_mfma_f32_16x16x16_bf16 v[52:55], a[194:195], v[176:177], v[52:55]\n"
"s_waitcnt lgkmcnt(2)\n"
"v_pk_mul_f32 v[130:131], v[130:131], v[30:31] op_sel_hi:[1,0]\n"
"v_pk_mul_f32 v[132:133], v[132:133], v[30:31] op_sel_hi:[1,0]\n"
"v_mfma_f32_16x16x32_bf16 v[56:59], a[48:51], v[168:171], 0\n"
"v_pk_mul_f32 v[134:135], v[134:135], v[30:31] op_sel_hi:[1,0]\n"
"v_cvt_pk_bf16_f32 v116, v128, v129\n"
"v_mfma_f32_16x16x32_bf16 v[60:63], a[56:59], v[168:171], 0\n"
"v_cvt_pk_bf16_f32 v200, v48, v49\n"
"v_cvt_pk_bf16_f32 v201, v50, v51\n"
"v_mfma_f32_16x16x32_bf16 v[56:59], a[52:55], v[172:175], v[56:59]\n"
"v_cvt_pk_bf16_f32 v202, v52, v53\n"
"v_cvt_pk_bf16_f32 v203, v54, v55\n"
"v_mfma_f32_16x16x32_bf16 v[60:63], a[60:63], v[172:175], v[60:63]\n"
"ds_write_b128 v24, v[200:203] offset:2048\n"
"v_mfma_f32_16x16x16_bf16 v[56:59], a[196:197], v[176:177], v[56:59]\n"
"v_cvt_pk_bf16_f32 v117, v130, v131\n"
"v_cvt_pk_bf16_f32 v118, v132, v133\n"
"v_mfma_f32_16x16x16_bf16 v[60:63], a[198:199], v[176:177], v[60:63]\n"
"v_cvt_pk_bf16_f32 v119, v134, v135\n"
"v_pk_mul_f32 v[136:137], v[136:137], v[30:31] op_sel_hi:[1,0]\n"
"v_mfma_f32_16x16x32_bf16 v[32:35], a[0:3], v[178:181], 0\n"
"v_pk_mul_f32 v[138:139], v[138:139], v[30:31] op_sel_hi:[1,0]\n"
"v_pk_mul_f32 v[140:141], v[140:141], v[30:31] op_sel_hi:[1,0]\n"
"v_mfma_f32_16x16x32_bf16 v[36:39], a[8:11], v[178:181], 0\n"
"v_cvt_pk_bf16_f32 v204, v56, v57\n"
"v_cvt_pk_bf16_f32 v205, v58, v59\n"
"v_mfma_f32_16x16x32_bf16 v[32:35], a[4:7], v[182:185], v[32:35]\n"
"v_cvt_pk_bf16_f32 v206, v60, v61\n"
"v_cvt_pk_bf16_f32 v207, v62, v63\n"
"v_mfma_f32_16x16x32_bf16 v[36:39], a[12:15], v[182:185], v[36:39]\n"
"ds_write_b128 v24, v[204:207] offset:3072\n"
"v_mfma_f32_16x16x16_bf16 v[32:35], a[184:185], v[186:187], v[32:35]\n"
"v_pk_mul_f32 v[142:143], v[142:143], v[30:31] op_sel_hi:[1,0]\n"
"v_cvt_pk_bf16_f32 v120, v136, v137\n"
"v_mfma_f32_16x16x16_bf16 v[36:39], a[186:187], v[186:187], v[36:39]\n"
"v_cvt_pk_bf16_f32 v121, v138, v139\n"
"v_cvt_pk_bf16_f32 v122, v140, v141\n"
"v_mfma_f32_16x16x32_bf16 v[40:43], a[16:19], v[178:181], 0\n"
"v_cvt_pk_bf16_f32 v123, v142, v143\n"
"v_pk_mul_f32 v[144:145], v[144:145], v[30:31] op_sel_hi:[1,0]\n"
"v_mfma_f32_16x16x32_bf16 v[44:47], a[24:27], v[178:181], 0\n"
"v_cvt_pk_bf16_f32 v208, v32, v33\n"
"v_cvt_pk_bf16_f32 v209, v34, v35\n"
"v_mfma_f32_16x16x32_bf16 v[40:43], a[20:23], v[182:185], v[40:43]\n"
"v_cvt_pk_bf16_f32 v210, v36, v37\n"
"v_cvt_pk_bf16_f32 v211, v38, v39\n"
"v_mfma_f32_16x16x32_bf16 v[44:47], a[28:31], v[182:185], v[44:47]\n"
"ds_write_b128 v24, v[208:211] offset:16384\n"
"v_mfma_f32_16x16x16_bf16 v[40:43], a[188:189], v[186:187], v[40:43]\n"
"v_cvt_pk_bf16_f32 v124, v144, v145\n"
"v_mfma_f32_16x16x16_bf16 v[44:47], a[190:191], v[186:187], v[44:47]\n"
"v_mfma_f32_16x16x32_bf16 v[48:51], a[32:35], v[178:181], 0\n"
"v_mfma_f32_16x16x32_bf16 v[52:55], a[40:43], v[178:181], 0\n"
"v_mfma_f32_16x16x32_bf16 v[48:51], a[36:39], v[182:185], v[48:51]\n"
"v_mfma_f32_16x16x32_bf16 v[52:55], a[44:47], v[182:185], v[52:55]\n"
"buffer_load_dword v4, v2, s[24:27], s78 offen\n"
"v_mfma_f32_16x16x16_bf16 v[48:51], a[192:193], v[186:187], v[48:51]\n"
"v_cvt_pk_bf16_f32 v212, v40, v41\n"
"v_cvt_pk_bf16_f32 v213, v42, v43\n"
"v_mfma_f32_16x16x16_bf16 v[52:55], a[194:195], v[186:187], v[52:55]\n"
"v_cvt_pk_bf16_f32 v214, v44, v45\n"
"v_cvt_pk_bf16_f32 v215, v46, v47\n"
"v_mfma_f32_16x16x32_bf16 v[56:59], a[48:51], v[178:181], 0\n"
"ds_write_b128 v24, v[212:215] offset:17408\n"
"v_mfma_f32_16x16x32_bf16 v[60:63], a[56:59], v[178:181], 0\n"
"v_cvt_pk_bf16_f32 v216, v48, v49\n"
"v_cvt_pk_bf16_f32 v217, v50, v51\n"
"v_mfma_f32_16x16x32_bf16 v[56:59], a[52:55], v[182:185], v[56:59]\n"
"v_cvt_pk_bf16_f32 v218, v52, v53\n"
"v_cvt_pk_bf16_f32 v219, v54, v55\n"
"v_mfma_f32_16x16x32_bf16 v[60:63], a[60:63], v[182:185], v[60:63]\n"
"ds_write_b128 v24, v[216:219] offset:18432\n"
"v_mfma_f32_16x16x16_bf16 v[56:59], a[196:197], v[186:187], v[56:59]\n"
"v_lshlrev_b32 v184, 2, v17\n"
"v_sub_u32 v184, v18, v184\n"
"v_mfma_f32_16x16x16_bf16 v[60:63], a[198:199], v[186:187], v[60:63]\n"
"v_and_b32 v185, 1, v184\n"
"v_lshlrev_b32 v185, 4, v185\n"
"v_mfma_f32_16x16x32_bf16 v[32:35], a[0:3], v[96:99], 0\n"
"v_mov_b32 v186, 0x3f80\n"
"v_lshlrev_b32 v186, v185, v186\n"
"v_mfma_f32_16x16x32_bf16 v[36:39], a[8:11], v[96:99], 0\n"
"v_cvt_pk_bf16_f32 v220, v56, v57\n"
"v_cvt_pk_bf16_f32 v221, v58, v59\n"
"v_mfma_f32_16x16x32_bf16 v[32:35], a[4:7], v[100:103], v[32:35]\n"
"v_cvt_pk_bf16_f32 v222, v60, v61\n"
"v_cvt_pk_bf16_f32 v223, v62, v63\n"
"v_mfma_f32_16x16x32_bf16 v[36:39], a[12:15], v[100:103], v[36:39]\n"
"ds_write_b128 v24, v[220:223] offset:19456\n"
"v_mfma_f32_16x16x16_bf16 v[32:35], a[184:185], v[104:105], v[32:35]\n"
"s_waitcnt lgkmcnt(0)\n"
"s_barrier\n"
"ds_read_b128 a[64:67], v21\n"
"v_lshrrev_b32 v187, 1, v184\n"
"v_mfma_f32_16x16x16_bf16 v[36:39], a[186:187], v[104:105], v[36:39]\n"
"ds_read_b128 a[68:71], v21 offset:16384\n"
"v_mfma_f32_16x16x32_bf16 v[40:43], a[16:19], v[96:99], 0\n"
"ds_read_b128 a[72:75], v22\n"
"v_mfma_f32_16x16x32_bf16 v[44:47], a[24:27], v[96:99], 0\n"
"ds_read_b128 a[76:79], v22 offset:16384\n"
"v_cvt_pk_bf16_f32 v224, v32, v33\n"
"v_mfma_f32_16x16x32_bf16 v[40:43], a[20:23], v[100:103], v[40:43]\n"
"ds_read_b128 a[80:83], v23\n"
"v_cvt_pk_bf16_f32 v225, v34, v35\n"
"v_mfma_f32_16x16x32_bf16 v[44:47], a[28:31], v[100:103], v[44:47]\n"
"ds_read_b128 a[84:87], v23 offset:16384\n"
"v_cvt_pk_bf16_f32 v226, v36, v37\n"
"v_mfma_f32_16x16x16_bf16 v[40:43], a[188:189], v[104:105], v[40:43]\n"
"ds_read_b128 a[88:91], v21 offset:1024\n"
"v_cvt_pk_bf16_f32 v227, v38, v39\n"
"v_mfma_f32_16x16x16_bf16 v[44:47], a[190:191], v[104:105], v[44:47]\n"
"ds_read_b128 a[92:95], v21 offset:17408\n"
"v_mfma_f32_16x16x32_bf16 v[48:51], a[32:35], v[96:99], 0\n"
"ds_read_b128 a[96:99], v22 offset:1024\n"
"v_mfma_f32_16x16x32_bf16 v[52:55], a[40:43], v[96:99], 0\n"
"ds_read_b128 a[100:103], v22 offset:17408\n"
"v_cvt_pk_bf16_f32 v228, v40, v41\n"
"v_mfma_f32_16x16x32_bf16 v[48:51], a[36:39], v[100:103], v[48:51]\n"
"ds_read_b128 a[104:107], v23 offset:1024\n"
"v_cvt_pk_bf16_f32 v229, v42, v43\n"
"v_mfma_f32_16x16x32_bf16 v[52:55], a[44:47], v[100:103], v[52:55]\n"
"ds_read_b128 a[108:111], v23 offset:17408\n"
"v_cvt_pk_bf16_f32 v230, v44, v45\n"
"v_mfma_f32_16x16x16_bf16 v[48:51], a[192:193], v[104:105], v[48:51]\n"
"ds_read_b128 a[112:115], v21 offset:2048\n"
"v_cvt_pk_bf16_f32 v231, v46, v47\n"
"v_mfma_f32_16x16x16_bf16 v[52:55], a[194:195], v[104:105], v[52:55]\n"
"ds_read_b128 a[116:119], v21 offset:18432\n"
"v_mfma_f32_16x16x32_bf16 v[56:59], a[48:51], v[96:99], 0\n"
"ds_read_b128 a[120:123], v22 offset:2048\n"
"v_mfma_f32_16x16x32_bf16 v[60:63], a[56:59], v[96:99], 0\n"
"ds_read_b128 a[124:127], v22 offset:18432\n"
"v_cvt_pk_bf16_f32 v232, v48, v49\n"
"v_mfma_f32_16x16x32_bf16 v[56:59], a[52:55], v[100:103], v[56:59]\n"
"ds_read_b128 a[128:131], v23 offset:2048\n"
"v_cvt_pk_bf16_f32 v233, v50, v51\n"
"v_mfma_f32_16x16x32_bf16 v[60:63], a[60:63], v[100:103], v[60:63]\n"
"ds_read_b128 a[132:135], v23 offset:18432\n"
"v_cvt_pk_bf16_f32 v234, v52, v53\n"
"v_mfma_f32_16x16x16_bf16 v[56:59], a[196:197], v[104:105], v[56:59]\n"
"ds_read_b128 a[136:139], v21 offset:3072\n"
"v_cvt_pk_bf16_f32 v235, v54, v55\n"
"v_mfma_f32_16x16x16_bf16 v[60:63], a[198:199], v[104:105], v[60:63]\n"
"ds_read_b128 a[140:143], v21 offset:19456\n"
"v_mfma_f32_16x16x32_bf16 v[32:35], a[0:3], v[106:109], 0\n"
"ds_read_b128 a[144:147], v22 offset:3072\n"
"v_mfma_f32_16x16x32_bf16 v[36:39], a[8:11], v[106:109], 0\n"
"ds_read_b128 a[148:151], v22 offset:19456\n"
"v_cvt_pk_bf16_f32 v236, v56, v57\n"
"v_mfma_f32_16x16x32_bf16 v[32:35], a[4:7], v[110:113], v[32:35]\n"
"ds_read_b128 a[152:155], v23 offset:3072\n"
"v_cvt_pk_bf16_f32 v237, v58, v59\n"
"v_mfma_f32_16x16x32_bf16 v[36:39], a[12:15], v[110:113], v[36:39]\n"
"ds_read_b128 a[156:159], v23 offset:19456\n"
"v_cvt_pk_bf16_f32 v238, v60, v61\n"
"v_mfma_f32_16x16x16_bf16 v[32:35], a[184:185], v[114:115], v[32:35]\n"
"ds_write_b128 v24, v[224:227] offset:32768\n"
"v_mfma_f32_16x16x16_bf16 v[36:39], a[186:187], v[114:115], v[36:39]\n"
"ds_write_b128 v24, v[228:231] offset:33792\n"
"v_mfma_f32_16x16x32_bf16 v[40:43], a[16:19], v[106:109], 0\n"
"ds_write_b128 v24, v[232:235] offset:34816\n"
"v_mfma_f32_16x16x32_bf16 v[44:47], a[24:27], v[106:109], 0\n"
"v_cvt_pk_bf16_f32 v239, v62, v63\n"
"v_mfma_f32_16x16x32_bf16 v[40:43], a[20:23], v[110:113], v[40:43]\n"
"ds_write_b128 v24, v[236:239] offset:35840\n"
"v_mfma_f32_16x16x32_bf16 v[44:47], a[28:31], v[110:113], v[44:47]\n"
"v_cvt_pk_bf16_f32 v240, v32, v33\n"
"v_cvt_pk_bf16_f32 v241, v34, v35\n"
"v_mfma_f32_16x16x16_bf16 v[40:43], a[188:189], v[114:115], v[40:43]\n"
"v_cvt_pk_bf16_f32 v242, v36, v37\n"
"v_cvt_pk_bf16_f32 v243, v38, v39\n"
"v_mfma_f32_16x16x16_bf16 v[44:47], a[190:191], v[114:115], v[44:47]\n"
"s_waitcnt vmcnt(6) lgkmcnt(0)\n"
"s_barrier\n"
"v_mfma_f32_16x16x32_bf16 v[48:51], a[32:35], v[106:109], 0\n"
"ds_write_b128 v24, v[240:243]\n"
"v_mfma_f32_16x16x32_bf16 v[52:55], a[40:43], v[106:109], 0\n"
"v_cmp_eq_u32 vcc, 0, v187\n"
"s_nop 1\n"
"v_cndmask_b32 v184, 0, v186, vcc\n"
"v_mfma_f32_16x16x32_bf16 v[48:51], a[36:39], v[110:113], v[48:51]\n"
"v_cvt_pk_bf16_f32 v244, v40, v41\n"
"v_cvt_pk_bf16_f32 v245, v42, v43\n"
"v_mfma_f32_16x16x32_bf16 v[52:55], a[44:47], v[110:113], v[52:55]\n"
"v_cvt_pk_bf16_f32 v246, v44, v45\n"
"v_cvt_pk_bf16_f32 v247, v46, v47\n"
"v_mfma_f32_16x16x16_bf16 v[48:51], a[192:193], v[114:115], v[48:51]\n"
"ds_write_b128 v24, v[244:247] offset:1024\n"
"v_mfma_f32_16x16x16_bf16 v[52:55], a[194:195], v[114:115], v[52:55]\n"
"v_cmp_eq_u32 vcc, 1, v187\n"
"s_nop 1\n"
"v_cndmask_b32 v185, 0, v186, vcc\n"
"v_mfma_f32_16x16x32_bf16 v[56:59], a[48:51], v[106:109], 0\n"
"v_mfma_f32_16x16x32_bf16 v[60:63], a[56:59], v[106:109], 0\n"
"v_mfma_f32_16x16x32_bf16 v[56:59], a[52:55], v[110:113], v[56:59]\n"
"v_cvt_pk_bf16_f32 v248, v48, v49\n"
"v_cvt_pk_bf16_f32 v249, v50, v51\n"
"v_mfma_f32_16x16x32_bf16 v[60:63], a[60:63], v[110:113], v[60:63]\n"
"v_cvt_pk_bf16_f32 v250, v52, v53\n"
"v_cvt_pk_bf16_f32 v251, v54, v55\n"
"v_mfma_f32_16x16x16_bf16 v[56:59], a[196:197], v[114:115], v[56:59]\n"
"ds_write_b128 v24, v[248:251] offset:2048\n"
"v_mfma_f32_16x16x16_bf16 v[60:63], a[198:199], v[114:115], v[60:63]\n"
"v_mfma_f32_16x16x32_bf16 v[32:35], a[0:3], v[116:119], 0\n"
"v_mfma_f32_16x16x32_bf16 v[36:39], a[8:11], v[116:119], 0\n"
"v_mfma_f32_16x16x32_bf16 v[32:35], a[4:7], v[120:123], v[32:35]\n"
"v_mfma_f32_16x16x32_bf16 v[36:39], a[12:15], v[120:123], v[36:39]\n"
"v_mfma_f32_16x16x16_bf16 v[32:35], a[184:185], v[124:125], v[32:35]\n"
"v_mfma_f32_16x16x16_bf16 v[36:39], a[186:187], v[124:125], v[36:39]\n"
"v_cvt_pk_bf16_f32 v252, v56, v57\n"
"v_cvt_pk_bf16_f32 v253, v58, v59\n"
"v_mfma_f32_16x16x32_bf16 v[40:43], a[16:19], v[116:119], 0\n"
"buffer_store_dwordx4 v[12:15], v0, s[44:47], s52 offen offset:512 sc1\n"
"v_mfma_f32_16x16x32_bf16 v[44:47], a[24:27], v[116:119], 0\n"
"buffer_store_dwordx4 v[12:15], v0, s[44:47], s52 offen offset:1536 sc1\n"
"v_mfma_f32_16x16x32_bf16 v[40:43], a[20:23], v[120:123], v[40:43]\n"
"buffer_store_dwordx4 v[12:15], v0, s[44:47], s52 offen offset:2560 sc1\n"
"v_mfma_f32_16x16x32_bf16 v[44:47], a[28:31], v[120:123], v[44:47]\n"
"v_cvt_pk_bf16_f32 v254, v60, v61\n"
"v_cvt_pk_bf16_f32 v255, v62, v63\n"
"v_mfma_f32_16x16x16_bf16 v[40:43], a[188:189], v[124:125], v[40:43]\n"
"ds_write_b128 v24, v[252:255] offset:3072\n"
"v_mfma_f32_16x16x16_bf16 v[44:47], a[190:191], v[124:125], v[44:47]\n"
"buffer_store_dwordx4 v[12:15], v0, s[44:47], s52 offen offset:3584 sc1\n"
"v_mfma_f32_16x16x32_bf16 v[48:51], a[32:35], v[116:119], 0\n"
"ds_read_b128 a[160:163], v21 offset:32768\n"
"v_cvt_pk_bf16_f32 v64, v32, v33\n"
"v_mfma_f32_16x16x32_bf16 v[52:55], a[40:43], v[116:119], 0\n"
"ds_read_b128 a[164:167], v21 offset:33792\n"
"v_cvt_pk_bf16_f32 v65, v34, v35\n"
"v_mfma_f32_16x16x32_bf16 v[48:51], a[36:39], v[120:123], v[48:51]\n"
"ds_read_b128 a[168:171], v21 offset:34816\n"
"v_cvt_pk_bf16_f32 v66, v36, v37\n"
"v_mfma_f32_16x16x32_bf16 v[52:55], a[44:47], v[120:123], v[52:55]\n"
"ds_read_b128 a[172:175], v21 offset:35840\n"
"v_cvt_pk_bf16_f32 v67, v38, v39\n"
"v_mfma_f32_16x16x16_bf16 v[48:51], a[192:193], v[124:125], v[48:51]\n"
"ds_read_b128 a[176:179], v22 offset:32768\n"
"v_cvt_pk_bf16_f32 v68, v40, v41\n"
"v_mfma_f32_16x16x16_bf16 v[52:55], a[194:195], v[124:125], v[52:55]\n"
"ds_read_b128 a[180:183], v22 offset:33792\n"
"v_cvt_pk_bf16_f32 v69, v42, v43\n"
"v_mfma_f32_16x16x32_bf16 v[56:59], a[48:51], v[116:119], 0\n"
"buffer_store_dwordx4 v[12:15], v0, s[44:47], s53 offen offset:512 sc1\n"
"v_mfma_f32_16x16x32_bf16 v[60:63], a[56:59], v[116:119], 0\n"
"buffer_load_dwordx4 v8, s[24:27], s72 offen lds\n"
"v_mfma_f32_16x16x32_bf16 v[56:59], a[52:55], v[120:123], v[56:59]\n"
"ds_read_b128 a[200:203], v22 offset:34816\n"
"v_cvt_pk_bf16_f32 v70, v44, v45\n"
"v_mfma_f32_16x16x32_bf16 v[60:63], a[60:63], v[120:123], v[60:63]\n"
"ds_read_b128 a[204:207], v22 offset:35840\n"
"v_cvt_pk_bf16_f32 v71, v46, v47\n"
"v_mfma_f32_16x16x16_bf16 v[56:59], a[196:197], v[124:125], v[56:59]\n"
"ds_read_b128 a[208:211], v23 offset:32768\n"
"v_cvt_pk_bf16_f32 v72, v48, v49\n"
"v_mfma_f32_16x16x16_bf16 v[60:63], a[198:199], v[124:125], v[60:63]\n"
"ds_read_b128 a[212:215], v23 offset:33792\n"
"ds_read_b128 a[216:219], v23 offset:34816\n"
"ds_read_b128 a[220:223], v23 offset:35840\n"
"ds_read_b64_tr_b16 v[80:81], v92\n"
"ds_read_b64_tr_b16 v[82:83], v92 offset:512\n"
"buffer_load_dwordx4 v9, s[24:27], s72 offen offset:1024 lds\n"
"ds_read_b64_tr_b16 v[84:85], v92 offset:16384\n"
"ds_read_b64_tr_b16 v[86:87], v92 offset:16896\n"
"buffer_load_dwordx4 v10, s[24:27], s72 offen offset:2048 lds\n"
"ds_read_b64_tr_b16 v[88:89], v92 offset:32768\n"
"ds_read_b64_tr_b16 v[90:91], v92 offset:33280\n"
"ds_read_b64_tr_b16 v[168:169], v92 offset:49152\n"
"ds_read_b64_tr_b16 v[170:171], v92 offset:49664\n"
"ds_read_b64_tr_b16 v[172:173], v93\n"
"ds_read_b64_tr_b16 v[174:175], v93 offset:512\n"
"ds_read_b64_tr_b16 v[176:177], v93 offset:16384\n"
"ds_read_b64_tr_b16 v[178:179], v93 offset:16896\n"
"ds_read_b64_tr_b16 v[180:181], v93 offset:32768\n"
"ds_read_b64_tr_b16 v[182:183], v93 offset:33280\n"
"ds_write_b128 v24, v[64:67] offset:16384\n"
"ds_write_b128 v24, v[68:71] offset:17408\n"
"v_cvt_pk_bf16_f32 v73, v50, v51\n"
"v_cvt_pk_bf16_f32 v74, v52, v53\n"
"v_cvt_pk_bf16_f32 v75, v54, v55\n"
"ds_write_b128 v24, v[72:75] offset:18432\n"
"s_waitcnt lgkmcnt(15)\n"
"v_mfma_f32_16x16x32_bf16 v[128:131], v[80:83], v[192:195], 0\n"
"buffer_load_dwordx4 v11, s[24:27], s72 offen offset:3072 lds\n"
"v_mfma_f32_16x16x32_bf16 v[132:135], v[80:83], v[208:211], 0\n"
"s_add_u32 m0, s69, 147456\n"
"s_add_u32 s72, s55, 65536\n"
"v_cvt_pk_bf16_f32 v76, v56, v57\n"
"v_mfma_f32_16x16x32_bf16 v[136:139], v[80:83], v[224:227], 0\n"
"buffer_load_dwordx4 v8, s[24:27], s72 offen lds\n"
"v_mfma_f32_16x16x32_bf16 v[140:143], v[80:83], v[240:243], 0\n"
"v_cvt_pk_bf16_f32 v77, v58, v59\n"
"v_cvt_pk_bf16_f32 v78, v60, v61\n"
"v_mfma_f32_16x16x32_bf16 v[144:147], v[80:83], v[64:67], 0\n"
"s_waitcnt lgkmcnt(13)\n"
"v_cvt_pk_bf16_f32 v79, v62, v63\n"
"v_add_u32 v92, 16384, v92\n"
"v_mfma_f32_16x16x32_bf16 v[128:131], v[84:87], v[196:199], v[128:131]\n"
"ds_write_b128 v24, v[76:79] offset:19456\n"
"v_mfma_f32_16x16x32_bf16 v[132:135], v[84:87], v[212:215], v[132:135]\n"
"s_waitcnt lgkmcnt(0)\n"
"s_barrier\n"
"ds_read_b128 a[224:227], v21\n"
"v_mfma_f32_16x16x32_bf16 v[136:139], v[84:87], v[228:231], v[136:139]\n"
"buffer_load_dwordx4 v9, s[24:27], s72 offen offset:1024 lds\n"
"v_mfma_f32_16x16x32_bf16 v[140:143], v[84:87], v[244:247], v[140:143]\n"
"buffer_load_dwordx4 v10, s[24:27], s72 offen offset:2048 lds\n"
"v_mfma_f32_16x16x32_bf16 v[144:147], v[84:87], v[68:71], v[144:147]\n"
"ds_read_b128 a[228:231], v21 offset:16384\n"
"ds_read_b64_tr_b16 a[184:185], v93 offset:49152\n"
"v_mfma_f32_16x16x32_bf16 v[128:131], v[88:91], v[200:203], v[128:131]\n"
"ds_read_b64_tr_b16 a[186:187], v93 offset:49664\n"
"ds_read_b128 a[232:235], v21 offset:1024\n"
"v_mfma_f32_16x16x32_bf16 v[132:135], v[88:91], v[216:219], v[132:135]\n"
"buffer_load_dwordx4 v11, s[24:27], s72 offen offset:3072 lds\n"
"v_mfma_f32_16x16x32_bf16 v[136:139], v[88:91], v[232:235], v[136:139]\n"
"s_add_u32 m0, s69, 114688\n"
"s_add_u32 s72, s55, 98304\n"
"ds_read_b128 a[236:239], v21 offset:17408\n"
"v_mfma_f32_16x16x32_bf16 v[140:143], v[88:91], v[248:251], v[140:143]\n"
"ds_read_b64_tr_b16 a[188:189], v94\n"
"ds_read_b64_tr_b16 a[190:191], v94 offset:512\n"
"v_mfma_f32_16x16x32_bf16 v[144:147], v[88:91], v[72:75], v[144:147]\n"
"ds_read_b128 a[240:243], v21 offset:2048\n"
"v_add_u32 v93, 16384, v93\n"
"v_mfma_f32_16x16x32_bf16 v[128:131], v[168:171], v[204:207], v[128:131]\n"
"buffer_load_dwordx4 v8, s[24:27], s72 offen lds\n"
"v_mfma_f32_16x16x32_bf16 v[132:135], v[168:171], v[220:223], v[132:135]\n"
"ds_read_b128 a[244:247], v21 offset:18432\n"
"ds_read_b64_tr_b16 a[192:193], v94 offset:16384\n"
"v_mfma_f32_16x16x32_bf16 v[136:139], v[168:171], v[236:239], v[136:139]\n"
"ds_read_b64_tr_b16 a[194:195], v94 offset:16896\n"
"ds_read_b128 a[248:251], v21 offset:3072\n"
"v_mfma_f32_16x16x32_bf16 v[140:143], v[168:171], v[252:255], v[140:143]\n"
"ds_read_b128 a[252:255], v21 offset:19456\n"
"v_mfma_f32_16x16x32_bf16 v[144:147], v[168:171], v[76:79], v[144:147]\n"
"ds_read_b128 a[0:3], v22\n"
"v_mfma_f32_16x16x32_bf16 v[128:131], v[172:175], a[64:67], v[128:131]\n"
"buffer_load_dwordx4 v9, s[24:27], s72 offen offset:1024 lds\n"
"v_mfma_f32_16x16x32_bf16 v[132:135], v[172:175], a[68:71], v[132:135]\n"
"ds_read_b128 a[4:7], v22 offset:16384\n"
"v_mfma_f32_16x16x32_bf16 v[136:139], v[172:175], a[160:163], v[136:139]\n"
"s_waitcnt lgkmcnt(12)\n"
"ds_read_b128 a[8:11], v22 offset:1024\n"
"v_mfma_f32_16x16x32_bf16 v[140:143], v[172:175], a[224:227], v[140:143]\n"
"ds_read_b128 a[12:15], v22 offset:17408\n"
"v_mfma_f32_16x16x32_bf16 v[144:147], v[172:175], a[228:231], v[144:147]\n"
"buffer_load_dwordx4 v10, s[24:27], s72 offen offset:2048 lds\n"
"v_mfma_f32_16x16x32_bf16 v[128:131], v[176:179], a[88:91], v[128:131]\n"
"ds_read_b128 a[16:19], v22 offset:2048\n"
"v_mfma_f32_16x16x32_bf16 v[132:135], v[176:179], a[92:95], v[132:135]\n"
"ds_read_b128 a[20:23], v22 offset:18432\n"
"v_mfma_f32_16x16x32_bf16 v[136:139], v[176:179], a[164:167], v[136:139]\n"
"s_waitcnt lgkmcnt(11)\n"
"ds_read_b128 a[24:27], v22 offset:3072\n"
"v_mfma_f32_16x16x32_bf16 v[140:143], v[176:179], a[232:235], v[140:143]\n"
"buffer_load_dwordx4 v11, s[24:27], s72 offen offset:3072 lds\n"
"v_mfma_f32_16x16x32_bf16 v[144:147], v[176:179], a[236:239], v[144:147]\n"
"s_add_u32 m0, s69, 81920\n"
"ds_read_b128 a[28:31], v22 offset:19456\n"
"s_add_u32 s72, s56, 32768\n"
"v_mfma_f32_16x16x32_bf16 v[128:131], v[180:183], a[112:115], v[128:131]\n"
"ds_read_b128 a[32:35], v23\n"
"v_mfma_f32_16x16x32_bf16 v[132:135], v[180:183], a[116:119], v[132:135]\n"
"ds_read_b128 a[36:39], v23 offset:16384\n"
"v_mfma_f32_16x16x32_bf16 v[136:139], v[180:183], a[168:171], v[136:139]\n"
"ds_read_b128 a[40:43], v23 offset:1024\n"
"v_mfma_f32_16x16x32_bf16 v[140:143], v[180:183], a[240:243], v[140:143]\n"
"s_waitcnt lgkmcnt(9)\n"
"ds_read_b128 a[44:47], v23 offset:17408\n"
"v_mfma_f32_16x16x32_bf16 v[144:147], v[180:183], a[244:247], v[144:147]\n"
"ds_read_b128 a[48:51], v23 offset:2048\n"
"v_mfma_f32_16x16x32_bf16 v[128:131], a[184:187], a[136:139], v[128:131]\n"
"ds_read_b128 a[52:55], v23 offset:18432\n"
"v_mfma_f32_16x16x32_bf16 v[132:135], a[184:187], a[140:143], v[132:135]\n"
"ds_read_b128 a[56:59], v23 offset:3072\n"
"v_mfma_f32_16x16x32_bf16 v[136:139], a[184:187], a[172:175], v[136:139]\n"
"ds_read_b128 a[60:63], v23 offset:19456\n"
"ds_read_b64_tr_b16 a[196:197], v94 offset:32768\n"
"v_mfma_f32_16x16x32_bf16 v[140:143], a[184:187], a[248:251], v[140:143]\n"
"ds_read_b64_tr_b16 a[198:199], v94 offset:33280\n"
"ds_read_b64_tr_b16 v[80:81], v94 offset:49152\n"
"v_mfma_f32_16x16x32_bf16 v[144:147], a[184:187], a[252:255], v[144:147]\n"
"ds_read_b64_tr_b16 v[82:83], v94 offset:49664\n"
"ds_read_b64_tr_b16 v[84:85], v95\n"
"v_mfma_f32_16x16x32_bf16 v[128:131], a[188:191], a[72:75], v[128:131]\n"
"ds_read_b64_tr_b16 v[86:87], v95 offset:512\n"
"ds_read_b64_tr_b16 v[88:89], v95 offset:16384\n"
"v_mfma_f32_16x16x32_bf16 v[132:135], a[188:191], a[76:79], v[132:135]\n"
"ds_read_b64_tr_b16 v[90:91], v95 offset:16896\n"
"ds_read_b64_tr_b16 v[168:169], v95 offset:32768\n"
"v_mfma_f32_16x16x32_bf16 v[136:139], a[188:191], a[176:179], v[136:139]\n"
"ds_read_b64_tr_b16 v[170:171], v95 offset:33280\n"
"ds_read_b64_tr_b16 v[172:173], v95 offset:49152\n"
"v_mfma_f32_16x16x32_bf16 v[140:143], a[188:191], a[0:3], v[140:143]\n"
"ds_read_b64_tr_b16 v[174:175], v95 offset:49664\n"
"s_waitcnt lgkmcnt(0)\n"
"s_barrier\n"
"v_mfma_f32_16x16x32_bf16 v[144:147], a[188:191], a[4:7], v[144:147]\n"
"buffer_load_dwordx4 v8, s[24:27], s56 offen lds\n"
"v_mfma_f32_16x16x32_bf16 v[128:131], a[192:195], a[96:99], v[128:131]\n"
"buffer_load_dwordx4 v9, s[24:27], s56 offen offset:1024 lds\n"
"v_mfma_f32_16x16x32_bf16 v[132:135], a[192:195], a[100:103], v[132:135]\n"
"buffer_load_dwordx4 v10, s[24:27], s56 offen offset:2048 lds\n"
"v_mfma_f32_16x16x32_bf16 v[136:139], a[192:195], a[180:183], v[136:139]\n"
"buffer_load_dwordx4 v11, s[24:27], s56 offen offset:3072 lds\n"
"v_mfma_f32_16x16x32_bf16 v[140:143], a[192:195], a[8:11], v[140:143]\n"
"s_add_u32 m0, s69, 98304\n"
"v_add_u32 v94, 16384, v94\n"
"v_add_u32 v95, 16384, v95\n"
"v_mfma_f32_16x16x32_bf16 v[144:147], a[192:195], a[12:15], v[144:147]\n"
"buffer_load_dwordx4 v8, s[24:27], s72 offen lds\n"
"v_mfma_f32_16x16x32_bf16 v[128:131], a[196:199], a[120:123], v[128:131]\n"
"v_mfma_f32_16x16x32_bf16 v[132:135], a[196:199], a[124:127], v[132:135]\n"
"v_mfma_f32_16x16x32_bf16 v[136:139], a[196:199], a[200:203], v[136:139]\n"
"v_mfma_f32_16x16x32_bf16 v[140:143], a[196:199], a[16:19], v[140:143]\n"
"buffer_load_dwordx4 v9, s[24:27], s72 offen offset:1024 lds\n"
"v_mfma_f32_16x16x32_bf16 v[144:147], a[196:199], a[20:23], v[144:147]\n"
"v_mfma_f32_16x16x32_bf16 v[128:131], v[80:83], a[144:147], v[128:131]\n"
"v_mfma_f32_16x16x32_bf16 v[132:135], v[80:83], a[148:151], v[132:135]\n"
"v_mfma_f32_16x16x32_bf16 v[136:139], v[80:83], a[204:207], v[136:139]\n"
"buffer_load_dwordx4 v10, s[24:27], s72 offen offset:2048 lds\n"
"v_mfma_f32_16x16x32_bf16 v[140:143], v[80:83], a[24:27], v[140:143]\n"
"v_mfma_f32_16x16x32_bf16 v[144:147], v[80:83], a[28:31], v[144:147]\n"
"v_mfma_f32_16x16x32_bf16 v[128:131], v[84:87], a[80:83], v[128:131]\n"
"v_mfma_f32_16x16x32_bf16 v[132:135], v[84:87], a[84:87], v[132:135]\n"
"v_mfma_f32_16x16x32_bf16 v[136:139], v[84:87], a[208:211], v[136:139]\n"
"v_mfma_f32_16x16x32_bf16 v[140:143], v[84:87], a[32:35], v[140:143]\n"
"buffer_load_dwordx4 v11, s[24:27], s72 offen offset:3072 lds\n"
"v_mfma_f32_16x16x32_bf16 v[144:147], v[84:87], a[36:39], v[144:147]\n"
"s_add_u32 m0, s69, 0\n"
"s_add_u32 s72, s56, 65536\n"
"v_mfma_f32_16x16x32_bf16 v[128:131], v[88:91], a[104:107], v[128:131]\n"
"v_mfma_f32_16x16x32_bf16 v[132:135], v[88:91], a[108:111], v[132:135]\n"
"v_mfma_f32_16x16x32_bf16 v[136:139], v[88:91], a[212:215], v[136:139]\n"
"v_mfma_f32_16x16x32_bf16 v[140:143], v[88:91], a[40:43], v[140:143]\n"
"v_mfma_f32_16x16x32_bf16 v[144:147], v[88:91], a[44:47], v[144:147]\n"
"s_waitcnt vmcnt(8)\n"
"s_barrier\n"
"buffer_load_dwordx4 v8, s[24:27], s72 offen lds\n"
"v_mfma_f32_16x16x32_bf16 v[128:131], v[168:171], a[128:131], v[128:131]\n"
"ds_read_b64_tr_b16 v[176:177], v92 offset:49152\n"
"ds_read_b64_tr_b16 v[178:179], v92 offset:49664\n"
"v_mfma_f32_16x16x32_bf16 v[132:135], v[168:171], a[132:135], v[132:135]\n"
"buffer_load_dwordx4 v9, s[24:27], s72 offen offset:1024 lds\n"
"v_mfma_f32_16x16x32_bf16 v[136:139], v[168:171], a[216:219], v[136:139]\n"
"ds_read_b64_tr_b16 v[180:181], v93 offset:49152\n"
"ds_read_b64_tr_b16 v[182:183], v93 offset:49664\n"
"v_mfma_f32_16x16x32_bf16 v[140:143], v[168:171], a[48:51], v[140:143]\n"
"ds_read_b64_tr_b16 a[184:185], v94 offset:49152\n"
"ds_read_b64_tr_b16 a[186:187], v94 offset:49664\n"
"v_mfma_f32_16x16x32_bf16 v[144:147], v[168:171], a[52:55], v[144:147]\n"
"ds_read_b64_tr_b16 a[188:189], v95 offset:49152\n"
"ds_read_b64_tr_b16 a[190:191], v95 offset:49664\n"
"v_mfma_f32_16x16x32_bf16 v[128:131], v[172:175], a[152:155], v[128:131]\n"
"v_add_u32 v92, 81920, v92\n"
"ds_read_b64_tr_b16 a[192:193], v92 offset:32768\n"
"v_mfma_f32_16x16x32_bf16 v[132:135], v[172:175], a[156:159], v[132:135]\n"
"buffer_load_dwordx4 v10, s[24:27], s72 offen offset:2048 lds\n"
"v_mfma_f32_16x16x32_bf16 v[136:139], v[172:175], a[220:223], v[136:139]\n"
"ds_read_b64_tr_b16 a[194:195], v92 offset:33280\n"
"v_add_u32 v93, 81920, v93\n"
"v_mfma_f32_16x16x32_bf16 v[140:143], v[172:175], a[56:59], v[140:143]\n"
"v_cvt_pk_bf16_f32 v96, v128, v129\n"
"v_cvt_pk_bf16_f32 v97, v130, v131\n"
"v_mfma_f32_16x16x32_bf16 v[144:147], v[172:175], a[60:63], v[144:147]\n"
"v_cvt_pk_bf16_f32 v98, v132, v133\n"
"v_cvt_pk_bf16_f32 v99, v134, v135\n"
"s_waitcnt lgkmcnt(8)\n"
"v_mfma_f32_16x16x32_bf16 v[148:151], v[176:179], v[192:195], 0\n"
"buffer_load_dwordx4 v11, s[24:27], s72 offen offset:3072 lds\n"
"v_mfma_f32_16x16x32_bf16 v[152:155], v[176:179], v[208:211], 0\n"
"s_add_u32 m0, s69, 16384\n"
"s_add_u32 s72, s56, 98304\n"
"v_cvt_pk_bf16_f32 v100, v136, v137\n"
"v_mfma_f32_16x16x32_bf16 v[156:159], v[176:179], v[224:227], 0\n"
"v_cvt_pk_bf16_f32 v104, v144, v145\n"
"v_cvt_pk_bf16_f32 v105, v146, v147\n"
"v_mfma_f32_16x16x32_bf16 v[160:163], v[176:179], v[240:243], 0\n"
"v_cvt_pk_bf16_f32 v101, v138, v139\n"
"v_cvt_pk_bf16_f32 v102, v140, v141\n"
"v_mfma_f32_16x16x32_bf16 v[164:167], v[176:179], v[64:67], 0\n"
"s_waitcnt lgkmcnt(6)\n"
"buffer_load_dwordx4 v8, s[24:27], s72 offen lds\n"
"v_mfma_f32_16x16x32_bf16 v[148:151], v[180:183], a[64:67], v[148:151]\n"
"v_cvt_pk_bf16_f32 v103, v142, v143\n"
"ds_read_b64_tr_b16 a[196:197], v93 offset:32768\n"
"v_mfma_f32_16x16x32_bf16 v[152:155], v[180:183], a[68:71], v[152:155]\n"
"v_permlane32_swap_b32 v104, v106\n"
"v_mfma_f32_16x16x32_bf16 v[156:159], v[180:183], a[160:163], v[156:159]\n"
"v_permlane32_swap_b32 v105, v107\n"
"v_mfma_f32_16x16x32_bf16 v[160:163], v[180:183], a[224:227], v[160:163]\n"
"buffer_load_dwordx4 v9, s[24:27], s72 offen offset:1024 lds\n"
"v_mfma_f32_16x16x32_bf16 v[164:167], v[180:183], a[228:231], v[164:167]\n"
"s_waitcnt lgkmcnt(1)\n"
"v_add_u32 v94, 81920, v94\n"
"ds_read_b64_tr_b16 a[198:199], v93 offset:33280\n"
"v_mfma_f32_16x16x32_bf16 v[148:151], a[184:187], a[72:75], v[148:151]\n"
"ds_read_b64_tr_b16 v[80:81], v94 offset:32768\n"
"v_add_u32 v95, 81920, v95\n"
"v_mfma_f32_16x16x32_bf16 v[152:155], a[184:187], a[76:79], v[152:155]\n"
"ds_read_b64_tr_b16 v[82:83], v94 offset:33280\n"
"ds_read_b64_tr_b16 v[84:85], v95 offset:32768\n"
"v_mfma_f32_16x16x32_bf16 v[156:159], a[184:187], a[176:179], v[156:159]\n"
"buffer_load_dwordx4 v10, s[24:27], s72 offen offset:2048 lds\n"
"v_mfma_f32_16x16x32_bf16 v[160:163], a[184:187], a[0:3], v[160:163]\n"
"ds_read_b64_tr_b16 v[86:87], v95 offset:33280\n"
"ds_read_b64_tr_b16 v[88:89], v92 offset:49152\n"
"v_mfma_f32_16x16x32_bf16 v[164:167], a[184:187], a[4:7], v[164:167]\n"
"ds_read_b64_tr_b16 v[90:91], v92 offset:49664\n"
"ds_read_b64_tr_b16 v[168:169], v93 offset:49152\n"
"v_mfma_f32_16x16x32_bf16 v[148:151], a[188:191], a[80:83], v[148:151]\n"
"ds_read_b64_tr_b16 v[170:171], v93 offset:49664\n"
"ds_read_b64_tr_b16 v[172:173], v94 offset:49152\n"
"v_mfma_f32_16x16x32_bf16 v[152:155], a[188:191], a[84:87], v[152:155]\n"
"buffer_load_dwordx4 v11, s[24:27], s72 offen offset:3072 lds\n"
"v_mfma_f32_16x16x32_bf16 v[156:159], a[188:191], a[208:211], v[156:159]\n"
"s_add_u32 m0, s69, 32768\n"
"s_add_u32 s72, s57, 32768\n"
"ds_read_b64_tr_b16 v[174:175], v94 offset:49664\n"
"v_mfma_f32_16x16x32_bf16 v[160:163], a[188:191], a[32:35], v[160:163]\n"
"ds_read_b64_tr_b16 v[176:177], v95 offset:49152\n"
"ds_read_b64_tr_b16 v[178:179], v95 offset:49664\n"
"v_mfma_f32_16x16x32_bf16 v[164:167], a[188:191], a[36:39], v[164:167]\n"
"ds_read_b64_tr_b16 v[180:181], v92 offset:16384\n"
"ds_read_b64_tr_b16 v[182:183], v92 offset:16896\n"
"v_mfma_f32_16x16x32_bf16 v[148:151], a[192:195], v[196:199], v[148:151]\n"
"ds_read_b64_tr_b16 a[184:185], v93 offset:16384\n"
"ds_read_b64_tr_b16 a[186:187], v93 offset:16896\n"
"v_mfma_f32_16x16x32_bf16 v[152:155], a[192:195], v[212:215], v[152:155]\n"
"ds_read_b64_tr_b16 a[188:189], v94 offset:16384\n"
"ds_read_b64_tr_b16 a[190:191], v94 offset:16896\n"
"v_mfma_f32_16x16x32_bf16 v[156:159], a[192:195], v[228:231], v[156:159]\n"
"v_add_u32 v92, 4294918144, v92\n"
"v_add_u32 v93, 4294918144, v93\n"
"v_mfma_f32_16x16x32_bf16 v[160:163], a[192:195], v[244:247], v[160:163]\n"
"v_add_u32 v94, 4294918144, v94\n"
"v_mfma_f32_16x16x32_bf16 v[164:167], a[192:195], v[68:71], v[164:167]\n"
"s_waitcnt lgkmcnt(15)\n"
"buffer_store_dwordx4 v[96:99], v0, s[28:31], s62 offen sc1\n"
"v_mfma_f32_16x16x32_bf16 v[148:151], a[196:199], a[88:91], v[148:151]\n"
"ds_read_b64_tr_b16 a[192:193], v95 offset:16384\n"
"ds_read_b64_tr_b16 a[194:195], v95 offset:16896\n"
"v_mfma_f32_16x16x32_bf16 v[152:155], a[196:199], a[92:95], v[152:155]\n"
"v_add_u32 v95, 4294918144, v95\n"
"v_mfma_f32_16x16x32_bf16 v[156:159], a[196:199], a[164:167], v[156:159]\n"
"v_mfma_f32_16x16x32_bf16 v[160:163], a[196:199], a[232:235], v[160:163]\n"
"v_mfma_f32_16x16x32_bf16 v[164:167], a[196:199], a[236:239], v[164:167]\n"
"v_mfma_f32_16x16x32_bf16 v[148:151], v[80:83], a[96:99], v[148:151]\n"
"v_mfma_f32_16x16x32_bf16 v[152:155], v[80:83], a[100:103], v[152:155]\n"
"v_mfma_f32_16x16x32_bf16 v[156:159], v[80:83], a[180:183], v[156:159]\n"
"buffer_store_dwordx4 v[100:103], v0, s[28:31], s62 offen offset:1024 sc1\n"
"v_mfma_f32_16x16x32_bf16 v[160:163], v[80:83], a[8:11], v[160:163]\n"
"v_mfma_f32_16x16x32_bf16 v[164:167], v[80:83], a[12:15], v[164:167]\n"
"s_waitcnt lgkmcnt(2)\n"
"v_mfma_f32_16x16x32_bf16 v[148:151], v[84:87], a[104:107], v[148:151]\n"
"buffer_store_dwordx4 v[104:107], v1, s[28:31], s62 offen offset:2048 sc1\n"
"v_mfma_f32_16x16x32_bf16 v[152:155], v[84:87], a[108:111], v[152:155]\n"
"v_mfma_f32_16x16x32_bf16 v[156:159], v[84:87], a[212:215], v[156:159]\n"
"v_mfma_f32_16x16x32_bf16 v[160:163], v[84:87], a[40:43], v[160:163]\n"
"v_mfma_f32_16x16x32_bf16 v[164:167], v[84:87], a[44:47], v[164:167]\n"
"buffer_load_dwordx4 v8, s[24:27], s57 offen lds\n"
"v_mfma_f32_16x16x32_bf16 v[148:151], v[88:91], v[200:203], v[148:151]\n"
"v_mfma_f32_16x16x32_bf16 v[152:155], v[88:91], v[216:219], v[152:155]\n"
"v_mfma_f32_16x16x32_bf16 v[156:159], v[88:91], v[232:235], v[156:159]\n"
"v_mfma_f32_16x16x32_bf16 v[160:163], v[88:91], v[248:251], v[160:163]\n"
"buffer_load_dwordx4 v9, s[24:27], s57 offen offset:1024 lds\n"
"v_mfma_f32_16x16x32_bf16 v[164:167], v[88:91], v[72:75], v[164:167]\n"
"v_mfma_f32_16x16x32_bf16 v[148:151], v[168:171], a[112:115], v[148:151]\n"
"v_mfma_f32_16x16x32_bf16 v[152:155], v[168:171], a[116:119], v[152:155]\n"
"v_mfma_f32_16x16x32_bf16 v[156:159], v[168:171], a[168:171], v[156:159]\n"
"v_mfma_f32_16x16x32_bf16 v[160:163], v[168:171], a[240:243], v[160:163]\n"
"v_mfma_f32_16x16x32_bf16 v[164:167], v[168:171], a[244:247], v[164:167]\n"
"v_mfma_f32_16x16x32_bf16 v[148:151], v[172:175], a[120:123], v[148:151]\n"
"v_mfma_f32_16x16x32_bf16 v[152:155], v[172:175], a[124:127], v[152:155]\n"
"buffer_load_dwordx4 v10, s[24:27], s57 offen offset:2048 lds\n"
"v_mfma_f32_16x16x32_bf16 v[156:159], v[172:175], a[200:203], v[156:159]\n"
"buffer_load_dwordx4 v11, s[24:27], s57 offen offset:3072 lds\n"
"v_mfma_f32_16x16x32_bf16 v[160:163], v[172:175], a[16:19], v[160:163]\n"
"s_add_u32 m0, s69, 49152\n"
"v_mfma_f32_16x16x32_bf16 v[164:167], v[172:175], a[20:23], v[164:167]\n"
"v_mfma_f32_16x16x32_bf16 v[148:151], v[176:179], a[128:131], v[148:151]\n"
"v_mfma_f32_16x16x32_bf16 v[152:155], v[176:179], a[132:135], v[152:155]\n"
"s_waitcnt vmcnt(7) lgkmcnt(0)\n"
"s_barrier\n"
"ds_read_b64_tr_b16 a[196:197], v92 offset:32768\n"
"ds_read_b64_tr_b16 a[198:199], v92 offset:33280\n"
"v_mfma_f32_16x16x32_bf16 v[156:159], v[176:179], a[216:219], v[156:159]\n"
"ds_read_b64_tr_b16 v[80:81], v93 offset:32768\n"
"ds_read_b64_tr_b16 v[82:83], v93 offset:33280\n"
"v_mfma_f32_16x16x32_bf16 v[160:163], v[176:179], a[48:51], v[160:163]\n"
"ds_read_b64_tr_b16 v[84:85], v94 offset:32768\n"
"ds_read_b64_tr_b16 v[86:87], v94 offset:33280\n"
"v_mfma_f32_16x16x32_bf16 v[164:167], v[176:179], a[52:55], v[164:167]\n"
"buffer_load_dwordx4 v8, s[24:27], s72 offen lds\n"
"v_mfma_f32_16x16x32_bf16 v[148:151], v[180:183], v[204:207], v[148:151]\n"
"buffer_load_dwordx4 v9, s[24:27], s72 offen offset:1024 lds\n"
"v_mfma_f32_16x16x32_bf16 v[152:155], v[180:183], v[220:223], v[152:155]\n"
"ds_read_b64_tr_b16 v[88:89], v95 offset:32768\n"
"ds_read_b64_tr_b16 v[90:91], v95 offset:33280\n"
"v_mfma_f32_16x16x32_bf16 v[156:159], v[180:183], v[236:239], v[156:159]\n"
"ds_read_b64_tr_b16 v[168:169], v92 offset:49152\n"
"ds_read_b64_tr_b16 v[170:171], v92 offset:49664\n"
"v_mfma_f32_16x16x32_bf16 v[160:163], v[180:183], v[252:255], v[160:163]\n"
"buffer_load_dwordx4 v10, s[24:27], s72 offen offset:2048 lds\n"
"v_mfma_f32_16x16x32_bf16 v[164:167], v[180:183], v[76:79], v[164:167]\n"
"ds_read_b64_tr_b16 v[172:173], v93 offset:49152\n"
"ds_read_b64_tr_b16 v[174:175], v93 offset:49664\n"
"v_mfma_f32_16x16x32_bf16 v[148:151], a[184:187], a[136:139], v[148:151]\n"
"ds_read_b64_tr_b16 v[176:177], v94 offset:49152\n"
"ds_read_b64_tr_b16 v[178:179], v94 offset:49664\n"
"v_mfma_f32_16x16x32_bf16 v[152:155], a[184:187], a[140:143], v[152:155]\n"
"ds_read_b64_tr_b16 v[180:181], v95 offset:49152\n"
"ds_read_b64_tr_b16 v[182:183], v95 offset:49664\n"
"v_mfma_f32_16x16x32_bf16 v[156:159], a[184:187], a[172:175], v[156:159]\n"
"v_add_u32 v92, 4294918144, v92\n"
"v_add_u32 v93, 4294918144, v93\n"
"v_mfma_f32_16x16x32_bf16 v[160:163], a[184:187], a[248:251], v[160:163]\n"
"buffer_load_dwordx4 v11, s[24:27], s72 offen offset:3072 lds\n"
"v_mfma_f32_16x16x32_bf16 v[164:167], a[184:187], a[252:255], v[164:167]\n"
"s_add_u32 m0, s69, 65536\n"
"s_add_u32 s72, s57, 65536\n"
"ds_read_b64_tr_b16 a[184:185], v92\n"
"v_mfma_f32_16x16x32_bf16 v[148:151], a[188:191], a[144:147], v[148:151]\n"
"ds_read_b64_tr_b16 a[186:187], v92 offset:512\n"
"v_add_u32 v94, 4294918144, v94\n"
"v_mfma_f32_16x16x32_bf16 v[152:155], a[188:191], a[148:151], v[152:155]\n"
"v_add_u32 v95, 4294918144, v95\n"
"v_mfma_f32_16x16x32_bf16 v[156:159], a[188:191], a[204:207], v[156:159]\n"
"buffer_load_dwordx4 v8, s[24:27], s72 offen lds\n"
"v_mfma_f32_16x16x32_bf16 v[160:163], a[188:191], a[24:27], v[160:163]\n"
"v_mfma_f32_16x16x32_bf16 v[164:167], a[188:191], a[28:31], v[164:167]\n"
"ds_read_b64_tr_b16 a[188:189], v93\n"
"ds_read_b64_tr_b16 a[190:191], v93 offset:512\n"
"v_mfma_f32_16x16x32_bf16 v[148:151], a[192:195], a[152:155], v[148:151]\n"
"v_mfma_f32_16x16x32_bf16 v[152:155], a[192:195], a[156:159], v[152:155]\n"
"buffer_load_dwordx4 v9, s[24:27], s72 offen offset:1024 lds\n"
"v_mfma_f32_16x16x32_bf16 v[156:159], a[192:195], a[220:223], v[156:159]\n"
"v_mfma_f32_16x16x32_bf16 v[160:163], a[192:195], a[56:59], v[160:163]\n"
"v_mfma_f32_16x16x32_bf16 v[164:167], a[192:195], a[60:63], v[164:167]\n"
"ds_read_b64_tr_b16 a[192:193], v94\n"
"ds_read_b64_tr_b16 a[194:195], v94 offset:512\n"
"s_waitcnt lgkmcnt(6)\n"
"v_mfma_f32_16x16x32_bf16 v[128:131], a[196:199], v[192:195], 0\n"
"buffer_load_dwordx4 v10, s[24:27], s72 offen offset:2048 lds\n"
"v_mfma_f32_16x16x32_bf16 v[132:135], a[196:199], v[208:211], 0\n"
"v_cvt_pk_bf16_f32 v96, v148, v149\n"
"v_cvt_pk_bf16_f32 v97, v150, v151\n"
"v_mfma_f32_16x16x32_bf16 v[136:139], a[196:199], v[224:227], 0\n"
"v_cvt_pk_bf16_f32 v104, v164, v165\n"
"v_cvt_pk_bf16_f32 v105, v166, v167\n"
"v_mfma_f32_16x16x32_bf16 v[140:143], a[196:199], v[240:243], 0\n"
"v_cvt_pk_bf16_f32 v98, v152, v153\n"
"v_cvt_pk_bf16_f32 v99, v154, v155\n"
"v_mfma_f32_16x16x32_bf16 v[144:147], a[196:199], v[64:67], 0\n"
"buffer_load_dwordx4 v11, s[24:27], s72 offen offset:3072 lds\n"
"v_mfma_f32_16x16x32_bf16 v[128:131], v[80:83], a[64:67], v[128:131]\n"
"s_add_u32 m0, s69, 131072\n"
"s_add_u32 s72, s57, 98304\n"
"v_cvt_pk_bf16_f32 v100, v156, v157\n"
"v_mfma_f32_16x16x32_bf16 v[132:135], v[80:83], a[68:71], v[132:135]\n"
"v_cvt_pk_bf16_f32 v101, v158, v159\n"
"v_cvt_pk_bf16_f32 v102, v160, v161\n"
"v_mfma_f32_16x16x32_bf16 v[136:139], v[80:83], a[160:163], v[136:139]\n"
"v_cvt_pk_bf16_f32 v103, v162, v163\n"
"ds_read_b64_tr_b16 a[196:197], v95\n"
"v_mfma_f32_16x16x32_bf16 v[140:143], v[80:83], a[224:227], v[140:143]\n"
"buffer_load_dwordx4 v8, s[24:27], s72 offen lds\n"
"v_mfma_f32_16x16x32_bf16 v[144:147], v[80:83], a[228:231], v[144:147]\n"
"v_permlane32_swap_b32 v104, v106\n"
"v_mfma_f32_16x16x32_bf16 v[128:131], v[84:87], a[72:75], v[128:131]\n"
"v_permlane32_swap_b32 v105, v107\n"
"v_mfma_f32_16x16x32_bf16 v[132:135], v[84:87], a[76:79], v[132:135]\n"
"ds_read_b64_tr_b16 a[198:199], v95 offset:512\n"
"ds_read_b64_tr_b16 v[80:81], v92 offset:16384\n"
"v_mfma_f32_16x16x32_bf16 v[136:139], v[84:87], a[176:179], v[136:139]\n"
"buffer_load_dwordx4 v9, s[24:27], s72 offen offset:1024 lds\n"
"v_mfma_f32_16x16x32_bf16 v[140:143], v[84:87], a[0:3], v[140:143]\n"
"ds_read_b64_tr_b16 v[82:83], v92 offset:16896\n"
"v_mfma_f32_16x16x32_bf16 v[144:147], v[84:87], a[4:7], v[144:147]\n"
"ds_read_b64_tr_b16 v[84:85], v93 offset:16384\n"
"ds_read_b64_tr_b16 v[86:87], v93 offset:16896\n"
"v_mfma_f32_16x16x32_bf16 v[128:131], v[88:91], a[80:83], v[128:131]\n"
"v_mfma_f32_16x16x32_bf16 v[132:135], v[88:91], a[84:87], v[132:135]\n"
"buffer_load_dwordx4 v10, s[24:27], s72 offen offset:2048 lds\n"
"v_mfma_f32_16x16x32_bf16 v[136:139], v[88:91], a[208:211], v[136:139]\n"
"v_mfma_f32_16x16x32_bf16 v[140:143], v[88:91], a[32:35], v[140:143]\n"
"v_mfma_f32_16x16x32_bf16 v[144:147], v[88:91], a[36:39], v[144:147]\n"
"ds_read_b64_tr_b16 v[88:89], v94 offset:16384\n"
"ds_read_b64_tr_b16 v[90:91], v94 offset:16896\n"
"v_mfma_f32_16x16x32_bf16 v[128:131], v[168:171], v[196:199], v[128:131]\n"
"v_mfma_f32_16x16x32_bf16 v[132:135], v[168:171], v[212:215], v[132:135]\n"
"buffer_load_dwordx4 v11, s[24:27], s72 offen offset:3072 lds\n"
"v_mfma_f32_16x16x32_bf16 v[136:139], v[168:171], v[228:231], v[136:139]\n"
"s_add_u32 m0, s69, 147456\n"
"s_add_u32 s72, s58, 32768\n"
"v_mfma_f32_16x16x32_bf16 v[140:143], v[168:171], v[244:247], v[140:143]\n"
"v_mfma_f32_16x16x32_bf16 v[144:147], v[168:171], v[68:71], v[144:147]\n"
"ds_read_b64_tr_b16 v[168:169], v95 offset:16384\n"
"ds_read_b64_tr_b16 v[170:171], v95 offset:16896\n"
"v_mfma_f32_16x16x32_bf16 v[128:131], v[172:175], a[88:91], v[128:131]\n"
"v_mfma_f32_16x16x32_bf16 v[132:135], v[172:175], a[92:95], v[132:135]\n"
"v_mfma_f32_16x16x32_bf16 v[136:139], v[172:175], a[164:167], v[136:139]\n"
"v_mfma_f32_16x16x32_bf16 v[140:143], v[172:175], a[232:235], v[140:143]\n"
"v_mfma_f32_16x16x32_bf16 v[144:147], v[172:175], a[236:239], v[144:147]\n"
"buffer_store_dwordx4 v[96:99], v0, s[28:31], s63 offen sc1\n"
"v_mfma_f32_16x16x32_bf16 v[128:131], v[176:179], a[96:99], v[128:131]\n"
"v_mfma_f32_16x16x32_bf16 v[132:135], v[176:179], a[100:103], v[132:135]\n"
"v_mfma_f32_16x16x32_bf16 v[136:139], v[176:179], a[180:183], v[136:139]\n"
"v_mfma_f32_16x16x32_bf16 v[140:143], v[176:179], a[8:11], v[140:143]\n"
"v_mfma_f32_16x16x32_bf16 v[144:147], v[176:179], a[12:15], v[144:147]\n"
"v_mfma_f32_16x16x32_bf16 v[128:131], v[180:183], a[104:107], v[128:131]\n"
"v_mfma_f32_16x16x32_bf16 v[132:135], v[180:183], a[108:111], v[132:135]\n"
"buffer_store_dwordx4 v[100:103], v0, s[28:31], s63 offen offset:1024 sc1\n"
"v_mfma_f32_16x16x32_bf16 v[136:139], v[180:183], a[212:215], v[136:139]\n"
"v_mfma_f32_16x16x32_bf16 v[140:143], v[180:183], a[40:43], v[140:143]\n"
"v_mfma_f32_16x16x32_bf16 v[144:147], v[180:183], a[44:47], v[144:147]\n"
"s_waitcnt lgkmcnt(0)\n"
"v_mfma_f32_16x16x32_bf16 v[128:131], a[184:187], v[200:203], v[128:131]\n"
"buffer_store_dwordx4 v[104:107], v1, s[28:31], s63 offen offset:2048 sc1\n"
"v_mfma_f32_16x16x32_bf16 v[132:135], a[184:187], v[216:219], v[132:135]\n"
"s_waitcnt vmcnt(19)\n"
"buffer_store_dword v31, v191, s[36:39], s71 offen offset:128 sc1\n"
"v_mfma_f32_16x16x32_bf16 v[136:139], a[184:187], v[232:235], v[136:139]\n"
"s_lshl_b32 s71, s55, 1\n"
"v_mfma_f32_16x16x32_bf16 v[140:143], a[184:187], v[248:251], v[140:143]\n"
"buffer_load_dwordx4 v8, s[24:27], s58 offen lds\n"
"v_mfma_f32_16x16x32_bf16 v[144:147], a[184:187], v[72:75], v[144:147]\n"
"v_mfma_f32_16x16x32_bf16 v[128:131], a[188:191], a[112:115], v[128:131]\n"
"v_mfma_f32_16x16x32_bf16 v[132:135], a[188:191], a[116:119], v[132:135]\n"
"v_mfma_f32_16x16x32_bf16 v[136:139], a[188:191], a[168:171], v[136:139]\n"
"buffer_load_dwordx4 v9, s[24:27], s58 offen offset:1024 lds\n"
"v_mfma_f32_16x16x32_bf16 v[140:143], a[188:191], a[240:243], v[140:143]\n"
"v_mfma_f32_16x16x32_bf16 v[144:147], a[188:191], a[244:247], v[144:147]\n"
"v_mfma_f32_16x16x32_bf16 v[128:131], a[192:195], a[120:123], v[128:131]\n"
"v_mfma_f32_16x16x32_bf16 v[132:135], a[192:195], a[124:127], v[132:135]\n"
"v_mfma_f32_16x16x32_bf16 v[136:139], a[192:195], a[200:203], v[136:139]\n"
"v_mfma_f32_16x16x32_bf16 v[140:143], a[192:195], a[16:19], v[140:143]\n"
"v_mfma_f32_16x16x32_bf16 v[144:147], a[192:195], a[20:23], v[144:147]\n"
"v_mfma_f32_16x16x32_bf16 v[128:131], a[196:199], a[128:131], v[128:131]\n"
"buffer_load_dwordx4 v10, s[24:27], s58 offen offset:2048 lds\n"
"v_mfma_f32_16x16x32_bf16 v[132:135], a[196:199], a[132:135], v[132:135]\n"
"buffer_load_dwordx4 v11, s[24:27], s58 offen offset:3072 lds\n"
"v_mfma_f32_16x16x32_bf16 v[136:139], a[196:199], a[216:219], v[136:139]\n"
"s_add_u32 m0, s69, 114688\n"
"v_mfma_f32_16x16x32_bf16 v[140:143], a[196:199], a[48:51], v[140:143]\n"
"v_mfma_f32_16x16x32_bf16 v[144:147], a[196:199], a[52:55], v[144:147]\n"
"v_mfma_f32_16x16x32_bf16 v[128:131], v[80:83], v[204:207], v[128:131]\n"
"s_waitcnt vmcnt(8)\n"
"s_barrier\n"
"ds_read_b64_tr_b16 v[172:173], v92 offset:32768\n"
"ds_read_b64_tr_b16 v[174:175], v92 offset:33280\n"
"v_mfma_f32_16x16x32_bf16 v[132:135], v[80:83], v[220:223], v[132:135]\n"
"ds_read_b64_tr_b16 v[176:177], v93 offset:32768\n"
"ds_read_b64_tr_b16 v[178:179], v93 offset:33280\n"
"v_mfma_f32_16x16x32_bf16 v[136:139], v[80:83], v[236:239], v[136:139]\n"
"ds_read_b64_tr_b16 v[180:181], v94 offset:32768\n"
"ds_read_b64_tr_b16 v[182:183], v94 offset:33280\n"
"v_mfma_f32_16x16x32_bf16 v[140:143], v[80:83], v[252:255], v[140:143]\n"
"ds_read_b64_tr_b16 a[184:185], v95 offset:32768\n"
"ds_read_b64_tr_b16 a[186:187], v95 offset:33280\n"
"v_mfma_f32_16x16x32_bf16 v[144:147], v[80:83], v[76:79], v[144:147]\n"
"buffer_load_dwordx4 v8, s[24:27], s72 offen lds\n"
"v_mfma_f32_16x16x32_bf16 v[128:131], v[84:87], a[136:139], v[128:131]\n"
"buffer_load_dwordx4 v9, s[24:27], s72 offen offset:1024 lds\n"
"v_mfma_f32_16x16x32_bf16 v[132:135], v[84:87], a[140:143], v[132:135]\n"
"ds_read_b64_tr_b16 a[188:189], v92 offset:49152\n"
"ds_read_b64_tr_b16 a[190:191], v92 offset:49664\n"
"v_mfma_f32_16x16x32_bf16 v[136:139], v[84:87], a[172:175], v[136:139]\n"
"ds_read_b64_tr_b16 a[192:193], v93 offset:49152\n"
"ds_read_b64_tr_b16 a[194:195], v93 offset:49664\n"
"v_mfma_f32_16x16x32_bf16 v[140:143], v[84:87], a[248:251], v[140:143]\n"
"buffer_load_dwordx4 v10, s[24:27], s72 offen offset:2048 lds\n"
"v_mfma_f32_16x16x32_bf16 v[144:147], v[84:87], a[252:255], v[144:147]\n"
"ds_read_b64_tr_b16 a[196:197], v94 offset:49152\n"
"ds_read_b64_tr_b16 a[198:199], v94 offset:49664\n"
"v_mfma_f32_16x16x32_bf16 v[128:131], v[88:91], a[144:147], v[128:131]\n"
"ds_read_b64_tr_b16 v[80:81], v95 offset:49152\n"
"ds_read_b64_tr_b16 v[82:83], v95 offset:49664\n"
"v_mfma_f32_16x16x32_bf16 v[132:135], v[88:91], a[148:151], v[132:135]\n"
"v_add_u32 v92, 16384, v92\n"
"ds_read_b64_tr_b16 v[84:85], v92 offset:49152\n"
"v_mfma_f32_16x16x32_bf16 v[136:139], v[88:91], a[204:207], v[136:139]\n"
"buffer_load_dwordx4 v11, s[24:27], s72 offen offset:3072 lds\n"
"v_mfma_f32_16x16x32_bf16 v[140:143], v[88:91], a[24:27], v[140:143]\n"
"s_add_u32 m0, s69, 81920\n"
"s_add_u32 s72, s58, 65536\n"
"v_add_u32 v93, 16384, v93\n"
"v_mfma_f32_16x16x32_bf16 v[144:147], v[88:91], a[28:31], v[144:147]\n"
"ds_read_b64_tr_b16 v[86:87], v92 offset:49664\n"
"ds_read_b64_tr_b16 v[88:89], v93 offset:49152\n"
"v_mfma_f32_16x16x32_bf16 v[128:131], v[168:171], a[152:155], v[128:131]\n"
"v_add_u32 v94, 16384, v94\n"
"ds_read_b64_tr_b16 v[90:91], v93 offset:49664\n"
"v_mfma_f32_16x16x32_bf16 v[132:135], v[168:171], a[156:159], v[132:135]\n"
"v_add_u32 v95, 16384, v95\n"
"v_add_u32 v92, 81920, v92\n"
"v_mfma_f32_16x16x32_bf16 v[136:139], v[168:171], a[220:223], v[136:139]\n"
"buffer_load_dwordx4 v8, s[24:27], s72 offen lds\n"
"v_mfma_f32_16x16x32_bf16 v[140:143], v[168:171], a[56:59], v[140:143]\n"
"v_cvt_pk_bf16_f32 v96, v128, v129\n"
"v_cvt_pk_bf16_f32 v97, v130, v131\n"
"v_mfma_f32_16x16x32_bf16 v[144:147], v[168:171], a[60:63], v[144:147]\n"
"s_waitcnt lgkmcnt(9)\n"
"v_cvt_pk_bf16_f32 v98, v132, v133\n"
"v_cvt_pk_bf16_f32 v99, v134, v135\n"
"v_mfma_f32_16x16x32_bf16 v[148:151], v[172:175], v[192:195], 0\n"
"v_cvt_pk_bf16_f32 v100, v136, v137\n"
"v_cvt_pk_bf16_f32 v101, v138, v139\n"
"v_mfma_f32_16x16x32_bf16 v[152:155], v[172:175], v[208:211], 0\n"
"buffer_load_dwordx4 v9, s[24:27], s72 offen offset:1024 lds\n"
"v_mfma_f32_16x16x32_bf16 v[156:159], v[172:175], v[224:227], 0\n"
"v_cvt_pk_bf16_f32 v104, v144, v145\n"
"v_cvt_pk_bf16_f32 v105, v146, v147\n"
"v_mfma_f32_16x16x32_bf16 v[160:163], v[172:175], v[240:243], 0\n"
"v_cvt_pk_bf16_f32 v102, v140, v141\n"
"v_cvt_pk_bf16_f32 v103, v142, v143\n"
"v_mfma_f32_16x16x32_bf16 v[164:167], v[172:175], v[64:67], 0\n"
"v_permlane32_swap_b32 v104, v106\n"
"v_mfma_f32_16x16x32_bf16 v[148:151], v[176:179], a[64:67], v[148:151]\n"
"buffer_load_dwordx4 v10, s[24:27], s72 offen offset:2048 lds\n"
"v_mfma_f32_16x16x32_bf16 v[152:155], v[176:179], a[68:71], v[152:155]\n"
"v_permlane32_swap_b32 v105, v107\n"
"v_mfma_f32_16x16x32_bf16 v[156:159], v[176:179], a[160:163], v[156:159]\n"
"ds_read_b64_tr_b16 v[168:169], v94 offset:49152\n"
"ds_read_b64_tr_b16 v[170:171], v94 offset:49664\n"
"v_mfma_f32_16x16x32_bf16 v[160:163], v[176:179], a[224:227], v[160:163]\n"
"ds_read_b64_tr_b16 v[172:173], v95 offset:49152\n"
"ds_read_b64_tr_b16 v[174:175], v95 offset:49664\n"
"v_mfma_f32_16x16x32_bf16 v[164:167], v[176:179], a[228:231], v[164:167]\n"
"buffer_load_dwordx4 v11, s[24:27], s72 offen offset:3072 lds\n"
"v_mfma_f32_16x16x32_bf16 v[148:151], v[180:183], a[72:75], v[148:151]\n"
"s_add_u32 m0, s69, 98304\n"
"s_add_u32 s72, s58, 98304\n"
"ds_read_b64_tr_b16 v[176:177], v92 offset:32768\n"
"v_mfma_f32_16x16x32_bf16 v[152:155], v[180:183], a[76:79], v[152:155]\n"
"v_add_u32 v93, 81920, v93\n"
"ds_read_b64_tr_b16 v[178:179], v92 offset:33280\n"
"v_mfma_f32_16x16x32_bf16 v[156:159], v[180:183], a[176:179], v[156:159]\n"
"v_add_u32 v94, 81920, v94\n"
"v_add_u32 v95, 81920, v95\n"
"v_mfma_f32_16x16x32_bf16 v[160:163], v[180:183], a[0:3], v[160:163]\n"
"buffer_load_dwordx4 v8, s[24:27], s72 offen lds\n"
"v_mfma_f32_16x16x32_bf16 v[164:167], v[180:183], a[4:7], v[164:167]\n"
"ds_read_b64_tr_b16 v[180:181], v93 offset:32768\n"
"ds_read_b64_tr_b16 v[182:183], v93 offset:33280\n"
"v_mfma_f32_16x16x32_bf16 v[148:151], a[184:187], a[80:83], v[148:151]\n"
"v_mfma_f32_16x16x32_bf16 v[152:155], a[184:187], a[84:87], v[152:155]\n"
"v_mfma_f32_16x16x32_bf16 v[156:159], a[184:187], a[208:211], v[156:159]\n"
"buffer_load_dwordx4 v9, s[24:27], s72 offen offset:1024 lds\n"
"v_mfma_f32_16x16x32_bf16 v[160:163], a[184:187], a[32:35], v[160:163]\n"
"v_mfma_f32_16x16x32_bf16 v[164:167], a[184:187], a[36:39], v[164:167]\n"
"ds_read_b64_tr_b16 a[184:185], v94 offset:32768\n"
"ds_read_b64_tr_b16 a[186:187], v94 offset:33280\n"
"v_mfma_f32_16x16x32_bf16 v[148:151], a[188:191], v[196:199], v[148:151]\n"
"v_mfma_f32_16x16x32_bf16 v[152:155], a[188:191], v[212:215], v[152:155]\n"
"buffer_load_dwordx4 v10, s[24:27], s72 offen offset:2048 lds\n"
"v_mfma_f32_16x16x32_bf16 v[156:159], a[188:191], v[228:231], v[156:159]\n"
"v_mfma_f32_16x16x32_bf16 v[160:163], a[188:191], v[244:247], v[160:163]\n"
"v_mfma_f32_16x16x32_bf16 v[164:167], a[188:191], v[68:71], v[164:167]\n"
"s_waitcnt lgkmcnt(4)\n"
"ds_read_b64_tr_b16 a[188:189], v95 offset:32768\n"
"ds_read_b64_tr_b16 a[190:191], v95 offset:33280\n"
"v_mfma_f32_16x16x32_bf16 v[148:151], a[192:195], a[88:91], v[148:151]\n"
"buffer_load_dwordx4 v11, s[24:27], s72 offen offset:3072 lds\n"
"v_mfma_f32_16x16x32_bf16 v[152:155], a[192:195], a[92:95], v[152:155]\n"
"s_add_u32 m0, s69, 0\n"
"s_add_u32 s72, s59, 32768\n"
"v_mfma_f32_16x16x32_bf16 v[156:159], a[192:195], a[164:167], v[156:159]\n"
"v_mfma_f32_16x16x32_bf16 v[160:163], a[192:195], a[232:235], v[160:163]\n"
"v_mfma_f32_16x16x32_bf16 v[164:167], a[192:195], a[236:239], v[164:167]\n"
"v_mfma_f32_16x16x32_bf16 v[148:151], a[196:199], a[96:99], v[148:151]\n"
"v_mfma_f32_16x16x32_bf16 v[152:155], a[196:199], a[100:103], v[152:155]\n"
"v_mfma_f32_16x16x32_bf16 v[156:159], a[196:199], a[180:183], v[156:159]\n"
"v_mfma_f32_16x16x32_bf16 v[160:163], a[196:199], a[8:11], v[160:163]\n"
"v_mfma_f32_16x16x32_bf16 v[164:167], a[196:199], a[12:15], v[164:167]\n"
"buffer_store_dwordx4 v[96:99], v0, s[28:31], s64 offen sc1\n"
"v_mfma_f32_16x16x32_bf16 v[148:151], v[80:83], a[104:107], v[148:151]\n"
"v_mfma_f32_16x16x32_bf16 v[152:155], v[80:83], a[108:111], v[152:155]\n"
"v_mfma_f32_16x16x32_bf16 v[156:159], v[80:83], a[212:215], v[156:159]\n"
"v_mfma_f32_16x16x32_bf16 v[160:163], v[80:83], a[40:43], v[160:163]\n"
"v_mfma_f32_16x16x32_bf16 v[164:167], v[80:83], a[44:47], v[164:167]\n"
"v_mfma_f32_16x16x32_bf16 v[148:151], v[84:87], v[200:203], v[148:151]\n"
"v_mfma_f32_16x16x32_bf16 v[152:155], v[84:87], v[216:219], v[152:155]\n"
"buffer_store_dwordx4 v[100:103], v0, s[28:31], s64 offen offset:1024 sc1\n"
"v_mfma_f32_16x16x32_bf16 v[156:159], v[84:87], v[232:235], v[156:159]\n"
"v_mfma_f32_16x16x32_bf16 v[160:163], v[84:87], v[248:251], v[160:163]\n"
"v_mfma_f32_16x16x32_bf16 v[164:167], v[84:87], v[72:75], v[164:167]\n"
"v_mfma_f32_16x16x32_bf16 v[148:151], v[88:91], a[112:115], v[148:151]\n"
"buffer_store_dwordx4 v[104:107], v1, s[28:31], s64 offen offset:2048 sc1\n"
"v_mfma_f32_16x16x32_bf16 v[152:155], v[88:91], a[116:119], v[152:155]\n"
"s_waitcnt vmcnt(20) lgkmcnt(0)\n"
"buffer_store_dword v31, v191, s[36:39], s71 offen offset:132 sc1\n"
"v_mfma_f32_16x16x32_bf16 v[156:159], v[88:91], a[168:171], v[156:159]\n"
"s_lshl_b32 s71, s56, 1\n"
"v_mfma_f32_16x16x32_bf16 v[160:163], v[88:91], a[240:243], v[160:163]\n"
"buffer_load_dwordx4 v8, s[24:27], s59 offen lds\n"
"v_mfma_f32_16x16x32_bf16 v[164:167], v[88:91], a[244:247], v[164:167]\n"
"v_mfma_f32_16x16x32_bf16 v[148:151], v[168:171], a[120:123], v[148:151]\n"
"v_mfma_f32_16x16x32_bf16 v[152:155], v[168:171], a[124:127], v[152:155]\n"
"v_mfma_f32_16x16x32_bf16 v[156:159], v[168:171], a[200:203], v[156:159]\n"
"buffer_load_dwordx4 v9, s[24:27], s59 offen offset:1024 lds\n"
"v_mfma_f32_16x16x32_bf16 v[160:163], v[168:171], a[16:19], v[160:163]\n"
"v_mfma_f32_16x16x32_bf16 v[164:167], v[168:171], a[20:23], v[164:167]\n"
"v_mfma_f32_16x16x32_bf16 v[148:151], v[172:175], a[128:131], v[148:151]\n"
"v_mfma_f32_16x16x32_bf16 v[152:155], v[172:175], a[132:135], v[152:155]\n"
"v_mfma_f32_16x16x32_bf16 v[156:159], v[172:175], a[216:219], v[156:159]\n"
"v_mfma_f32_16x16x32_bf16 v[160:163], v[172:175], a[48:51], v[160:163]\n"
"v_mfma_f32_16x16x32_bf16 v[164:167], v[172:175], a[52:55], v[164:167]\n"
"v_mfma_f32_16x16x32_bf16 v[148:151], v[176:179], v[204:207], v[148:151]\n"
"buffer_load_dwordx4 v10, s[24:27], s59 offen offset:2048 lds\n"
"v_mfma_f32_16x16x32_bf16 v[152:155], v[176:179], v[220:223], v[152:155]\n"
"buffer_load_dwordx4 v11, s[24:27], s59 offen offset:3072 lds\n"
"v_mfma_f32_16x16x32_bf16 v[156:159], v[176:179], v[236:239], v[156:159]\n"
"s_add_u32 m0, s69, 16384\n"
"v_mfma_f32_16x16x32_bf16 v[160:163], v[176:179], v[252:255], v[160:163]\n"
"v_mfma_f32_16x16x32_bf16 v[164:167], v[176:179], v[76:79], v[164:167]\n"
"v_mfma_f32_16x16x32_bf16 v[148:151], v[180:183], a[136:139], v[148:151]\n"
"s_waitcnt vmcnt(8)\n"
"s_barrier\n"
"ds_read_b64_tr_b16 a[192:193], v92 offset:49152\n"
"ds_read_b64_tr_b16 a[194:195], v92 offset:49664\n"
"v_mfma_f32_16x16x32_bf16 v[152:155], v[180:183], a[140:143], v[152:155]\n"
"ds_read_b64_tr_b16 a[196:197], v93 offset:49152\n"
"ds_read_b64_tr_b16 a[198:199], v93 offset:49664\n"
"v_mfma_f32_16x16x32_bf16 v[156:159], v[180:183], a[172:175], v[156:159]\n"
"ds_read_b64_tr_b16 v[80:81], v94 offset:49152\n"
"ds_read_b64_tr_b16 v[82:83], v94 offset:49664\n"
"v_mfma_f32_16x16x32_bf16 v[160:163], v[180:183], a[248:251], v[160:163]\n"
"ds_read_b64_tr_b16 v[84:85], v95 offset:49152\n"
"ds_read_b64_tr_b16 v[86:87], v95 offset:49664\n"
"v_mfma_f32_16x16x32_bf16 v[164:167], v[180:183], a[252:255], v[164:167]\n"
"buffer_load_dwordx4 v8, s[24:27], s72 offen lds\n"
"v_mfma_f32_16x16x32_bf16 v[148:151], a[184:187], a[144:147], v[148:151]\n"
"buffer_load_dwordx4 v9, s[24:27], s72 offen offset:1024 lds\n"
"v_mfma_f32_16x16x32_bf16 v[152:155], a[184:187], a[148:151], v[152:155]\n"
"ds_read_b64_tr_b16 v[88:89], v92 offset:16384\n"
"ds_read_b64_tr_b16 v[90:91], v92 offset:16896\n"
"v_mfma_f32_16x16x32_bf16 v[156:159], a[184:187], a[204:207], v[156:159]\n"
"ds_read_b64_tr_b16 v[168:169], v93 offset:16384\n"
"ds_read_b64_tr_b16 v[170:171], v93 offset:16896\n"
"v_mfma_f32_16x16x32_bf16 v[160:163], a[184:187], a[24:27], v[160:163]\n"
"buffer_load_dwordx4 v10, s[24:27], s72 offen offset:2048 lds\n"
"v_mfma_f32_16x16x32_bf16 v[164:167], a[184:187], a[28:31], v[164:167]\n"
"ds_read_b64_tr_b16 v[172:173], v94 offset:16384\n"
"ds_read_b64_tr_b16 v[174:175], v94 offset:16896\n"
"v_mfma_f32_16x16x32_bf16 v[148:151], a[188:191], a[152:155], v[148:151]\n"
"ds_read_b64_tr_b16 v[176:177], v95 offset:16384\n"
"ds_read_b64_tr_b16 v[178:179], v95 offset:16896\n"
"v_mfma_f32_16x16x32_bf16 v[152:155], a[188:191], a[156:159], v[152:155]\n"
"v_add_u32 v92, 4294918144, v92\n"
"ds_read_b64_tr_b16 v[180:181], v92 offset:32768\n"
"v_mfma_f32_16x16x32_bf16 v[156:159], a[188:191], a[220:223], v[156:159]\n"
"buffer_load_dwordx4 v11, s[24:27], s72 offen offset:3072 lds\n"
"v_mfma_f32_16x16x32_bf16 v[160:163], a[188:191], a[56:59], v[160:163]\n"
"s_add_u32 m0, s69, 32768\n"
"s_add_u32 s72, s59, 65536\n"
"v_cvt_pk_bf16_f32 v96, v148, v149\n"
"v_mfma_f32_16x16x32_bf16 v[164:167], a[188:191], a[60:63], v[164:167]\n"
"s_waitcnt lgkmcnt(12)\n"
"v_cvt_pk_bf16_f32 v97, v150, v151\n"
"v_cvt_pk_bf16_f32 v98, v152, v153\n"
"v_mfma_f32_16x16x32_bf16 v[128:131], a[192:195], v[192:195], 0\n"
"v_cvt_pk_bf16_f32 v99, v154, v155\n"
"v_cvt_pk_bf16_f32 v100, v156, v157\n"
"v_mfma_f32_16x16x32_bf16 v[132:135], a[192:195], v[208:211], 0\n"
"buffer_load_dwordx4 v8, s[24:27], s72 offen lds\n"
"v_mfma_f32_16x16x32_bf16 v[136:139], a[192:195], v[224:227], 0\n"
"v_cvt_pk_bf16_f32 v104, v164, v165\n"
"v_cvt_pk_bf16_f32 v105, v166, v167\n"
"v_mfma_f32_16x16x32_bf16 v[140:143], a[192:195], v[240:243], 0\n"
"v_cvt_pk_bf16_f32 v101, v158, v159\n"
"v_cvt_pk_bf16_f32 v102, v160, v161\n"
"v_mfma_f32_16x16x32_bf16 v[144:147], a[192:195], v[64:67], 0\n"
"v_cvt_pk_bf16_f32 v103, v162, v163\n"
"v_add_u32 v93, 4294918144, v93\n"
"v_mfma_f32_16x16x32_bf16 v[128:131], a[196:199], a[64:67], v[128:131]\n"
"v_permlane32_swap_b32 v104, v106\n"
"v_mfma_f32_16x16x32_bf16 v[132:135], a[196:199], a[68:71], v[132:135]\n"
"buffer_load_dwordx4 v9, s[24:27], s72 offen offset:1024 lds\n"
"v_mfma_f32_16x16x32_bf16 v[136:139], a[196:199], a[160:163], v[136:139]\n"
"v_permlane32_swap_b32 v105, v107\n"
"v_mfma_f32_16x16x32_bf16 v[140:143], a[196:199], a[224:227], v[140:143]\n"
"ds_read_b64_tr_b16 v[182:183], v92 offset:33280\n"
"ds_read_b64_tr_b16 a[184:185], v93 offset:32768\n"
"v_mfma_f32_16x16x32_bf16 v[144:147], a[196:199], a[228:231], v[144:147]\n"
"s_waitcnt lgkmcnt(2)\n"
"v_add_u32 v94, 4294918144, v94\n"
"ds_read_b64_tr_b16 a[186:187], v93 offset:33280\n"
"v_mfma_f32_16x16x32_bf16 v[128:131], v[80:83], a[72:75], v[128:131]\n"
"buffer_load_dwordx4 v10, s[24:27], s72 offen offset:2048 lds\n"
"v_mfma_f32_16x16x32_bf16 v[132:135], v[80:83], a[76:79], v[132:135]\n"
"ds_read_b64_tr_b16 a[188:189], v94 offset:32768\n"
"v_add_u32 v95, 4294918144, v95\n"
"v_mfma_f32_16x16x32_bf16 v[136:139], v[80:83], a[176:179], v[136:139]\n"
"ds_read_b64_tr_b16 a[190:191], v94 offset:33280\n"
"ds_read_b64_tr_b16 a[192:193], v95 offset:32768\n"
"v_mfma_f32_16x16x32_bf16 v[140:143], v[80:83], a[0:3], v[140:143]\n"
"ds_read_b64_tr_b16 a[194:195], v95 offset:33280\n"
"ds_read_b64_tr_b16 a[196:197], v92 offset:49152\n"
"v_mfma_f32_16x16x32_bf16 v[144:147], v[80:83], a[4:7], v[144:147]\n"
"buffer_load_dwordx4 v11, s[24:27], s72 offen offset:3072 lds\n"
"v_mfma_f32_16x16x32_bf16 v[128:131], v[84:87], a[80:83], v[128:131]\n"
"s_add_u32 m0, s69, 49152\n"
"s_add_u32 s72, s59, 98304\n"
"ds_read_b64_tr_b16 a[198:199], v92 offset:49664\n"
"v_mfma_f32_16x16x32_bf16 v[132:135], v[84:87], a[84:87], v[132:135]\n"
"ds_read_b64_tr_b16 v[80:81], v93 offset:49152\n"
"ds_read_b64_tr_b16 v[82:83], v93 offset:49664\n"
"v_mfma_f32_16x16x32_bf16 v[136:139], v[84:87], a[208:211], v[136:139]\n"
"v_add_u32 v92, 4294918144, v92\n"
"v_add_u32 v93, 4294918144, v93\n"
"v_mfma_f32_16x16x32_bf16 v[140:143], v[84:87], a[32:35], v[140:143]\n"
"buffer_load_dwordx4 v8, s[24:27], s72 offen lds\n"
"v_mfma_f32_16x16x32_bf16 v[144:147], v[84:87], a[36:39], v[144:147]\n"
"ds_read_b64_tr_b16 v[84:85], v94 offset:49152\n"
"ds_read_b64_tr_b16 v[86:87], v94 offset:49664\n"
"v_mfma_f32_16x16x32_bf16 v[128:131], v[88:91], v[196:199], v[128:131]\n"
"v_add_u32 v94, 4294918144, v94\n"
"v_mfma_f32_16x16x32_bf16 v[132:135], v[88:91], v[212:215], v[132:135]\n"
"v_mfma_f32_16x16x32_bf16 v[136:139], v[88:91], v[228:231], v[136:139]\n"
"buffer_load_dwordx4 v9, s[24:27], s72 offen offset:1024 lds\n"
"v_mfma_f32_16x16x32_bf16 v[140:143], v[88:91], v[244:247], v[140:143]\n"
"v_mfma_f32_16x16x32_bf16 v[144:147], v[88:91], v[68:71], v[144:147]\n"
"ds_read_b64_tr_b16 v[88:89], v95 offset:49152\n"
"ds_read_b64_tr_b16 v[90:91], v95 offset:49664\n"
"v_mfma_f32_16x16x32_bf16 v[128:131], v[168:171], a[88:91], v[128:131]\n"
"v_add_u32 v95, 4294918144, v95\n"
"v_mfma_f32_16x16x32_bf16 v[132:135], v[168:171], a[92:95], v[132:135]\n"
"buffer_load_dwordx4 v10, s[24:27], s72 offen offset:2048 lds\n"
"v_mfma_f32_16x16x32_bf16 v[136:139], v[168:171], a[164:167], v[136:139]\n"
"v_mfma_f32_16x16x32_bf16 v[140:143], v[168:171], a[232:235], v[140:143]\n"
"v_mfma_f32_16x16x32_bf16 v[144:147], v[168:171], a[236:239], v[144:147]\n"
"v_mfma_f32_16x16x32_bf16 v[128:131], v[172:175], a[96:99], v[128:131]\n"
"buffer_load_dwordx4 v11, s[24:27], s72 offen offset:3072 lds\n"
"v_mfma_f32_16x16x32_bf16 v[132:135], v[172:175], a[100:103], v[132:135]\n"
"s_add_u32 m0, s69, 65536\n"
"s_add_u32 s72, s60, 32768\n"
"v_mfma_f32_16x16x32_bf16 v[136:139], v[172:175], a[180:183], v[136:139]\n"
"v_mfma_f32_16x16x32_bf16 v[140:143], v[172:175], a[8:11], v[140:143]\n"
"v_mfma_f32_16x16x32_bf16 v[144:147], v[172:175], a[12:15], v[144:147]\n"
"v_mfma_f32_16x16x32_bf16 v[128:131], v[176:179], a[104:107], v[128:131]\n"
"v_mfma_f32_16x16x32_bf16 v[132:135], v[176:179], a[108:111], v[132:135]\n"
"v_mfma_f32_16x16x32_bf16 v[136:139], v[176:179], a[212:215], v[136:139]\n"
"v_mfma_f32_16x16x32_bf16 v[140:143], v[176:179], a[40:43], v[140:143]\n"
"v_mfma_f32_16x16x32_bf16 v[144:147], v[176:179], a[44:47], v[144:147]\n"
"s_waitcnt lgkmcnt(0)\n"
"buffer_store_dwordx4 v[96:99], v0, s[28:31], s65 offen sc1\n"
"v_mfma_f32_16x16x32_bf16 v[128:131], v[180:183], v[200:203], v[128:131]\n"
"v_mfma_f32_16x16x32_bf16 v[132:135], v[180:183], v[216:219], v[132:135]\n"
"v_mfma_f32_16x16x32_bf16 v[136:139], v[180:183], v[232:235], v[136:139]\n"
"v_mfma_f32_16x16x32_bf16 v[140:143], v[180:183], v[248:251], v[140:143]\n"
"v_mfma_f32_16x16x32_bf16 v[144:147], v[180:183], v[72:75], v[144:147]\n"
"v_mfma_f32_16x16x32_bf16 v[128:131], a[184:187], a[112:115], v[128:131]\n"
"v_mfma_f32_16x16x32_bf16 v[132:135], a[184:187], a[116:119], v[132:135]\n"
"buffer_store_dwordx4 v[100:103], v0, s[28:31], s65 offen offset:1024 sc1\n"
"v_mfma_f32_16x16x32_bf16 v[136:139], a[184:187], a[168:171], v[136:139]\n"
"v_mfma_f32_16x16x32_bf16 v[140:143], a[184:187], a[240:243], v[140:143]\n"
"v_mfma_f32_16x16x32_bf16 v[144:147], a[184:187], a[244:247], v[144:147]\n"
"buffer_store_dwordx4 v[104:107], v1, s[28:31], s65 offen offset:2048 sc1\n"
"v_mfma_f32_16x16x32_bf16 v[128:131], a[188:191], a[120:123], v[128:131]\n"
"s_waitcnt vmcnt(20)\n"
"buffer_store_dword v31, v191, s[36:39], s71 offen offset:136 sc1\n"
"v_mfma_f32_16x16x32_bf16 v[132:135], a[188:191], a[124:127], v[132:135]\n"
"s_lshl_b32 s71, s57, 1\n"
"v_mfma_f32_16x16x32_bf16 v[136:139], a[188:191], a[200:203], v[136:139]\n"
"v_mfma_f32_16x16x32_bf16 v[140:143], a[188:191], a[16:19], v[140:143]\n"
"buffer_load_dwordx4 v8, s[24:27], s60 offen lds\n"
"v_mfma_f32_16x16x32_bf16 v[144:147], a[188:191], a[20:23], v[144:147]\n"
"v_mfma_f32_16x16x32_bf16 v[128:131], a[192:195], a[128:131], v[128:131]\n"
"v_mfma_f32_16x16x32_bf16 v[132:135], a[192:195], a[132:135], v[132:135]\n"
"v_mfma_f32_16x16x32_bf16 v[136:139], a[192:195], a[216:219], v[136:139]\n"
"buffer_load_dwordx4 v9, s[24:27], s60 offen offset:1024 lds\n"
"v_mfma_f32_16x16x32_bf16 v[140:143], a[192:195], a[48:51], v[140:143]\n"
"v_mfma_f32_16x16x32_bf16 v[144:147], a[192:195], a[52:55], v[144:147]\n"
"v_mfma_f32_16x16x32_bf16 v[128:131], a[196:199], v[204:207], v[128:131]\n"
"v_mfma_f32_16x16x32_bf16 v[132:135], a[196:199], v[220:223], v[132:135]\n"
"v_mfma_f32_16x16x32_bf16 v[136:139], a[196:199], v[236:239], v[136:139]\n"
"v_mfma_f32_16x16x32_bf16 v[140:143], a[196:199], v[252:255], v[140:143]\n"
"v_mfma_f32_16x16x32_bf16 v[144:147], a[196:199], v[76:79], v[144:147]\n"
"v_mfma_f32_16x16x32_bf16 v[128:131], v[80:83], a[136:139], v[128:131]\n"
"buffer_load_dwordx4 v10, s[24:27], s60 offen offset:2048 lds\n"
"v_mfma_f32_16x16x32_bf16 v[132:135], v[80:83], a[140:143], v[132:135]\n"
"buffer_load_dwordx4 v11, s[24:27], s60 offen offset:3072 lds\n"
"v_mfma_f32_16x16x32_bf16 v[136:139], v[80:83], a[172:175], v[136:139]\n"
"s_add_u32 m0, s69, 131072\n"
"v_mfma_f32_16x16x32_bf16 v[140:143], v[80:83], a[248:251], v[140:143]\n"
"v_mfma_f32_16x16x32_bf16 v[144:147], v[80:83], a[252:255], v[144:147]\n"
"v_mfma_f32_16x16x32_bf16 v[128:131], v[84:87], a[144:147], v[128:131]\n"
"s_waitcnt vmcnt(8)\n"
"s_barrier\n"
"ds_read_b64_tr_b16 v[168:169], v92\n"
"ds_read_b64_tr_b16 v[170:171], v92 offset:512\n"
"v_mfma_f32_16x16x32_bf16 v[132:135], v[84:87], a[148:151], v[132:135]\n"
"ds_read_b64_tr_b16 v[172:173], v93\n"
"ds_read_b64_tr_b16 v[174:175], v93 offset:512\n"
"v_mfma_f32_16x16x32_bf16 v[136:139], v[84:87], a[204:207], v[136:139]\n"
"ds_read_b64_tr_b16 v[176:177], v94\n"
"ds_read_b64_tr_b16 v[178:179], v94 offset:512\n"
"v_mfma_f32_16x16x32_bf16 v[140:143], v[84:87], a[24:27], v[140:143]\n"
"buffer_load_dwordx4 v8, s[24:27], s72 offen lds\n"
"v_mfma_f32_16x16x32_bf16 v[144:147], v[84:87], a[28:31], v[144:147]\n"
"buffer_load_dwordx4 v9, s[24:27], s72 offen offset:1024 lds\n"
"v_mfma_f32_16x16x32_bf16 v[128:131], v[88:91], a[152:155], v[128:131]\n"
"ds_read_b64_tr_b16 v[180:181], v95\n"
"ds_read_b64_tr_b16 v[182:183], v95 offset:512\n"
"v_mfma_f32_16x16x32_bf16 v[132:135], v[88:91], a[156:159], v[132:135]\n"
"ds_read_b64_tr_b16 a[184:185], v92 offset:16384\n"
"ds_read_b64_tr_b16 a[186:187], v92 offset:16896\n"
"v_mfma_f32_16x16x32_bf16 v[136:139], v[88:91], a[220:223], v[136:139]\n"
"ds_read_b64_tr_b16 a[188:189], v93 offset:16384\n"
"ds_read_b64_tr_b16 a[190:191], v93 offset:16896\n"
"v_mfma_f32_16x16x32_bf16 v[140:143], v[88:91], a[56:59], v[140:143]\n"
"buffer_load_dwordx4 v10, s[24:27], s72 offen offset:2048 lds\n"
"v_mfma_f32_16x16x32_bf16 v[144:147], v[88:91], a[60:63], v[144:147]\n"
"s_waitcnt lgkmcnt(10)\n"
"v_cvt_pk_bf16_f32 v96, v128, v129\n"
"v_cvt_pk_bf16_f32 v97, v130, v131\n"
"v_mfma_f32_16x16x32_bf16 v[148:151], v[168:171], v[192:195], 0\n"
"v_cvt_pk_bf16_f32 v98, v132, v133\n"
"v_cvt_pk_bf16_f32 v99, v134, v135\n"
"v_mfma_f32_16x16x32_bf16 v[152:155], v[168:171], v[208:211], 0\n"
"v_cvt_pk_bf16_f32 v100, v136, v137\n"
"v_cvt_pk_bf16_f32 v101, v138, v139\n"
"v_mfma_f32_16x16x32_bf16 v[156:159], v[168:171], v[224:227], 0\n"
"buffer_load_dwordx4 v11, s[24:27], s72 offen offset:3072 lds\n"
"v_mfma_f32_16x16x32_bf16 v[160:163], v[168:171], v[240:243], 0\n"
"s_add_u32 m0, s69, 147456\n"
"s_add_u32 s72, s60, 65536\n"
"v_cvt_pk_bf16_f32 v104, v144, v145\n"
"v_mfma_f32_16x16x32_bf16 v[164:167], v[168:171], v[64:67], 0\n"
"s_waitcnt lgkmcnt(7)\n"
"v_cvt_pk_bf16_f32 v105, v146, v147\n"
"v_cvt_pk_bf16_f32 v102, v140, v141\n"
"v_mfma_f32_16x16x32_bf16 v[148:151], v[172:175], a[64:67], v[148:151]\n"
"v_cvt_pk_bf16_f32 v103, v142, v143\n"
"ds_read_b64_tr_b16 a[192:193], v94 offset:16384\n"
"v_mfma_f32_16x16x32_bf16 v[152:155], v[172:175], a[68:71], v[152:155]\n"
"buffer_load_dwordx4 v8, s[24:27], s72 offen lds\n"
"v_mfma_f32_16x16x32_bf16 v[156:159], v[172:175], a[160:163], v[156:159]\n"
"v_permlane32_swap_b32 v104, v106\n"
"v_mfma_f32_16x16x32_bf16 v[160:163], v[172:175], a[224:227], v[160:163]\n"
"v_permlane32_swap_b32 v105, v107\n"
"v_mfma_f32_16x16x32_bf16 v[164:167], v[172:175], a[228:231], v[164:167]\n"
"s_waitcnt lgkmcnt(2)\n"
"ds_read_b64_tr_b16 a[194:195], v94 offset:16896\n"
"ds_read_b64_tr_b16 a[196:197], v95 offset:16384\n"
"v_mfma_f32_16x16x32_bf16 v[148:151], v[176:179], a[72:75], v[148:151]\n"
"buffer_load_dwordx4 v9, s[24:27], s72 offen offset:1024 lds\n"
"v_mfma_f32_16x16x32_bf16 v[152:155], v[176:179], a[76:79], v[152:155]\n"
"ds_read_b64_tr_b16 a[198:199], v95 offset:16896\n"
"ds_read_b64_tr_b16 v[80:81], v92 offset:32768\n"
"v_mfma_f32_16x16x32_bf16 v[156:159], v[176:179], a[176:179], v[156:159]\n"
"ds_read_b64_tr_b16 v[82:83], v92 offset:33280\n"
"ds_read_b64_tr_b16 v[84:85], v93 offset:32768\n"
"v_mfma_f32_16x16x32_bf16 v[160:163], v[176:179], a[0:3], v[160:163]\n"
"ds_read_b64_tr_b16 v[86:87], v93 offset:33280\n"
"ds_read_b64_tr_b16 v[88:89], v94 offset:32768\n"
"v_mfma_f32_16x16x32_bf16 v[164:167], v[176:179], a[4:7], v[164:167]\n"
"ds_read_b64_tr_b16 v[90:91], v94 offset:33280\n"
"ds_read_b64_tr_b16 v[168:169], v95 offset:32768\n"
"v_mfma_f32_16x16x32_bf16 v[148:151], v[180:183], a[80:83], v[148:151]\n"
"buffer_load_dwordx4 v10, s[24:27], s72 offen offset:2048 lds\n"
"v_mfma_f32_16x16x32_bf16 v[152:155], v[180:183], a[84:87], v[152:155]\n"
"ds_read_b64_tr_b16 v[170:171], v95 offset:33280\n"
"ds_read_b64_tr_b16 v[172:173], v92 offset:49152\n"
"v_mfma_f32_16x16x32_bf16 v[156:159], v[180:183], a[208:211], v[156:159]\n"
"ds_read_b64_tr_b16 v[174:175], v92 offset:49664\n"
"ds_read_b64_tr_b16 v[176:177], v93 offset:49152\n"
"v_mfma_f32_16x16x32_bf16 v[160:163], v[180:183], a[32:35], v[160:163]\n"
"ds_read_b64_tr_b16 v[178:179], v93 offset:49664\n"
"v_add_u32 v92, 16384, v92\n"
"v_mfma_f32_16x16x32_bf16 v[164:167], v[180:183], a[36:39], v[164:167]\n"
"buffer_load_dwordx4 v11, s[24:27], s72 offen offset:3072 lds\n"
"v_mfma_f32_16x16x32_bf16 v[148:151], a[184:187], v[196:199], v[148:151]\n"
"s_add_u32 m0, s69, 114688\n"
"s_add_u32 s72, s60, 98304\n"
"ds_read_b64_tr_b16 v[180:181], v94 offset:49152\n"
"v_mfma_f32_16x16x32_bf16 v[152:155], a[184:187], v[212:215], v[152:155]\n"
"ds_read_b64_tr_b16 v[182:183], v94 offset:49664\n"
"v_add_u32 v93, 16384, v93\n"
"v_mfma_f32_16x16x32_bf16 v[156:159], a[184:187], v[228:231], v[156:159]\n"
"v_add_u32 v94, 16384, v94\n"
"v_mfma_f32_16x16x32_bf16 v[160:163], a[184:187], v[244:247], v[160:163]\n"
"buffer_load_dwordx4 v8, s[24:27], s72 offen lds\n"
"v_mfma_f32_16x16x32_bf16 v[164:167], a[184:187], v[68:71], v[164:167]\n"
"s_waitcnt lgkmcnt(12)\n"
"ds_read_b64_tr_b16 a[184:185], v95 offset:49152\n"
"ds_read_b64_tr_b16 a[186:187], v95 offset:49664\n"
"v_mfma_f32_16x16x32_bf16 v[148:151], a[188:191], a[88:91], v[148:151]\n"
"v_add_u32 v95, 16384, v95\n"
"v_mfma_f32_16x16x32_bf16 v[152:155], a[188:191], a[92:95], v[152:155]\n"
"v_mfma_f32_16x16x32_bf16 v[156:159], a[188:191], a[164:167], v[156:159]\n"
"buffer_load_dwordx4 v9, s[24:27], s72 offen offset:1024 lds\n"
"v_mfma_f32_16x16x32_bf16 v[160:163], a[188:191], a[232:235], v[160:163]\n"
"v_mfma_f32_16x16x32_bf16 v[164:167], a[188:191], a[236:239], v[164:167]\n"
"v_mfma_f32_16x16x32_bf16 v[148:151], a[192:195], a[96:99], v[148:151]\n"
"v_mfma_f32_16x16x32_bf16 v[152:155], a[192:195], a[100:103], v[152:155]\n"
"buffer_load_dwordx4 v10, s[24:27], s72 offen offset:2048 lds\n"
"v_mfma_f32_16x16x32_bf16 v[156:159], a[192:195], a[180:183], v[156:159]\n"
"v_mfma_f32_16x16x32_bf16 v[160:163], a[192:195], a[8:11], v[160:163]\n"
"v_mfma_f32_16x16x32_bf16 v[164:167], a[192:195], a[12:15], v[164:167]\n"
"v_mfma_f32_16x16x32_bf16 v[148:151], a[196:199], a[104:107], v[148:151]\n"
"buffer_load_dwordx4 v11, s[24:27], s72 offen offset:3072 lds\n"
"v_mfma_f32_16x16x32_bf16 v[152:155], a[196:199], a[108:111], v[152:155]\n"
"s_add_u32 m0, s69, 81920\n"
"s_add_u32 s72, s61, 32768\n"
"v_mfma_f32_16x16x32_bf16 v[156:159], a[196:199], a[212:215], v[156:159]\n"
"v_mfma_f32_16x16x32_bf16 v[160:163], a[196:199], a[40:43], v[160:163]\n"
"v_mfma_f32_16x16x32_bf16 v[164:167], a[196:199], a[44:47], v[164:167]\n"
"v_mfma_f32_16x16x32_bf16 v[148:151], v[80:83], v[200:203], v[148:151]\n"
"v_mfma_f32_16x16x32_bf16 v[152:155], v[80:83], v[216:219], v[152:155]\n"
"v_mfma_f32_16x16x32_bf16 v[156:159], v[80:83], v[232:235], v[156:159]\n"
"v_mfma_f32_16x16x32_bf16 v[160:163], v[80:83], v[248:251], v[160:163]\n"
"buffer_store_dwordx4 v[96:99], v0, s[28:31], s66 offen sc1\n"
"v_mfma_f32_16x16x32_bf16 v[164:167], v[80:83], v[72:75], v[164:167]\n"
"s_waitcnt lgkmcnt(0)\n"
"v_mfma_f32_16x16x32_bf16 v[148:151], v[84:87], a[112:115], v[148:151]\n"
"v_mfma_f32_16x16x32_bf16 v[152:155], v[84:87], a[116:119], v[152:155]\n"
"v_mfma_f32_16x16x32_bf16 v[156:159], v[84:87], a[168:171], v[156:159]\n"
"v_mfma_f32_16x16x32_bf16 v[160:163], v[84:87], a[240:243], v[160:163]\n"
"v_mfma_f32_16x16x32_bf16 v[164:167], v[84:87], a[244:247], v[164:167]\n"
"v_mfma_f32_16x16x32_bf16 v[148:151], v[88:91], a[120:123], v[148:151]\n"
"v_mfma_f32_16x16x32_bf16 v[152:155], v[88:91], a[124:127], v[152:155]\n"
"buffer_store_dwordx4 v[100:103], v0, s[28:31], s66 offen offset:1024 sc1\n"
"v_mfma_f32_16x16x32_bf16 v[156:159], v[88:91], a[200:203], v[156:159]\n"
"v_mfma_f32_16x16x32_bf16 v[160:163], v[88:91], a[16:19], v[160:163]\n"
"v_mfma_f32_16x16x32_bf16 v[164:167], v[88:91], a[20:23], v[164:167]\n"
"buffer_store_dwordx4 v[104:107], v1, s[28:31], s66 offen offset:2048 sc1\n"
"v_mfma_f32_16x16x32_bf16 v[148:151], v[168:171], a[128:131], v[148:151]\n"
"s_waitcnt vmcnt(20)\n"
"buffer_store_dword v31, v191, s[36:39], s71 offen offset:140 sc1\n"
"v_mfma_f32_16x16x32_bf16 v[152:155], v[168:171], a[132:135], v[152:155]\n"
"s_lshl_b32 s71, s58, 1\n"
"v_mfma_f32_16x16x32_bf16 v[156:159], v[168:171], a[216:219], v[156:159]\n"
"v_mfma_f32_16x16x32_bf16 v[160:163], v[168:171], a[48:51], v[160:163]\n"
"buffer_load_dwordx4 v8, s[24:27], s61 offen lds\n"
"v_mfma_f32_16x16x32_bf16 v[164:167], v[168:171], a[52:55], v[164:167]\n"
"v_mfma_f32_16x16x32_bf16 v[148:151], v[172:175], v[204:207], v[148:151]\n"
"v_mfma_f32_16x16x32_bf16 v[152:155], v[172:175], v[220:223], v[152:155]\n"
"v_mfma_f32_16x16x32_bf16 v[156:159], v[172:175], v[236:239], v[156:159]\n"
"buffer_load_dwordx4 v9, s[24:27], s61 offen offset:1024 lds\n"
"v_mfma_f32_16x16x32_bf16 v[160:163], v[172:175], v[252:255], v[160:163]\n"
"v_mfma_f32_16x16x32_bf16 v[164:167], v[172:175], v[76:79], v[164:167]\n"
"v_mfma_f32_16x16x32_bf16 v[148:151], v[176:179], a[136:139], v[148:151]\n"
"v_mfma_f32_16x16x32_bf16 v[152:155], v[176:179], a[140:143], v[152:155]\n"
"v_mfma_f32_16x16x32_bf16 v[156:159], v[176:179], a[172:175], v[156:159]\n"
"v_mfma_f32_16x16x32_bf16 v[160:163], v[176:179], a[248:251], v[160:163]\n"
"v_mfma_f32_16x16x32_bf16 v[164:167], v[176:179], a[252:255], v[164:167]\n"
"v_mfma_f32_16x16x32_bf16 v[148:151], v[180:183], a[144:147], v[148:151]\n"
"buffer_load_dwordx4 v10, s[24:27], s61 offen offset:2048 lds\n"
"v_mfma_f32_16x16x32_bf16 v[152:155], v[180:183], a[148:151], v[152:155]\n"
"buffer_load_dwordx4 v11, s[24:27], s61 offen offset:3072 lds\n"
"v_mfma_f32_16x16x32_bf16 v[156:159], v[180:183], a[204:207], v[156:159]\n"
"s_add_u32 m0, s69, 98304\n"
"v_mfma_f32_16x16x32_bf16 v[160:163], v[180:183], a[24:27], v[160:163]\n"
"v_mfma_f32_16x16x32_bf16 v[164:167], v[180:183], a[28:31], v[164:167]\n"
"v_mfma_f32_16x16x32_bf16 v[148:151], a[184:187], a[152:155], v[148:151]\n"
"s_waitcnt vmcnt(8)\n"
"s_barrier\n"
"ds_read_b64_tr_b16 a[188:189], v92 offset:49152\n"
"ds_read_b64_tr_b16 a[190:191], v92 offset:49664\n"
"v_mfma_f32_16x16x32_bf16 v[152:155], a[184:187], a[156:159], v[152:155]\n"
"ds_read_b64_tr_b16 a[192:193], v93 offset:49152\n"
"ds_read_b64_tr_b16 a[194:195], v93 offset:49664\n"
"v_mfma_f32_16x16x32_bf16 v[156:159], a[184:187], a[220:223], v[156:159]\n"
"ds_read_b64_tr_b16 a[196:197], v94 offset:49152\n"
"ds_read_b64_tr_b16 a[198:199], v94 offset:49664\n"
"v_mfma_f32_16x16x32_bf16 v[160:163], a[184:187], a[56:59], v[160:163]\n"
"buffer_load_dwordx4 v8, s[24:27], s72 offen lds\n"
"v_mfma_f32_16x16x32_bf16 v[164:167], a[184:187], a[60:63], v[164:167]\n"
"buffer_load_dwordx4 v9, s[24:27], s72 offen offset:1024 lds\n"
"ds_read_b64_tr_b16 v[80:81], v95 offset:49152\n"
"ds_read_b64_tr_b16 v[82:83], v95 offset:49664\n"
"v_add_u32 v92, 81920, v92\n"
"ds_read_b64_tr_b16 v[84:85], v92 offset:32768\n"
"ds_read_b64_tr_b16 v[86:87], v92 offset:33280\n"
"v_add_u32 v93, 81920, v93\n"
"ds_read_b64_tr_b16 v[88:89], v93 offset:32768\n"
"v_cvt_pk_bf16_f32 v104, v164, v165\n"
"v_cvt_pk_bf16_f32 v105, v166, v167\n"
"ds_read_b64_tr_b16 v[90:91], v93 offset:33280\n"
"buffer_load_dwordx4 v10, s[24:27], s72 offen offset:2048 lds\n"
"v_add_u32 v94, 81920, v94\n"
"ds_read_b64_tr_b16 v[168:169], v94 offset:32768\n"
"ds_read_b64_tr_b16 v[170:171], v94 offset:33280\n"
"v_add_u32 v95, 81920, v95\n"
"ds_read_b64_tr_b16 v[172:173], v95 offset:32768\n"
"ds_read_b64_tr_b16 v[174:175], v95 offset:33280\n"
"s_waitcnt lgkmcnt(14)\n"
"v_mfma_f32_16x16x32_bf16 v[128:131], a[188:191], v[192:195], 0\n"
"v_cvt_pk_bf16_f32 v96, v148, v149\n"
"v_cvt_pk_bf16_f32 v97, v150, v151\n"
"v_mfma_f32_16x16x32_bf16 v[132:135], a[188:191], v[208:211], 0\n"
"v_cvt_pk_bf16_f32 v98, v152, v153\n"
"v_cvt_pk_bf16_f32 v99, v154, v155\n"
"v_mfma_f32_16x16x32_bf16 v[136:139], a[188:191], v[224:227], 0\n"
"v_cvt_pk_bf16_f32 v100, v156, v157\n"
"v_cvt_pk_bf16_f32 v101, v158, v159\n"
"v_mfma_f32_16x16x32_bf16 v[140:143], a[188:191], v[240:243], 0\n"
"buffer_load_dwordx4 v11, s[24:27], s72 offen offset:3072 lds\n"
"v_mfma_f32_16x16x32_bf16 v[144:147], a[188:191], v[64:67], 0\n"
"s_add_u32 m0, s69, 0\n"
"buffer_load_dword v26, v190, s[36:39], s76 offen offset:128 sc1\n"
"s_waitcnt lgkmcnt(12)\n"
"v_mfma_f32_16x16x32_bf16 v[128:131], a[192:195], a[64:67], v[128:131]\n"
"s_add_u32 s72, s61, 65536\n"
"v_cvt_pk_bf16_f32 v102, v160, v161\n"
"v_cvt_pk_bf16_f32 v103, v162, v163\n"
"v_mfma_f32_16x16x32_bf16 v[132:135], a[192:195], a[68:71], v[132:135]\n"
"v_permlane32_swap_b32 v104, v106\n"
"v_mfma_f32_16x16x32_bf16 v[136:139], a[192:195], a[160:163], v[136:139]\n"
"buffer_load_dwordx4 v8, s[24:27], s72 offen lds\n"
"v_mfma_f32_16x16x32_bf16 v[140:143], a[192:195], a[224:227], v[140:143]\n"
"v_permlane32_swap_b32 v105, v107\n"
"v_mfma_f32_16x16x32_bf16 v[144:147], a[192:195], a[228:231], v[144:147]\n"
"s_waitcnt lgkmcnt(5)\n"
"ds_read_b64_tr_b16 v[176:177], v92 offset:49152\n"
"ds_read_b64_tr_b16 v[178:179], v92 offset:49664\n"
"v_mfma_f32_16x16x32_bf16 v[128:131], a[196:199], a[72:75], v[128:131]\n"
"ds_read_b64_tr_b16 v[180:181], v93 offset:49152\n"
"ds_read_b64_tr_b16 v[182:183], v93 offset:49664\n"
"v_mfma_f32_16x16x32_bf16 v[132:135], a[196:199], a[76:79], v[132:135]\n"
"buffer_load_dwordx4 v9, s[24:27], s72 offen offset:1024 lds\n"
"v_mfma_f32_16x16x32_bf16 v[136:139], a[196:199], a[176:179], v[136:139]\n"
"ds_read_b64_tr_b16 a[184:185], v94 offset:49152\n"
"ds_read_b64_tr_b16 a[186:187], v94 offset:49664\n"
"v_mfma_f32_16x16x32_bf16 v[140:143], a[196:199], a[0:3], v[140:143]\n"
"ds_read_b64_tr_b16 a[188:189], v95 offset:49152\n"
"ds_read_b64_tr_b16 a[190:191], v95 offset:49664\n"
"v_mfma_f32_16x16x32_bf16 v[144:147], a[196:199], a[4:7], v[144:147]\n"
"ds_read_b64_tr_b16 a[192:193], v92 offset:16384\n"
"ds_read_b64_tr_b16 a[194:195], v92 offset:16896\n"
"v_mfma_f32_16x16x32_bf16 v[128:131], v[80:83], a[80:83], v[128:131]\n"
"buffer_load_dwordx4 v10, s[24:27], s72 offen offset:2048 lds\n"
"v_mfma_f32_16x16x32_bf16 v[132:135], v[80:83], a[84:87], v[132:135]\n"
"ds_read_b64_tr_b16 a[196:197], v93 offset:16384\n"
"ds_read_b64_tr_b16 a[198:199], v93 offset:16896\n"
"v_mfma_f32_16x16x32_bf16 v[136:139], v[80:83], a[208:211], v[136:139]\n"
"v_add_u32 v92, 4294918144, v92\n"
"v_add_u32 v93, 4294918144, v93\n"
"v_mfma_f32_16x16x32_bf16 v[140:143], v[80:83], a[32:35], v[140:143]\n"
"v_mfma_f32_16x16x32_bf16 v[144:147], v[80:83], a[36:39], v[144:147]\n"
"buffer_load_dwordx4 v11, s[24:27], s72 offen offset:3072 lds\n"
"v_mfma_f32_16x16x32_bf16 v[128:131], v[84:87], v[196:199], v[128:131]\n"
"s_add_u32 m0, s69, 16384\n"
"s_add_u32 s72, s61, 98304\n"
"ds_read_b64_tr_b16 v[80:81], v94 offset:16384\n"
"v_mfma_f32_16x16x32_bf16 v[132:135], v[84:87], v[212:215], v[132:135]\n"
"ds_read_b64_tr_b16 v[82:83], v94 offset:16896\n"
"v_add_u32 v94, 4294918144, v94\n"
"v_mfma_f32_16x16x32_bf16 v[136:139], v[84:87], v[228:231], v[136:139]\n"
"v_mfma_f32_16x16x32_bf16 v[140:143], v[84:87], v[244:247], v[140:143]\n"
"buffer_load_dwordx4 v8, s[24:27], s72 offen lds\n"
"v_mfma_f32_16x16x32_bf16 v[144:147], v[84:87], v[68:71], v[144:147]\n"
"s_waitcnt lgkmcnt(9)\n"
"ds_read_b64_tr_b16 v[84:85], v95 offset:16384\n"
"ds_read_b64_tr_b16 v[86:87], v95 offset:16896\n"
"v_mfma_f32_16x16x32_bf16 v[128:131], v[88:91], a[88:91], v[128:131]\n"
"v_add_u32 v95, 4294918144, v95\n"
"v_mfma_f32_16x16x32_bf16 v[132:135], v[88:91], a[92:95], v[132:135]\n"
"v_mfma_f32_16x16x32_bf16 v[136:139], v[88:91], a[164:167], v[136:139]\n"
"buffer_load_dwordx4 v9, s[24:27], s72 offen offset:1024 lds\n"
"v_mfma_f32_16x16x32_bf16 v[140:143], v[88:91], a[232:235], v[140:143]\n"
"v_mfma_f32_16x16x32_bf16 v[144:147], v[88:91], a[236:239], v[144:147]\n"
"v_mfma_f32_16x16x32_bf16 v[128:131], v[168:171], a[96:99], v[128:131]\n"
"v_mfma_f32_16x16x32_bf16 v[132:135], v[168:171], a[100:103], v[132:135]\n"
"buffer_load_dwordx4 v10, s[24:27], s72 offen offset:2048 lds\n"
"v_mfma_f32_16x16x32_bf16 v[136:139], v[168:171], a[180:183], v[136:139]\n"
"v_mfma_f32_16x16x32_bf16 v[140:143], v[168:171], a[8:11], v[140:143]\n"
"v_mfma_f32_16x16x32_bf16 v[144:147], v[168:171], a[12:15], v[144:147]\n"
"v_mfma_f32_16x16x32_bf16 v[128:131], v[172:175], a[104:107], v[128:131]\n"
"v_mfma_f32_16x16x32_bf16 v[132:135], v[172:175], a[108:111], v[132:135]\n"
"v_mfma_f32_16x16x32_bf16 v[136:139], v[172:175], a[212:215], v[136:139]\n"
"v_mfma_f32_16x16x32_bf16 v[140:143], v[172:175], a[40:43], v[140:143]\n"
"v_mfma_f32_16x16x32_bf16 v[144:147], v[172:175], a[44:47], v[144:147]\n"
"v_mfma_f32_16x16x32_bf16 v[128:131], v[176:179], v[200:203], v[128:131]\n"
"v_mfma_f32_16x16x32_bf16 v[132:135], v[176:179], v[216:219], v[132:135]\n"
"v_mfma_f32_16x16x32_bf16 v[136:139], v[176:179], v[232:235], v[136:139]\n"
"v_mfma_f32_16x16x32_bf16 v[140:143], v[176:179], v[248:251], v[140:143]\n"
"v_mfma_f32_16x16x32_bf16 v[144:147], v[176:179], v[72:75], v[144:147]\n"
"v_mfma_f32_16x16x32_bf16 v[128:131], v[180:183], a[112:115], v[128:131]\n"
"v_mfma_f32_16x16x32_bf16 v[132:135], v[180:183], a[116:119], v[132:135]\n"
"buffer_load_dwordx4 v11, s[24:27], s72 offen offset:3072 lds\n"
"v_mfma_f32_16x16x32_bf16 v[136:139], v[180:183], a[168:171], v[136:139]\n"
"v_mfma_f32_16x16x32_bf16 v[140:143], v[180:183], a[240:243], v[140:143]\n"
"v_mfma_f32_16x16x32_bf16 v[144:147], v[180:183], a[244:247], v[144:147]\n"
"s_waitcnt lgkmcnt(0)\n"
"v_mfma_f32_16x16x32_bf16 v[128:131], a[184:187], a[120:123], v[128:131]\n"
"v_mfma_f32_16x16x32_bf16 v[132:135], a[184:187], a[124:127], v[132:135]\n"
"v_mfma_f32_16x16x32_bf16 v[136:139], a[184:187], a[200:203], v[136:139]\n"
"v_mfma_f32_16x16x32_bf16 v[140:143], a[184:187], a[16:19], v[140:143]\n"
"v_mfma_f32_16x16x32_bf16 v[144:147], a[184:187], a[20:23], v[144:147]\n"
"s_waitcnt vmcnt(8)\n"
"v_cmp_eq_u32 vcc, v26, v31\n"
"s_nop 1\n"
"s_and_b32 s70, vcc_lo, 7\n"
"s_cmp_eq_u32 s70, 7\n"
"s_cbranch_scc0 LS0P0%=\n"
"LB0P0%=:\n"
"v_mfma_f32_16x16x32_bf16 v[128:131], a[188:191], a[128:131], v[128:131]\n"
"buffer_store_dwordx4 v[96:99], v0, s[28:31], s67 offen sc1\n"
"v_mfma_f32_16x16x32_bf16 v[132:135], a[188:191], a[132:135], v[132:135]\n"
"v_mfma_f32_16x16x32_bf16 v[136:139], a[188:191], a[216:219], v[136:139]\n"
"v_mfma_f32_16x16x32_bf16 v[140:143], a[188:191], a[48:51], v[140:143]\n"
"v_mfma_f32_16x16x32_bf16 v[144:147], a[188:191], a[52:55], v[144:147]\n"
"v_mfma_f32_16x16x32_bf16 v[128:131], a[192:195], v[204:207], v[128:131]\n"
"v_mfma_f32_16x16x32_bf16 v[132:135], a[192:195], v[220:223], v[132:135]\n"
"buffer_store_dwordx4 v[100:103], v0, s[28:31], s67 offen offset:1024 sc1\n"
"v_mfma_f32_16x16x32_bf16 v[136:139], a[192:195], v[236:239], v[136:139]\n"
"v_mfma_f32_16x16x32_bf16 v[140:143], a[192:195], v[252:255], v[140:143]\n"
"v_mfma_f32_16x16x32_bf16 v[144:147], a[192:195], v[76:79], v[144:147]\n"
"buffer_store_dwordx4 v[104:107], v1, s[28:31], s67 offen offset:2048 sc1\n"
"v_mfma_f32_16x16x32_bf16 v[128:131], a[196:199], a[136:139], v[128:131]\n"
"buffer_store_dword v31, v191, s[36:39], s71 offen offset:144 sc1\n"
"v_mfma_f32_16x16x32_bf16 v[132:135], a[196:199], a[140:143], v[132:135]\n"
"v_mfma_f32_16x16x32_bf16 v[136:139], a[196:199], a[172:175], v[136:139]\n"
"v_mfma_f32_16x16x32_bf16 v[140:143], a[196:199], a[248:251], v[140:143]\n"
"buffer_load_dwordx4 v[108:111], v0, s[44:47], 0 offen sc1\n"
"v_mfma_f32_16x16x32_bf16 v[144:147], a[196:199], a[252:255], v[144:147]\n"
"v_mfma_f32_16x16x32_bf16 v[128:131], v[80:83], a[144:147], v[128:131]\n"
"v_mfma_f32_16x16x32_bf16 v[132:135], v[80:83], a[148:151], v[132:135]\n"
"v_mfma_f32_16x16x32_bf16 v[136:139], v[80:83], a[204:207], v[136:139]\n"
"buffer_load_dwordx4 v[112:115], v0, s[44:47], 0 offen offset:1024 sc1\n"
"v_mfma_f32_16x16x32_bf16 v[140:143], v[80:83], a[24:27], v[140:143]\n"
"v_mfma_f32_16x16x32_bf16 v[144:147], v[80:83], a[28:31], v[144:147]\n"
"v_mfma_f32_16x16x32_bf16 v[128:131], v[84:87], a[152:155], v[128:131]\n"
"v_mfma_f32_16x16x32_bf16 v[132:135], v[84:87], a[156:159], v[132:135]\n"
"v_mfma_f32_16x16x32_bf16 v[136:139], v[84:87], a[220:223], v[136:139]\n"
"v_mfma_f32_16x16x32_bf16 v[140:143], v[84:87], a[56:59], v[140:143]\n"
"v_mfma_f32_16x16x32_bf16 v[144:147], v[84:87], a[60:63], v[144:147]\n"
"s_nop 3\n"
"v_cvt_pk_bf16_f32 v96, v128, v129\n"
"v_cvt_pk_bf16_f32 v97, v130, v131\n"
"v_cvt_pk_bf16_f32 v98, v132, v133\n"
"buffer_load_dwordx2 v[116:117], v188, s[44:47], 0 offen offset:2048 sc1\n"
"v_cvt_pk_bf16_f32 v99, v134, v135\n"
"v_cvt_pk_bf16_f32 v100, v136, v137\n"
"buffer_load_dwordx4 v[118:121], v0, s[44:47], 0 offen offset:2560 sc1\n"
"v_cvt_pk_bf16_f32 v101, v138, v139\n"
"v_cvt_pk_bf16_f32 v104, v144, v145\n"
"v_cvt_pk_bf16_f32 v105, v146, v147\n"
"v_cvt_pk_bf16_f32 v102, v140, v141\n"
"v_cvt_pk_bf16_f32 v103, v142, v143\n"
"v_permlane32_swap_b32 v104, v106\n"
"s_waitcnt vmcnt(8)\n"
"s_barrier\n"
"ds_read_b64_tr_b16 v[88:89], v92 offset:32768\n"
"ds_read_b64_tr_b16 v[90:91], v92 offset:33280\n"
"ds_read_b64_tr_b16 v[168:169], v93 offset:32768\n"
"ds_read_b64_tr_b16 v[170:171], v93 offset:33280\n"
"v_permlane32_swap_b32 v105, v107\n"
"ds_read_b64_tr_b16 v[172:173], v94 offset:32768\n"
"ds_read_b64_tr_b16 v[174:175], v94 offset:33280\n"
"ds_read_b64_tr_b16 v[176:177], v95 offset:32768\n"
"ds_read_b64_tr_b16 v[178:179], v95 offset:33280\n"
"ds_read_b64_tr_b16 v[180:181], v92 offset:49152\n"
"ds_read_b64_tr_b16 v[182:183], v92 offset:49664\n"
"ds_read_b64_tr_b16 a[184:185], v93 offset:49152\n"
"ds_read_b64_tr_b16 a[186:187], v93 offset:49664\n"
"ds_read_b64_tr_b16 a[188:189], v94 offset:49152\n"
"buffer_load_dwordx4 v[122:125], v0, s[44:47], 0 offen offset:3584 sc1\n"
"ds_read_b64_tr_b16 a[190:191], v94 offset:49664\n"
"ds_read_b64_tr_b16 a[192:193], v95 offset:49152\n"
"buffer_load_dwordx2 v[126:127], v188, s[44:47], s50 offen offset:512 sc1\n"
"ds_read_b64_tr_b16 a[194:195], v95 offset:49664\n"
"v_add_u32 v92, 4294918144, v92\n"
"buffer_load_dwordx4 v[32:35], v0, s[44:47], s50 offen offset:1024 sc1\n"
"ds_read_b64_tr_b16 a[196:197], v92\n"
"ds_read_b64_tr_b16 a[198:199], v92 offset:512\n"
"v_add_u32 v93, 4294918144, v93\n"
"ds_read_b64_tr_b16 v[80:81], v93\n"
"ds_read_b64_tr_b16 v[82:83], v93 offset:512\n"
"buffer_load_dwordx4 v[36:39], v0, s[44:47], s50 offen offset:2048 sc1\n"
"v_add_u32 v94, 4294918144, v94\n"
"ds_read_b64_tr_b16 v[84:85], v94\n"
"s_waitcnt lgkmcnt(15)\n"
"v_mfma_f32_16x16x32_bf16 v[148:151], v[88:91], v[192:195], 0\n"
"buffer_load_dwordx2 v[40:41], v188, s[44:47], s50 offen offset:3072 sc1\n"
"v_mfma_f32_16x16x32_bf16 v[152:155], v[88:91], v[208:211], 0\n"
"ds_read_b64_tr_b16 v[86:87], v94 offset:512\n"
"v_add_u32 v95, 4294918144, v95\n"
"v_mfma_f32_16x16x32_bf16 v[156:159], v[88:91], v[224:227], 0\n"
"v_mfma_f32_16x16x32_bf16 v[160:163], v[88:91], v[240:243], 0\n"
"v_mfma_f32_16x16x32_bf16 v[164:167], v[88:91], v[64:67], 0\n"
"ds_read_b64_tr_b16 v[88:89], v95\n"
"ds_read_b64_tr_b16 v[90:91], v95 offset:512\n"
"v_mfma_f32_16x16x32_bf16 v[148:151], v[168:171], a[64:67], v[148:151]\n"
"v_mfma_f32_16x16x32_bf16 v[152:155], v[168:171], a[68:71], v[152:155]\n"
"v_mfma_f32_16x16x32_bf16 v[156:159], v[168:171], a[160:163], v[156:159]\n"
"v_mfma_f32_16x16x32_bf16 v[160:163], v[168:171], a[224:227], v[160:163]\n"
"v_mfma_f32_16x16x32_bf16 v[164:167], v[168:171], a[228:231], v[164:167]\n"
"ds_read_b64_tr_b16 v[168:169], v92 offset:16384\n"
"ds_read_b64_tr_b16 v[170:171], v92 offset:16896\n"
"v_mfma_f32_16x16x32_bf16 v[148:151], v[172:175], a[72:75], v[148:151]\n"
"v_mfma_f32_16x16x32_bf16 v[152:155], v[172:175], a[76:79], v[152:155]\n"
"v_mfma_f32_16x16x32_bf16 v[156:159], v[172:175], a[176:179], v[156:159]\n"
"v_mfma_f32_16x16x32_bf16 v[160:163], v[172:175], a[0:3], v[160:163]\n"
"v_mfma_f32_16x16x32_bf16 v[164:167], v[172:175], a[4:7], v[164:167]\n"
"s_waitcnt lgkmcnt(4)\n"
"ds_read_b64_tr_b16 v[172:173], v93 offset:16384\n"
"ds_read_b64_tr_b16 v[174:175], v93 offset:16896\n"
"v_mfma_f32_16x16x32_bf16 v[148:151], v[176:179], a[80:83], v[148:151]\n"
"v_mfma_f32_16x16x32_bf16 v[152:155], v[176:179], a[84:87], v[152:155]\n"
"v_mfma_f32_16x16x32_bf16 v[156:159], v[176:179], a[208:211], v[156:159]\n"
"v_mfma_f32_16x16x32_bf16 v[160:163], v[176:179], a[32:35], v[160:163]\n"
"v_mfma_f32_16x16x32_bf16 v[164:167], v[176:179], a[36:39], v[164:167]\n"
"ds_read_b64_tr_b16 v[176:177], v94 offset:16384\n"
"ds_read_b64_tr_b16 v[178:179], v94 offset:16896\n"
"v_mfma_f32_16x16x32_bf16 v[148:151], v[180:183], v[196:199], v[148:151]\n"
"v_mfma_f32_16x16x32_bf16 v[152:155], v[180:183], v[212:215], v[152:155]\n"
"v_mfma_f32_16x16x32_bf16 v[156:159], v[180:183], v[228:231], v[156:159]\n"
"v_mfma_f32_16x16x32_bf16 v[160:163], v[180:183], v[244:247], v[160:163]\n"
"v_mfma_f32_16x16x32_bf16 v[164:167], v[180:183], v[68:71], v[164:167]\n"
"ds_read_b64_tr_b16 v[180:181], v95 offset:16384\n"
"ds_read_b64_tr_b16 v[182:183], v95 offset:16896\n"
"v_mfma_f32_16x16x32_bf16 v[148:151], a[184:187], a[88:91], v[148:151]\n"
"v_mfma_f32_16x16x32_bf16 v[152:155], a[184:187], a[92:95], v[152:155]\n"
"v_mfma_f32_16x16x32_bf16 v[156:159], a[184:187], a[164:167], v[156:159]\n"
"v_mfma_f32_16x16x32_bf16 v[160:163], a[184:187], a[232:235], v[160:163]\n"
"v_mfma_f32_16x16x32_bf16 v[164:167], a[184:187], a[236:239], v[164:167]\n"
"v_mfma_f32_16x16x32_bf16 v[148:151], a[188:191], a[96:99], v[148:151]\n"
"v_mfma_f32_16x16x32_bf16 v[152:155], a[188:191], a[100:103], v[152:155]\n"
"v_mfma_f32_16x16x32_bf16 v[156:159], a[188:191], a[180:183], v[156:159]\n"
"v_mfma_f32_16x16x32_bf16 v[160:163], a[188:191], a[8:11], v[160:163]\n"
"v_mfma_f32_16x16x32_bf16 v[164:167], a[188:191], a[12:15], v[164:167]\n"
"v_mfma_f32_16x16x32_bf16 v[148:151], a[192:195], a[104:107], v[148:151]\n"
"v_mfma_f32_16x16x32_bf16 v[152:155], a[192:195], a[108:111], v[152:155]\n"
"v_mfma_f32_16x16x32_bf16 v[156:159], a[192:195], a[212:215], v[156:159]\n"
"buffer_load_dword v26, v190, s[36:39], s76 offen offset:128 sc1\n"
"v_mfma_f32_16x16x32_bf16 v[160:163], a[192:195], a[40:43], v[160:163]\n"
"v_mfma_f32_16x16x32_bf16 v[164:167], a[192:195], a[44:47], v[164:167]\n"
"v_mfma_f32_16x16x32_bf16 v[148:151], a[196:199], v[200:203], v[148:151]\n"
"v_mfma_f32_16x16x32_bf16 v[152:155], a[196:199], v[216:219], v[152:155]\n"
"v_mfma_f32_16x16x32_bf16 v[156:159], a[196:199], v[232:235], v[156:159]\n"
"v_mfma_f32_16x16x32_bf16 v[160:163], a[196:199], v[248:251], v[160:163]\n"
"v_mfma_f32_16x16x32_bf16 v[164:167], a[196:199], v[72:75], v[164:167]\n"
"v_mfma_f32_16x16x32_bf16 v[148:151], v[80:83], a[112:115], v[148:151]\n"
"v_mfma_f32_16x16x32_bf16 v[152:155], v[80:83], a[116:119], v[152:155]\n"
"v_mfma_f32_16x16x32_bf16 v[156:159], v[80:83], a[168:171], v[156:159]\n"
"v_mfma_f32_16x16x32_bf16 v[160:163], v[80:83], a[240:243], v[160:163]\n"
"v_mfma_f32_16x16x32_bf16 v[164:167], v[80:83], a[244:247], v[164:167]\n"
"buffer_store_dwordx4 v[96:99], v0, s[28:31], s68 offen sc1\n"
"v_mfma_f32_16x16x32_bf16 v[148:151], v[84:87], a[120:123], v[148:151]\n"
"v_mfma_f32_16x16x32_bf16 v[152:155], v[84:87], a[124:127], v[152:155]\n"
"v_mfma_f32_16x16x32_bf16 v[156:159], v[84:87], a[200:203], v[156:159]\n"
"v_mfma_f32_16x16x32_bf16 v[160:163], v[84:87], a[16:19], v[160:163]\n"
"v_mfma_f32_16x16x32_bf16 v[164:167], v[84:87], a[20:23], v[164:167]\n"
"s_waitcnt lgkmcnt(0)\n"
"v_mfma_f32_16x16x32_bf16 v[148:151], v[88:91], a[128:131], v[148:151]\n"
"v_mfma_f32_16x16x32_bf16 v[152:155], v[88:91], a[132:135], v[152:155]\n"
"v_mfma_f32_16x16x32_bf16 v[156:159], v[88:91], a[216:219], v[156:159]\n"
"buffer_store_dwordx4 v[100:103], v0, s[28:31], s68 offen offset:1024 sc1\n"
"v_mfma_f32_16x16x32_bf16 v[160:163], v[88:91], a[48:51], v[160:163]\n"
"v_mfma_f32_16x16x32_bf16 v[164:167], v[88:91], a[52:55], v[164:167]\n"
"v_mfma_f32_16x16x32_bf16 v[148:151], v[168:171], v[204:207], v[148:151]\n"
"v_mfma_f32_16x16x32_bf16 v[152:155], v[168:171], v[220:223], v[152:155]\n"
"v_mfma_f32_16x16x32_bf16 v[156:159], v[168:171], v[236:239], v[156:159]\n"
"buffer_store_dwordx4 v[104:107], v1, s[28:31], s68 offen offset:2048 sc1\n"
"v_mfma_f32_16x16x32_bf16 v[160:163], v[168:171], v[252:255], v[160:163]\n"
"v_mfma_f32_16x16x32_bf16 v[164:167], v[168:171], v[76:79], v[164:167]\n"
"v_mfma_f32_16x16x32_bf16 v[148:151], v[172:175], a[136:139], v[148:151]\n"
"v_mfma_f32_16x16x32_bf16 v[152:155], v[172:175], a[140:143], v[152:155]\n"
"v_mfma_f32_16x16x32_bf16 v[156:159], v[172:175], a[172:175], v[156:159]\n"
"v_mfma_f32_16x16x32_bf16 v[160:163], v[172:175], a[248:251], v[160:163]\n"
"v_mfma_f32_16x16x32_bf16 v[164:167], v[172:175], a[252:255], v[164:167]\n"
"v_mfma_f32_16x16x32_bf16 v[148:151], v[176:179], a[144:147], v[148:151]\n"
"v_mfma_f32_16x16x32_bf16 v[152:155], v[176:179], a[148:151], v[152:155]\n"
"v_mfma_f32_16x16x32_bf16 v[156:159], v[176:179], a[204:207], v[156:159]\n"
"v_mfma_f32_16x16x32_bf16 v[160:163], v[176:179], a[24:27], v[160:163]\n"
"v_mfma_f32_16x16x32_bf16 v[164:167], v[176:179], a[28:31], v[164:167]\n"
"s_mov_b32 s55, 32768\n"
"s_mov_b32 s56, 65536\n"
"s_mov_b32 s57, 98304\n"
"v_mfma_f32_16x16x32_bf16 v[148:151], v[180:183], a[152:155], v[148:151]\n"
"s_mov_b32 s58, 131072\n"
"v_mfma_f32_16x16x32_bf16 v[152:155], v[180:183], a[156:159], v[152:155]\n"
"v_mfma_f32_16x16x32_bf16 v[156:159], v[180:183], a[220:223], v[156:159]\n"
"v_mfma_f32_16x16x32_bf16 v[160:163], v[180:183], a[56:59], v[160:163]\n"
"v_mfma_f32_16x16x32_bf16 v[164:167], v[180:183], a[60:63], v[164:167]\n"
"s_waitcnt vmcnt(12)\n"
"v_mfma_f32_16x16x16_bf16 v[148:151], v[184:185], v[108:109], v[148:151]\n"
"v_mfma_f32_16x16x16_bf16 v[152:155], v[184:185], v[110:111], v[152:155]\n"
"s_waitcnt vmcnt(11)\n"
"v_mfma_f32_16x16x16_bf16 v[156:159], v[184:185], v[112:113], v[156:159]\n"
"v_mfma_f32_16x16x16_bf16 v[160:163], v[184:185], v[114:115], v[160:163]\n"
"s_waitcnt vmcnt(10)\n"
"v_mfma_f32_16x16x16_bf16 v[164:167], v[184:185], v[116:117], v[164:167]\n"
"s_waitcnt vmcnt(9)\n"
"v_mfma_f32_16x16x16_bf16 v[148:151], v[184:185], v[118:119], v[148:151]\n"
"s_waitcnt vmcnt(3)\n"
"v_cmp_eq_u32 vcc, v26, v31\n"
"s_nop 1\n"
"s_and_b32 s70, vcc_lo, 8\n"
"s_cmp_eq_u32 s70, 8\n"
"s_cbranch_scc0 LS1P0%=\n"
"LB1P0%=:\n"
"v_mfma_f32_16x16x16_bf16 v[152:155], v[184:185], v[120:121], v[152:155]\n"
"buffer_load_dwordx4 v[42:45], v0, s[44:47], s50 offen offset:3584 sc1\n"
"v_mfma_f32_16x16x16_bf16 v[156:159], v[184:185], v[122:123], v[156:159]\n"
"buffer_load_dwordx4 v[46:49], v0, s[44:47], s51 offen offset:512 sc1\n"
"v_mfma_f32_16x16x16_bf16 v[160:163], v[184:185], v[124:125], v[160:163]\n"
"buffer_load_dwordx2 v[50:51], v188, s[44:47], s51 offen offset:1536 sc1\n"
"v_mfma_f32_16x16x16_bf16 v[164:167], v[184:185], v[126:127], v[164:167]\n"
"buffer_load_dword v26, v190, s[36:39], s76 offen offset:128 sc1\n"
"v_mfma_f32_16x16x16_bf16 v[148:151], v[184:185], v[32:33], v[148:151]\n"
"buffer_load_dwordx4 v[128:131], v0, s[44:47], s52 offen offset:512 sc1\n"
"v_mfma_f32_16x16x16_bf16 v[152:155], v[184:185], v[34:35], v[152:155]\n"
"buffer_load_dwordx4 v[132:135], v0, s[44:47], s52 offen offset:1536 sc1\n"
"v_mfma_f32_16x16x16_bf16 v[156:159], v[184:185], v[36:37], v[156:159]\n"
"buffer_load_dwordx2 v[136:137], v188, s[44:47], s52 offen offset:2560 sc1\n"
"v_mfma_f32_16x16x16_bf16 v[160:163], v[184:185], v[38:39], v[160:163]\n"
"v_mfma_f32_16x16x16_bf16 v[164:167], v[184:185], v[40:41], v[164:167]\n"
"s_waitcnt vmcnt(6)\n"
"v_mfma_f32_16x16x16_bf16 v[148:151], v[184:185], v[42:43], v[148:151]\n"
"v_mfma_f32_16x16x16_bf16 v[152:155], v[184:185], v[44:45], v[152:155]\n"
"s_waitcnt vmcnt(5)\n"
"v_mfma_f32_16x16x16_bf16 v[156:159], v[184:185], v[46:47], v[156:159]\n"
"v_mfma_f32_16x16x16_bf16 v[160:163], v[184:185], v[48:49], v[160:163]\n"
"s_waitcnt vmcnt(4)\n"
"v_mfma_f32_16x16x16_bf16 v[164:167], v[184:185], v[50:51], v[164:167]\n"
"s_waitcnt vmcnt(3)\n"
"v_cmp_eq_u32 vcc, v26, v31\n"
"s_nop 1\n"
"s_and_b32 s70, vcc_lo, 16\n"
"s_cmp_eq_u32 s70, 16\n"
"s_cbranch_scc0 LS2P0%=\n"
"LB2P0%=:\n"
"buffer_load_dwordx4 v[52:55], v0, s[44:47], s51 offen offset:2048 sc1\n"
"buffer_load_dwordx4 v[56:59], v0, s[44:47], s51 offen offset:3072 sc1\n"
"buffer_load_dwordx2 v[60:61], v188, s[44:47], s52 offen sc1\n"
"s_waitcnt vmcnt(5)\n"
"v_max3_u32 v28, v128, v129, v130\n"
"buffer_load_dwordx4 v[138:141], v0, s[44:47], s52 offen offset:3072 sc1\n"
"buffer_load_dwordx4 v[142:145], v0, s[44:47], s53 offen sc1\n"
"buffer_load_dwordx2 v[146:147], v188, s[44:47], s53 offen offset:1024 sc1\n"
"s_waitcnt vmcnt(7)\n"
"v_max3_u32 v28, v28, v131, v132\n"
"v_max3_u32 v28, v28, v133, v134\n"
"s_waitcnt vmcnt(6)\n"
"v_max3_u32 v28, v28, v135, v136\n"
"v_max_u32 v28, v28, v137\n"
"v_cmp_eq_u32 vcc, -1, v28\n"
"s_nop 1\n"
"s_cbranch_vccnz LS3P0%=\n"
"LB3P0%=:\n"
"s_nop 1\n"
"v_mfma_f32_16x16x16_bf16 v[148:151], v[184:185], v[128:129], v[148:151]\n"
"v_mfma_f32_16x16x16_bf16 v[152:155], v[184:185], v[130:131], v[152:155]\n"
"v_mfma_f32_16x16x16_bf16 v[156:159], v[184:185], v[132:133], v[156:159]\n"
"v_mfma_f32_16x16x16_bf16 v[160:163], v[184:185], v[134:135], v[160:163]\n"
"v_mfma_f32_16x16x16_bf16 v[164:167], v[184:185], v[136:137], v[164:167]\n"
"s_waitcnt vmcnt(5)\n"
"v_mfma_f32_16x16x16_bf16 v[148:151], v[184:185], v[52:53], v[148:151]\n"
"v_mfma_f32_16x16x16_bf16 v[152:155], v[184:185], v[54:55], v[152:155]\n"
"s_waitcnt vmcnt(4)\n"
"v_mfma_f32_16x16x16_bf16 v[156:159], v[184:185], v[56:57], v[156:159]\n"
"v_mfma_f32_16x16x16_bf16 v[160:163], v[184:185], v[58:59], v[160:163]\n"
"s_waitcnt vmcnt(3)\n"
"v_mfma_f32_16x16x16_bf16 v[164:167], v[184:185], v[60:61], v[164:167]\n"
"s_waitcnt vmcnt(2)\n"
"v_max3_u32 v25, v138, v139, v140\n"
"s_waitcnt vmcnt(1)\n"
"v_max3_u32 v25, v25, v141, v142\n"
"v_max3_u32 v25, v25, v143, v144\n"
"s_waitcnt vmcnt(0)\n"
"v_max3_u32 v25, v25, v145, v146\n"
"v_max_u32 v25, v25, v147\n"
"v_cmp_eq_u32 vcc, -1, v25\n"
"s_nop 1\n"
"s_cbranch_vccnz LS4P0%=\n"
"LB4P0%=:\n"
"v_xor_b32 v19, 0x5bd1e995, v31\n"
"v_add_u32 v19, 1, v19\n"
"buffer_store_dword v19, v191, s[36:39], s76 offen sc1\n"
"v_mfma_f32_16x16x16_bf16 v[148:151], v[184:185], v[138:139], v[148:151]\n"
"v_mfma_f32_16x16x16_bf16 v[152:155], v[184:185], v[140:141], v[152:155]\n"
"v_mfma_f32_16x16x16_bf16 v[156:159], v[184:185], v[142:143], v[156:159]\n"
"v_mfma_f32_16x16x16_bf16 v[160:163], v[184:185], v[144:145], v[160:163]\n"
"v_mfma_f32_16x16x16_bf16 v[164:167], v[184:185], v[146:147], v[164:167]\n"
"s_nop 3\n"
"v_cvt_pk_bf16_f32 v96, v148, v149\n"
"v_cvt_pk_bf16_f32 v97, v150, v151\n"
"buffer_store_dwordx2 v[96:97], v189, s[40:43], 0 offen\n"
"v_cvt_pk_bf16_f32 v98, v152, v153\n"
"v_cvt_pk_bf16_f32 v99, v154, v155\n"
"buffer_store_dwordx2 v[98:99], v189, s[40:43], s55 offen\n"
"v_cvt_pk_bf16_f32 v100, v156, v157\n"
"v_cvt_pk_bf16_f32 v101, v158, v159\n"
"buffer_store_dwordx2 v[100:101], v189, s[40:43], s56 offen\n"
"v_cvt_pk_bf16_f32 v102, v160, v161\n"
"v_cvt_pk_bf16_f32 v103, v162, v163\n"
"buffer_store_dwordx2 v[102:103], v189, s[40:43], s57 offen\n"
"v_cvt_pk_bf16_f32 v104, v164, v165\n"
"v_cvt_pk_bf16_f32 v105, v166, v167\n"
"buffer_store_dwordx2 v[104:105], v189, s[40:43], s58 offen\n"
"s_branch LEND%=\n"
"LS0P0%=:\n"
"s_movk_i32 s77, 0x4000\n"
"LL0P0%=:\n"
"buffer_load_dword v26, v190, s[36:39], s76 offen offset:128 sc1\n"
"s_waitcnt vmcnt(0)\n"
"v_cmp_eq_u32 vcc, v26, v31\n"
"s_nop 1\n"
"s_and_b32 s70, vcc_lo, 7\n"
"s_cmp_eq_u32 s70, 7\n"
"s_cbranch_scc1 LB0P0%=\n"
"s_sub_u32 s77, s77, 1\n"
"s_cbranch_scc0 LL0P0%=\n"
"s_branch LB0P0%=\n"
"LS1P0%=:\n"
"s_movk_i32 s77, 0x4000\n"
"LL1P0%=:\n"
"buffer_load_dword v26, v190, s[36:39], s76 offen offset:128 sc1\n"
"s_waitcnt vmcnt(0)\n"
"v_cmp_eq_u32 vcc, v26, v31\n"
"s_nop 1\n"
"s_and_b32 s70, vcc_lo, 8\n"
"s_cmp_eq_u32 s70, 8\n"
"s_cbranch_scc1 LB1P0%=\n"
"s_sub_u32 s77, s77, 1\n"
"s_cbranch_scc0 LL1P0%=\n"
"s_branch LB1P0%=\n"
"LS2P0%=:\n"
"s_movk_i32 s77, 0x4000\n"
"LL2P0%=:\n"
"buffer_load_dword v26, v190, s[36:39], s76 offen offset:128 sc1\n"
"s_waitcnt vmcnt(0)\n"
"v_cmp_eq_u32 vcc, v26, v31\n"
"s_nop 1\n"
"s_and_b32 s70, vcc_lo, 16\n"
"s_cmp_eq_u32 s70, 16\n"
"s_cbranch_scc1 LB2P0%=\n"
"s_sub_u32 s77, s77, 1\n"
"s_cbranch_scc0 LL2P0%=\n"
"s_branch LB2P0%=\n"
"LS3P0%=:\n"
"s_movk_i32 s77, 0x1000\n"
"LL3P0%=:\n"
"buffer_load_dwordx4 v[128:131], v0, s[44:47], s52 offen offset:512 sc1\n"
"buffer_load_dwordx4 v[132:135], v0, s[44:47], s52 offen offset:1536 sc1\n"
"buffer_load_dwordx2 v[136:137], v188, s[44:47], s52 offen offset:2560 sc1\n"
"s_waitcnt vmcnt(0)\n"
"v_max3_u32 v28, v128, v129, v130\n"
"v_max3_u32 v28, v28, v131, v132\n"
"v_max3_u32 v28, v28, v133, v134\n"
"v_max3_u32 v28, v28, v135, v136\n"
"v_max_u32 v28, v28, v137\n"
"v_cmp_eq_u32 vcc, -1, v28\n"
"s_nop 1\n"
"s_cbranch_vccz LB3P0%=\n"
"s_sub_u32 s77, s77, 1\n"
"s_cbranch_scc0 LL3P0%=\n"
"s_branch LB3P0%=\n"
"LS4P0%=:\n"
"s_movk_i32 s77, 0x1000\n"
"LL4P0%=:\n"
"buffer_load_dwordx4 v[138:141], v0, s[44:47], s52 offen offset:3072 sc1\n"
"buffer_load_dwordx4 v[142:145], v0, s[44:47], s53 offen sc1\n"
"buffer_load_dwordx2 v[146:147], v188, s[44:47], s53 offen offset:1024 sc1\n"
"s_waitcnt vmcnt(0)\n"
"v_max3_u32 v25, v138, v139, v140\n"
"v_max3_u32 v25, v25, v141, v142\n"
"v_max3_u32 v25, v25, v143, v144\n"
"v_max3_u32 v25, v25, v145, v146\n"
"v_max_u32 v25, v25, v147\n"
"v_cmp_eq_u32 vcc, -1, v25\n"
"s_nop 1\n"
"s_cbranch_vccz LB4P0%=\n"
"s_sub_u32 s77, s77, 1\n"
"s_cbranch_scc0 LL4P0%=\n"
"s_branch LB4P0%=\n"
"LG1%=:\n"
"s_add_u32 s12, s12, s49\n"
"s_addc_u32 s13, s13, 0\n"
"s_add_u32 s20, s20, s73\n"
"s_addc_u32 s21, s21, 0\n"
"buffer_load_dwordx4 v[64:67], v0, s[20:23], s54 offen\n"
"buffer_load_dwordx4 v[68:71], v1, s[20:23], s54 offen\n"
"buffer_load_dwordx4 v[72:75], v2, s[20:23], s54 offen\n"
"buffer_load_dwordx4 v[76:79], v3, s[20:23], s54 offen\n"
"buffer_load_dwordx4 v[80:83], v4, s[20:23], s54 offen\n"
"buffer_load_dwordx4 v[84:87], v5, s[20:23], s54 offen\n"
"buffer_load_dwordx4 v[88:91], v6, s[20:23], s54 offen\n"
"buffer_load_dwordx4 v[92:95], v7, s[20:23], s54 offen\n"
"s_add_u32 s8, s8, s48\n"
"s_addc_u32 s9, s9, 0\n"
"s_add_u32 s16, s16, s49\n"
"s_addc_u32 s17, s17, 0\n"
"s_lshr_b32 s72, s54, 12\n"
"s_and_b32 s72, s72, 768\n"
"s_lshl_b32 s73, %2, 10\n"
"s_add_u32 m0, s73, 0\n"
"s_nop 0\n"
"buffer_load_dwordx4 v14, s[8:11], s72 offen lds\n"
"s_add_u32 s72, s72, 16384\n"
"s_add_u32 m0, s73, 4096\n"
"s_nop 0\n"
"buffer_load_dwordx4 v14, s[8:11], s72 offen lds\n"
"s_add_u32 s72, s72, 16384\n"
"s_add_u32 m0, s73, 8192\n"
"s_nop 0\n"
"buffer_load_dwordx4 v14, s[8:11], s72 offen lds\n"
"s_add_u32 s72, s72, 16384\n"
"s_add_u32 m0, s73, 12288\n"
"s_nop 0\n"
"buffer_load_dwordx4 v14, s[8:11], s72 offen lds\n"
"s_add_u32 s72, s72, 16384\n"
"s_add_u32 m0, s73, 16384\n"
"s_nop 0\n"
"buffer_load_dwordx4 v14, s[8:11], s72 offen lds\n"
"s_add_u32 s76, s33, 4\n"
"s_and_b32 s76, s76, 15\n"
"s_lshl_b32 s58, s76, 18\n"
"s_lshl_b32 s73, %2, 10\n"
"s_lshr_b32 s72, s58, 12\n"
"s_add_u32 m0, s73, 20480\n"
"s_and_b32 s72, s72, 768\n"
"s_bfe_u32 s6, %1, 0x10002\n"
"s_mov_b32 s35, s77\n"
"s_lshl_b32 s70, s34, 19\n"
"s_lshl_b32 s6, s6, 4\n"
"s_add_u32 s76, s33, 8\n"
"s_add_u32 s24, s24, s70\n"
"s_addc_u32 s25, s25, 0\n"
"s_add_u32 s6, s6, s35\n"
"s_and_b32 s76, s76, 15\n"
"s_lshl2_add_u32 s70, s6, %2\n"
"s_lshl_b32 s62, s76, 18\n"
"s_lshl_b32 s70, s70, 11\n"
"s_add_u32 s76, s33, 12\n"
"s_add_u32 s70, s70, 20971520\n"
"s_and_b32 s76, s76, 15\n"
"s_add_u32 s36, s28, s70\n"
"s_addc_u32 s37, s29, 0\n"
"s_lshl_b32 s66, s76, 18\n"
"s_mul_i32 s70, s6, 655360\n"
"s_mul_i32 s71, %2, 20480\n"
"s_add_u32 s70, s70, s71\n"
"s_mul_i32 s76, s34, 81920\n"
"s_add_u32 s76, s76, s70\n"
"s_add_u32 s44, s28, s76\n"
"s_addc_u32 s45, s29, 0\n"
"s_sub_u32 s30, 655360, s71\n"
"s_add_u32 s28, s28, s70\n"
"s_addc_u32 s29, s29, 0\n"
"s_lshr_b32 s71, s6, 4\n"
"s_mul_i32 s70, s35, 163840\n"
"s_lshl_b32 s71, s71, 10\n"
"s_add_u32 s70, s70, s71\n"
"s_lshl2_add_u32 s71, s34, %2\n"
"s_lshl_b32 s71, s71, 5\n"
"s_add_u32 s70, s70, s71\n"
"v_xor_b32 v19, v17, v18\n"
"v_lshlrev_b32 v19, 4, v19\n"
"s_and_b32 s71, s33, 3\n"
"s_add_u32 s40, s40, s70\n"
"s_addc_u32 s41, s41, 0\n"
"s_sub_u32 s42, 2621440, s70\n"
"v_lshl_add_u32 v19, v18, 8, v19\n"
"s_lshl_b32 s71, s71, 6\n"
"v_xor_b32 v8, s71, v19\n"
"s_add_u32 s70, s34, 7\n"
"v_sub_u32 v19, s70, v16\n"
"v_and_b32 v19, 7, v19\n"
"s_mov_b32 s38, 2048\n"
"s_mov_b32 s39, 0x20000\n"
"v_lshlrev_b32 v19, 8, v19\n"
"s_xor_b32 s59, s58, 262144\n"
"buffer_load_dwordx4 v[96:99], v0, s[20:23], s55 offen\n"
"s_mov_b32 s48, 0x05040100\n"
"s_mov_b32 s49, 0x07060302\n"
"s_xor_b32 s60, s58, 524288\n"
"s_waitcnt vmcnt(12)\n"
"v_perm_b32 v36, v68, v64, s49\n"
"v_perm_b32 v40, v69, v65, s48\n"
"v_perm_b32 v44, v69, v65, s49\n"
"v_perm_b32 v48, v70, v66, s48\n"
"v_perm_b32 v52, v70, v66, s49\n"
"v_perm_b32 v56, v71, v67, s48\n"
"v_perm_b32 v60, v71, v67, s49\n"
"s_waitcnt vmcnt(10)\n"
"v_perm_b32 v33, v76, v72, s48\n"
"v_perm_b32 v37, v76, v72, s49\n"
"v_perm_b32 v41, v77, v73, s48\n"
"v_perm_b32 v45, v77, v73, s49\n"
"v_perm_b32 v49, v78, v74, s48\n"
"v_perm_b32 v53, v78, v74, s49\n"
"v_perm_b32 v57, v79, v75, s48\n"
"v_perm_b32 v61, v79, v75, s49\n"
"s_waitcnt vmcnt(8)\n"
"v_perm_b32 v34, v84, v80, s48\n"
"v_perm_b32 v38, v84, v80, s49\n"
"v_perm_b32 v42, v85, v81, s48\n"
"v_perm_b32 v46, v85, v81, s49\n"
"v_perm_b32 v50, v86, v82, s48\n"
"v_perm_b32 v54, v86, v82, s49\n"
"v_perm_b32 v58, v87, v83, s48\n"
"v_perm_b32 v62, v87, v83, s49\n"
"s_xor_b32 s61, s58, 786432\n"
"s_xor_b32 s63, s62, 262144\n"
"s_xor_b32 s64, s62, 524288\n"
"s_xor_b32 s65, s62, 786432\n"
"s_xor_b32 s67, s66, 262144\n"
"s_xor_b32 s68, s66, 524288\n"
"s_xor_b32 s69, s66, 786432\n"
"v_xor_b32 v9, 64, v8\n"
"v_xor_b32 v10, 128, v8\n"
"v_xor_b32 v11, 192, v8\n"
"v_and_b32 v26, 11, v18\n"
"v_lshlrev_b32 v23, 2, v17\n"
"v_xor_b32 v26, v26, v23\n"
"v_lshlrev_b32 v26, 4, v26\n"
"v_lshl_add_u32 v26, v18, 8, v26\n"
"v_add_u32 v23, s75, v26\n"
"v_xor_b32 v24, 16, v23\n"
"v_xor_b32 v25, 32, v23\n"
"v_xor_b32 v26, 48, v23\n"
"s_mov_b32 s18, 20480\n"
"s_mov_b32 s19, 0x20000\n"
"s_mov_b32 s26, 524288\n"
"s_mov_b32 s27, 0x20000\n"
"s_mov_b32 s74, 0x3e0293ee\n"
"s_mov_b32 s46, 20480\n"
"s_mov_b32 s47, 0x20000\n"
"v_mov_b32 v15, -1\n"
"s_lshl_b32 s76, s34, 8\n"
"s_mov_b32 s7, s78\n"
"s_mov_b32 s31, 0x20000\n"
"s_mov_b32 s43, 0x20000\n"
"s_mov_b32 s79, 0\n"
"buffer_load_dwordx4 v[100:103], v1, s[20:23], s55 offen\n"
"buffer_load_dwordx4 v[104:107], v2, s[20:23], s55 offen\n"
"buffer_load_dwordx4 v[108:111], v3, s[20:23], s55 offen\n"
"buffer_load_dwordx4 v[112:115], v4, s[20:23], s55 offen\n"
"s_waitcnt vmcnt(10)\n"
"v_perm_b32 v32, v68, v64, s48\n"
"v_perm_b32 v35, v92, v88, s48\n"
"v_perm_b32 v39, v92, v88, s49\n"
"v_perm_b32 v43, v93, v89, s48\n"
"v_perm_b32 v47, v93, v89, s49\n"
"v_perm_b32 v51, v94, v90, s48\n"
"v_perm_b32 v55, v94, v90, s49\n"
"v_perm_b32 v59, v95, v91, s48\n"
"v_perm_b32 v63, v95, v91, s49\n"
"buffer_load_dwordx4 v[116:119], v5, s[20:23], s55 offen\n"
"buffer_load_dwordx4 v[120:123], v6, s[20:23], s55 offen\n"
"buffer_load_dwordx4 v[124:127], v7, s[20:23], s55 offen\n"
"buffer_load_dwordx4 v14, s[8:11], s72 offen lds\n"
"s_add_u32 m0, s73, 24576\n"
"s_add_u32 s72, s72, 16384\n"
"buffer_load_dwordx4 v14, s[8:11], s72 offen lds\n"
"s_add_u32 m0, s73, 28672\n"
"s_add_u32 s72, s72, 16384\n"
"s_waitcnt vmcnt(10)\n"
"s_barrier\n"
"ds_read_b128 a[160:163], v8 offset:0\n"
"ds_read_b128 a[164:167], v8 offset:4096\n"
"ds_read_b128 a[168:171], v8 offset:8192\n"
"ds_read_b128 a[172:175], v8 offset:12288\n"
"ds_read_b128 a[176:179], v8 offset:16384\n"
"ds_read_b128 a[180:183], v9 offset:0\n"
"ds_read_b128 a[184:187], v9 offset:4096\n"
"buffer_load_dwordx4 v14, s[8:11], s72 offen lds\n"
"s_add_u32 m0, s73, 32768\n"
"s_add_u32 s72, s72, 16384\n"
"ds_read_b128 a[188:191], v9 offset:8192\n"
"ds_read_b128 a[192:195], v9 offset:12288\n"
"ds_read_b128 a[196:199], v9 offset:16384\n"
"buffer_load_dwordx4 v14, s[8:11], s72 offen lds\n"
"s_add_u32 m0, s73, 36864\n"
"s_add_u32 s72, s72, 16384\n"
"s_lshl_b32 s73, %2, 10\n"
"s_waitcnt lgkmcnt(9)\n"
"v_mfma_f32_16x16x32_bf16 a[0:3], v[32:35], a[160:163], 0\n"
"s_waitcnt lgkmcnt(8)\n"
"v_mfma_f32_16x16x32_bf16 a[32:35], v[32:35], a[164:167], 0\n"
"s_waitcnt lgkmcnt(7)\n"
"v_mfma_f32_16x16x32_bf16 a[64:67], v[32:35], a[168:171], 0\n"
"s_waitcnt lgkmcnt(6)\n"
"buffer_load_dwordx4 v14, s[8:11], s72 offen lds\n"
"v_mfma_f32_16x16x32_bf16 a[96:99], v[32:35], a[172:175], 0\n"
"s_lshr_b32 s72, s62, 12\n"
"s_add_u32 m0, s73, 40960\n"
"s_and_b32 s72, s72, 768\n"
"s_waitcnt lgkmcnt(5)\n"
"v_mfma_f32_16x16x32_bf16 a[128:131], v[32:35], a[176:179], 0\n"
"v_mfma_f32_16x16x32_bf16 a[4:7], v[36:39], a[160:163], 0\n"
"v_mfma_f32_16x16x32_bf16 a[36:39], v[36:39], a[164:167], 0\n"
"buffer_load_dword v31, v19, s[36:39], 0 offen sc1\n"
"v_mfma_f32_16x16x32_bf16 a[68:71], v[36:39], a[168:171], 0\n"
"s_waitcnt vmcnt(10) lgkmcnt(3)\n"
"v_perm_b32 v33, v108, v104, s48\n"
"v_lshlrev_b32 v19, 1, v17\n"
"v_mfma_f32_16x16x32_bf16 a[100:103], v[36:39], a[172:175], 0\n"
"v_xor_b32 v19, v19, v18\n"
"v_lshlrev_b32 v19, 4, v19\n"
"v_mfma_f32_16x16x32_bf16 a[132:135], v[36:39], a[176:179], 0\n"
"v_perm_b32 v36, v100, v96, s49\n"
"v_perm_b32 v37, v108, v104, s49\n"
"v_mfma_f32_16x16x32_bf16 a[8:11], v[40:43], a[160:163], 0\n"
"v_mfma_f32_16x16x32_bf16 a[40:43], v[40:43], a[164:167], 0\n"
"buffer_load_dwordx4 v[128:131], v0, s[20:23], s56 offen\n"
"v_mfma_f32_16x16x32_bf16 a[72:75], v[40:43], a[168:171], 0\n"
"v_mfma_f32_16x16x32_bf16 a[104:107], v[40:43], a[172:175], 0\n"
"v_mfma_f32_16x16x32_bf16 a[136:139], v[40:43], a[176:179], 0\n"
"v_perm_b32 v40, v101, v97, s48\n"
"v_perm_b32 v41, v109, v105, s48\n"
"v_mfma_f32_16x16x32_bf16 a[12:15], v[44:47], a[160:163], 0\n"
"s_waitcnt vmcnt(9) lgkmcnt(0)\n"
"v_perm_b32 v34, v116, v112, s48\n"
"v_perm_b32 v38, v116, v112, s49\n"
"v_mfma_f32_16x16x32_bf16 a[44:47], v[44:47], a[164:167], 0\n"
"v_perm_b32 v42, v117, v113, s48\n"
"v_mfma_f32_16x16x32_bf16 a[76:79], v[44:47], a[168:171], 0\n"
"v_mfma_f32_16x16x32_bf16 a[108:111], v[44:47], a[172:175], 0\n"
"v_mfma_f32_16x16x32_bf16 a[140:143], v[44:47], a[176:179], 0\n"
"v_perm_b32 v44, v101, v97, s49\n"
"v_perm_b32 v45, v109, v105, s49\n"
"v_mfma_f32_16x16x32_bf16 a[16:19], v[48:51], a[160:163], 0\n"
"v_perm_b32 v46, v117, v113, s49\n"
"v_mfma_f32_16x16x32_bf16 a[48:51], v[48:51], a[164:167], 0\n"
"v_mfma_f32_16x16x32_bf16 a[80:83], v[48:51], a[168:171], 0\n"
"v_mfma_f32_16x16x32_bf16 a[112:115], v[48:51], a[172:175], 0\n"
"s_waitcnt vmcnt(7)\n"
"v_perm_b32 v32, v100, v96, s48\n"
"v_perm_b32 v35, v124, v120, s48\n"
"v_mfma_f32_16x16x32_bf16 a[144:147], v[48:51], a[176:179], 0\n"
"v_perm_b32 v48, v102, v98, s48\n"
"v_perm_b32 v49, v110, v106, s48\n"
"v_mfma_f32_16x16x32_bf16 a[20:23], v[52:55], a[160:163], 0\n"
"v_perm_b32 v50, v118, v114, s48\n"
"v_perm_b32 v39, v124, v120, s49\n"
"v_mfma_f32_16x16x32_bf16 a[52:55], v[52:55], a[164:167], 0\n"
"v_perm_b32 v43, v125, v121, s48\n"
"v_perm_b32 v47, v125, v121, s49\n"
"v_mfma_f32_16x16x32_bf16 a[84:87], v[52:55], a[168:171], 0\n"
"v_perm_b32 v51, v126, v122, s48\n"
"v_mfma_f32_16x16x32_bf16 a[116:119], v[52:55], a[172:175], 0\n"
"v_mfma_f32_16x16x32_bf16 a[148:151], v[52:55], a[176:179], 0\n"
"v_perm_b32 v52, v102, v98, s49\n"
"v_perm_b32 v53, v110, v106, s49\n"
"v_mfma_f32_16x16x32_bf16 a[24:27], v[56:59], a[160:163], 0\n"
"v_perm_b32 v54, v118, v114, s49\n"
"v_perm_b32 v55, v126, v122, s49\n"
"v_mfma_f32_16x16x32_bf16 a[56:59], v[56:59], a[164:167], 0\n"
"v_mfma_f32_16x16x32_bf16 a[88:91], v[56:59], a[168:171], 0\n"
"v_mfma_f32_16x16x32_bf16 a[120:123], v[56:59], a[172:175], 0\n"
"v_mfma_f32_16x16x32_bf16 a[152:155], v[56:59], a[176:179], 0\n"
"v_perm_b32 v56, v103, v99, s48\n"
"v_perm_b32 v57, v111, v107, s48\n"
"v_mfma_f32_16x16x32_bf16 a[28:31], v[60:63], a[160:163], 0\n"
"v_perm_b32 v58, v119, v115, s48\n"
"v_perm_b32 v59, v127, v123, s48\n"
"v_mfma_f32_16x16x32_bf16 a[60:63], v[60:63], a[164:167], 0\n"
"ds_read_b128 a[160:163], v10 offset:0\n"
"v_mfma_f32_16x16x32_bf16 a[92:95], v[60:63], a[168:171], 0\n"
"ds_read_b128 a[164:167], v10 offset:4096\n"
"v_mfma_f32_16x16x32_bf16 a[124:127], v[60:63], a[172:175], 0\n"
"ds_read_b128 a[168:171], v10 offset:8192\n"
"v_mfma_f32_16x16x32_bf16 a[156:159], v[60:63], a[176:179], 0\n"
"v_perm_b32 v60, v103, v99, s49\n"
"v_perm_b32 v61, v111, v107, s49\n"
"v_mfma_f32_16x16x32_bf16 a[0:3], v[32:35], a[180:183], a[0:3]\n"
"v_perm_b32 v62, v119, v115, s49\n"
"v_perm_b32 v63, v127, v123, s49\n"
"v_mfma_f32_16x16x32_bf16 a[32:35], v[32:35], a[184:187], a[32:35]\n"
"ds_read_b128 a[172:175], v10 offset:12288\n"
"v_mfma_f32_16x16x32_bf16 a[64:67], v[32:35], a[188:191], a[64:67]\n"
"ds_read_b128 a[176:179], v10 offset:16384\n"
"v_mfma_f32_16x16x32_bf16 a[96:99], v[32:35], a[192:195], a[96:99]\n"
"v_mfma_f32_16x16x32_bf16 a[128:131], v[32:35], a[196:199], a[128:131]\n"
"v_mfma_f32_16x16x32_bf16 a[4:7], v[36:39], a[180:183], a[4:7]\n"
"v_mfma_f32_16x16x32_bf16 a[36:39], v[36:39], a[184:187], a[36:39]\n"
"v_mfma_f32_16x16x32_bf16 a[68:71], v[36:39], a[188:191], a[68:71]\n"
"v_mfma_f32_16x16x32_bf16 a[100:103], v[36:39], a[192:195], a[100:103]\n"
"v_mfma_f32_16x16x32_bf16 a[132:135], v[36:39], a[196:199], a[132:135]\n"
"v_mfma_f32_16x16x32_bf16 a[8:11], v[40:43], a[180:183], a[8:11]\n"
"v_mfma_f32_16x16x32_bf16 a[40:43], v[40:43], a[184:187], a[40:43]\n"
"v_mfma_f32_16x16x32_bf16 a[72:75], v[40:43], a[188:191], a[72:75]\n"
"v_mfma_f32_16x16x32_bf16 a[104:107], v[40:43], a[192:195], a[104:107]\n"
"v_mfma_f32_16x16x32_bf16 a[136:139], v[40:43], a[196:199], a[136:139]\n"
"v_mfma_f32_16x16x32_bf16 a[12:15], v[44:47], a[180:183], a[12:15]\n"
"v_mfma_f32_16x16x32_bf16 a[44:47], v[44:47], a[184:187], a[44:47]\n"
"v_mfma_f32_16x16x32_bf16 a[76:79], v[44:47], a[188:191], a[76:79]\n"
"v_mfma_f32_16x16x32_bf16 a[108:111], v[44:47], a[192:195], a[108:111]\n"
"v_mfma_f32_16x16x32_bf16 a[140:143], v[44:47], a[196:199], a[140:143]\n"
"v_mfma_f32_16x16x32_bf16 a[16:19], v[48:51], a[180:183], a[16:19]\n"
"v_mfma_f32_16x16x32_bf16 a[48:51], v[48:51], a[184:187], a[48:51]\n"
"buffer_load_dwordx4 v[132:135], v1, s[20:23], s56 offen\n"
"v_mfma_f32_16x16x32_bf16 a[80:83], v[48:51], a[188:191], a[80:83]\n"
"v_mfma_f32_16x16x32_bf16 a[112:115], v[48:51], a[192:195], a[112:115]\n"
"v_mfma_f32_16x16x32_bf16 a[144:147], v[48:51], a[196:199], a[144:147]\n"
"v_mfma_f32_16x16x32_bf16 a[20:23], v[52:55], a[180:183], a[20:23]\n"
"buffer_load_dwordx4 v[136:139], v2, s[20:23], s56 offen\n"
"v_mfma_f32_16x16x32_bf16 a[52:55], v[52:55], a[184:187], a[52:55]\n"
"v_mfma_f32_16x16x32_bf16 a[84:87], v[52:55], a[188:191], a[84:87]\n"
"v_mfma_f32_16x16x32_bf16 a[116:119], v[52:55], a[192:195], a[116:119]\n"
"v_mfma_f32_16x16x32_bf16 a[148:151], v[52:55], a[196:199], a[148:151]\n"
"buffer_load_dwordx4 v[140:143], v3, s[20:23], s56 offen\n"
"v_mfma_f32_16x16x32_bf16 a[24:27], v[56:59], a[180:183], a[24:27]\n"
"v_mfma_f32_16x16x32_bf16 a[56:59], v[56:59], a[184:187], a[56:59]\n"
"v_mfma_f32_16x16x32_bf16 a[88:91], v[56:59], a[188:191], a[88:91]\n"
"v_mfma_f32_16x16x32_bf16 a[120:123], v[56:59], a[192:195], a[120:123]\n"
"v_mfma_f32_16x16x32_bf16 a[152:155], v[56:59], a[196:199], a[152:155]\n"
"buffer_load_dwordx4 v[144:147], v4, s[20:23], s56 offen\n"
"v_mfma_f32_16x16x32_bf16 a[28:31], v[60:63], a[180:183], a[28:31]\n"
"ds_read_b128 a[180:183], v11 offset:0\n"
"v_mfma_f32_16x16x32_bf16 a[60:63], v[60:63], a[184:187], a[60:63]\n"
"ds_read_b128 a[184:187], v11 offset:4096\n"
"v_mfma_f32_16x16x32_bf16 a[92:95], v[60:63], a[188:191], a[92:95]\n"
"ds_read_b128 a[188:191], v11 offset:8192\n"
"v_mfma_f32_16x16x32_bf16 a[124:127], v[60:63], a[192:195], a[124:127]\n"
"buffer_load_dwordx4 v[148:151], v5, s[20:23], s56 offen\n"
"v_mfma_f32_16x16x32_bf16 a[156:159], v[60:63], a[196:199], a[156:159]\n"
"ds_read_b128 a[192:195], v11 offset:12288\n"
"ds_read_b128 a[196:199], v11 offset:16384\n"
"buffer_load_dwordx4 v[152:155], v6, s[20:23], s56 offen\n"
"buffer_load_dwordx4 v[156:159], v7, s[20:23], s56 offen\n"
"buffer_load_dwordx4 v[160:163], v0, s[20:23], s57 offen\n"
"buffer_load_dwordx4 v[164:167], v1, s[20:23], s57 offen\n"
"s_waitcnt vmcnt(10) lgkmcnt(0)\n"
"v_xor_b32 v31, 0x5bd1e995, v31\n"
"buffer_load_dwordx4 v[168:171], v2, s[20:23], s57 offen\n"
"buffer_load_dwordx4 v[172:175], v3, s[20:23], s57 offen\n"
"s_waitcnt vmcnt(10)\n"
"v_perm_b32 v36, v132, v128, s49\n"
"v_perm_b32 v40, v133, v129, s48\n"
"v_perm_b32 v44, v133, v129, s49\n"
"v_perm_b32 v48, v134, v130, s48\n"
"v_perm_b32 v52, v134, v130, s49\n"
"v_perm_b32 v56, v135, v131, s48\n"
"v_perm_b32 v60, v135, v131, s49\n"
"buffer_load_dwordx4 v[176:179], v4, s[20:23], s57 offen\n"
"buffer_load_dwordx4 v[180:183], v5, s[20:23], s57 offen\n"
"s_waitcnt vmcnt(10)\n"
"v_perm_b32 v33, v140, v136, s48\n"
"v_perm_b32 v37, v140, v136, s49\n"
"v_perm_b32 v41, v141, v137, s48\n"
"v_perm_b32 v45, v141, v137, s49\n"
"v_perm_b32 v49, v142, v138, s48\n"
"v_perm_b32 v53, v142, v138, s49\n"
"v_perm_b32 v57, v143, v139, s48\n"
"v_perm_b32 v61, v143, v139, s49\n"
"buffer_load_dwordx4 v[184:187], v6, s[20:23], s57 offen\n"
"buffer_load_dwordx4 v[188:191], v7, s[20:23], s57 offen\n"
"s_waitcnt vmcnt(10)\n"
"v_perm_b32 v34, v148, v144, s48\n"
"v_perm_b32 v38, v148, v144, s49\n"
"v_perm_b32 v42, v149, v145, s48\n"
"v_perm_b32 v46, v149, v145, s49\n"
"v_perm_b32 v50, v150, v146, s48\n"
"v_perm_b32 v54, v150, v146, s49\n"
"v_perm_b32 v58, v151, v147, s48\n"
"v_perm_b32 v62, v151, v147, s49\n"
"buffer_load_dwordx4 v[192:195], v0, s[20:23], s58 offen\n"
"buffer_load_dwordx4 v[196:199], v1, s[20:23], s58 offen\n"
"s_waitcnt vmcnt(10)\n"
"v_perm_b32 v32, v132, v128, s48\n"
"v_perm_b32 v35, v156, v152, s48\n"
"v_perm_b32 v39, v156, v152, s49\n"
"v_perm_b32 v43, v157, v153, s48\n"
"v_mfma_f32_16x16x32_bf16 a[0:3], v[32:35], a[160:163], a[0:3]\n"
"v_perm_b32 v47, v157, v153, s49\n"
"v_perm_b32 v51, v158, v154, s48\n"
"v_mfma_f32_16x16x32_bf16 a[32:35], v[32:35], a[164:167], a[32:35]\n"
"v_perm_b32 v55, v158, v154, s49\n"
"v_perm_b32 v59, v159, v155, s48\n"
"v_mfma_f32_16x16x32_bf16 a[64:67], v[32:35], a[168:171], a[64:67]\n"
"v_perm_b32 v63, v159, v155, s49\n"
"v_mfma_f32_16x16x32_bf16 a[96:99], v[32:35], a[172:175], a[96:99]\n"
"buffer_load_dwordx4 v[200:203], v2, s[20:23], s58 offen\n"
"v_mfma_f32_16x16x32_bf16 a[128:131], v[32:35], a[176:179], a[128:131]\n"
"v_mfma_f32_16x16x32_bf16 a[4:7], v[36:39], a[160:163], a[4:7]\n"
"v_mfma_f32_16x16x32_bf16 a[36:39], v[36:39], a[164:167], a[36:39]\n"
"v_mfma_f32_16x16x32_bf16 a[68:71], v[36:39], a[168:171], a[68:71]\n"
"buffer_load_dwordx4 v[204:207], v3, s[20:23], s58 offen\n"
"v_mfma_f32_16x16x32_bf16 a[100:103], v[36:39], a[172:175], a[100:103]\n"
"v_mfma_f32_16x16x32_bf16 a[132:135], v[36:39], a[176:179], a[132:135]\n"
"s_waitcnt vmcnt(10)\n"
"v_perm_b32 v36, v164, v160, s49\n"
"v_mfma_f32_16x16x32_bf16 a[8:11], v[40:43], a[160:163], a[8:11]\n"
"v_mfma_f32_16x16x32_bf16 a[40:43], v[40:43], a[164:167], a[40:43]\n"
"buffer_load_dwordx4 v[208:211], v4, s[20:23], s58 offen\n"
"v_mfma_f32_16x16x32_bf16 a[72:75], v[40:43], a[168:171], a[72:75]\n"
"v_mfma_f32_16x16x32_bf16 a[104:107], v[40:43], a[172:175], a[104:107]\n"
"v_mfma_f32_16x16x32_bf16 a[136:139], v[40:43], a[176:179], a[136:139]\n"
"v_perm_b32 v40, v165, v161, s48\n"
"v_mfma_f32_16x16x32_bf16 a[12:15], v[44:47], a[160:163], a[12:15]\n"
"buffer_load_dwordx4 v[212:215], v5, s[20:23], s58 offen\n"
"v_mfma_f32_16x16x32_bf16 a[44:47], v[44:47], a[164:167], a[44:47]\n"
"s_waitcnt vmcnt(10)\n"
"v_perm_b32 v33, v172, v168, s48\n"
"v_perm_b32 v37, v172, v168, s49\n"
"v_mfma_f32_16x16x32_bf16 a[76:79], v[44:47], a[168:171], a[76:79]\n"
"v_perm_b32 v41, v173, v169, s48\n"
"v_mfma_f32_16x16x32_bf16 a[108:111], v[44:47], a[172:175], a[108:111]\n"
"v_mfma_f32_16x16x32_bf16 a[140:143], v[44:47], a[176:179], a[140:143]\n"
"buffer_load_dwordx4 v[216:219], v6, s[20:23], s58 offen\n"
"v_mfma_f32_16x16x32_bf16 a[16:19], v[48:51], a[160:163], a[16:19]\n"
"v_perm_b32 v44, v165, v161, s49\n"
"v_perm_b32 v45, v173, v169, s49\n"
"v_mfma_f32_16x16x32_bf16 a[48:51], v[48:51], a[164:167], a[48:51]\n"
"v_mfma_f32_16x16x32_bf16 a[80:83], v[48:51], a[168:171], a[80:83]\n"
"v_mfma_f32_16x16x32_bf16 a[112:115], v[48:51], a[172:175], a[112:115]\n"
"buffer_load_dwordx4 v[220:223], v7, s[20:23], s58 offen\n"
"v_mfma_f32_16x16x32_bf16 a[144:147], v[48:51], a[176:179], a[144:147]\n"
"v_perm_b32 v48, v166, v162, s48\n"
"v_perm_b32 v49, v174, v170, s48\n"
"v_mfma_f32_16x16x32_bf16 a[20:23], v[52:55], a[160:163], a[20:23]\n"
"s_waitcnt vmcnt(10)\n"
"v_perm_b32 v34, v180, v176, s48\n"
"v_perm_b32 v38, v180, v176, s49\n"
"v_mfma_f32_16x16x32_bf16 a[52:55], v[52:55], a[164:167], a[52:55]\n"
"v_perm_b32 v42, v181, v177, s48\n"
"v_perm_b32 v46, v181, v177, s49\n"
"v_mfma_f32_16x16x32_bf16 a[84:87], v[52:55], a[168:171], a[84:87]\n"
"buffer_load_dwordx4 v[224:227], v0, s[20:23], s59 offen\n"
"v_mfma_f32_16x16x32_bf16 a[116:119], v[52:55], a[172:175], a[116:119]\n"
"v_perm_b32 v50, v182, v178, s48\n"
"v_mfma_f32_16x16x32_bf16 a[148:151], v[52:55], a[176:179], a[148:151]\n"
"v_perm_b32 v52, v166, v162, s49\n"
"v_perm_b32 v53, v174, v170, s49\n"
"v_mfma_f32_16x16x32_bf16 a[24:27], v[56:59], a[160:163], a[24:27]\n"
"v_perm_b32 v54, v182, v178, s49\n"
"v_mfma_f32_16x16x32_bf16 a[56:59], v[56:59], a[164:167], a[56:59]\n"
"buffer_load_dwordx4 v[228:231], v1, s[20:23], s59 offen\n"
"v_mfma_f32_16x16x32_bf16 a[88:91], v[56:59], a[168:171], a[88:91]\n"
"s_waitcnt vmcnt(10)\n"
"v_perm_b32 v32, v164, v160, s48\n"
"v_perm_b32 v35, v188, v184, s48\n"
"v_mfma_f32_16x16x32_bf16 a[120:123], v[56:59], a[172:175], a[120:123]\n"
"v_perm_b32 v39, v188, v184, s49\n"
"v_perm_b32 v43, v189, v185, s48\n"
"v_mfma_f32_16x16x32_bf16 a[152:155], v[56:59], a[176:179], a[152:155]\n"
"v_perm_b32 v56, v167, v163, s48\n"
"v_perm_b32 v57, v175, v171, s48\n"
"v_mfma_f32_16x16x32_bf16 a[28:31], v[60:63], a[160:163], a[28:31]\n"
"v_perm_b32 v58, v183, v179, s48\n"
"v_perm_b32 v47, v189, v185, s49\n"
"v_mfma_f32_16x16x32_bf16 a[60:63], v[60:63], a[164:167], a[60:63]\n"
"buffer_load_dwordx4 v[232:235], v2, s[20:23], s59 offen\n"
"v_mfma_f32_16x16x32_bf16 a[92:95], v[60:63], a[168:171], a[92:95]\n"
"v_perm_b32 v51, v190, v186, s48\n"
"v_perm_b32 v55, v190, v186, s49\n"
"v_mfma_f32_16x16x32_bf16 a[124:127], v[60:63], a[172:175], a[124:127]\n"
"v_perm_b32 v59, v191, v187, s48\n"
"v_mfma_f32_16x16x32_bf16 a[156:159], v[60:63], a[176:179], a[156:159]\n"
"v_perm_b32 v60, v167, v163, s49\n"
"v_perm_b32 v61, v175, v171, s49\n"
"v_mfma_f32_16x16x32_bf16 a[0:3], v[32:35], a[180:183], a[0:3]\n"
"buffer_load_dwordx4 v[236:239], v3, s[20:23], s59 offen\n"
"v_mfma_f32_16x16x32_bf16 a[32:35], v[32:35], a[184:187], a[32:35]\n"
"v_perm_b32 v62, v183, v179, s49\n"
"v_perm_b32 v63, v191, v187, s49\n"
"v_mfma_f32_16x16x32_bf16 a[64:67], v[32:35], a[188:191], a[64:67]\n"
"v_mfma_f32_16x16x32_bf16 a[96:99], v[32:35], a[192:195], a[96:99]\n"
"v_mfma_f32_16x16x32_bf16 a[128:131], v[32:35], a[196:199], a[128:131]\n"
"buffer_load_dwordx4 v[240:243], v4, s[20:23], s59 offen\n"
"v_mfma_f32_16x16x32_bf16 a[4:7], v[36:39], a[180:183], a[4:7]\n"
"v_mfma_f32_16x16x32_bf16 a[36:39], v[36:39], a[184:187], a[36:39]\n"
"v_mfma_f32_16x16x32_bf16 a[68:71], v[36:39], a[188:191], a[68:71]\n"
"v_mfma_f32_16x16x32_bf16 a[100:103], v[36:39], a[192:195], a[100:103]\n"
"buffer_load_dwordx4 v[244:247], v5, s[20:23], s59 offen\n"
"v_mfma_f32_16x16x32_bf16 a[132:135], v[36:39], a[196:199], a[132:135]\n"
"s_waitcnt vmcnt(12)\n"
"v_perm_b32 v36, v196, v192, s49\n"
"s_waitcnt vmcnt(10)\n"
"v_perm_b32 v33, v204, v200, s48\n"
"v_mfma_f32_16x16x32_bf16 a[8:11], v[40:43], a[180:183], a[8:11]\n"
"v_perm_b32 v37, v204, v200, s49\n"
"v_mfma_f32_16x16x32_bf16 a[40:43], v[40:43], a[184:187], a[40:43]\n"
"v_mfma_f32_16x16x32_bf16 a[72:75], v[40:43], a[188:191], a[72:75]\n"
"buffer_load_dwordx4 v[248:251], v6, s[20:23], s59 offen\n"
"v_mfma_f32_16x16x32_bf16 a[104:107], v[40:43], a[192:195], a[104:107]\n"
"v_mfma_f32_16x16x32_bf16 a[136:139], v[40:43], a[196:199], a[136:139]\n"
"v_perm_b32 v40, v197, v193, s48\n"
"v_perm_b32 v41, v205, v201, s48\n"
"v_mfma_f32_16x16x32_bf16 a[12:15], v[44:47], a[180:183], a[12:15]\n"
"v_mfma_f32_16x16x32_bf16 a[44:47], v[44:47], a[184:187], a[44:47]\n"
"buffer_load_dwordx4 v[252:255], v7, s[20:23], s59 offen\n"
"v_mfma_f32_16x16x32_bf16 a[76:79], v[44:47], a[188:191], a[76:79]\n"
"s_waitcnt vmcnt(10)\n"
"v_perm_b32 v34, v212, v208, s48\n"
"v_perm_b32 v38, v212, v208, s49\n"
"v_mfma_f32_16x16x32_bf16 a[108:111], v[44:47], a[192:195], a[108:111]\n"
"v_perm_b32 v42, v213, v209, s48\n"
"v_mfma_f32_16x16x32_bf16 a[140:143], v[44:47], a[196:199], a[140:143]\n"
"v_perm_b32 v44, v197, v193, s49\n"
"v_perm_b32 v45, v205, v201, s49\n"
"v_mfma_f32_16x16x32_bf16 a[16:19], v[48:51], a[180:183], a[16:19]\n"
"buffer_load_dwordx4 v[64:67], v0, s[20:23], s60 offen\n"
"v_mfma_f32_16x16x32_bf16 a[48:51], v[48:51], a[184:187], a[48:51]\n"
"v_perm_b32 v46, v213, v209, s49\n"
"s_barrier\n"
"v_mfma_f32_16x16x32_bf16 a[80:83], v[48:51], a[188:191], a[80:83]\n"
"ds_read_b128 a[160:163], v8 offset:20480\n"
"v_mfma_f32_16x16x32_bf16 a[112:115], v[48:51], a[192:195], a[112:115]\n"
"ds_read_b128 a[164:167], v8 offset:24576\n"
"v_mfma_f32_16x16x32_bf16 a[144:147], v[48:51], a[196:199], a[144:147]\n"
"buffer_load_dwordx4 v[68:71], v1, s[20:23], s60 offen\n"
"v_mfma_f32_16x16x32_bf16 a[20:23], v[52:55], a[180:183], a[20:23]\n"
"s_waitcnt vmcnt(10)\n"
"v_perm_b32 v32, v196, v192, s48\n"
"v_perm_b32 v48, v198, v194, s48\n"
"v_mfma_f32_16x16x32_bf16 a[52:55], v[52:55], a[184:187], a[52:55]\n"
"v_perm_b32 v49, v206, v202, s48\n"
"v_perm_b32 v50, v214, v210, s48\n"
"v_mfma_f32_16x16x32_bf16 a[84:87], v[52:55], a[188:191], a[84:87]\n"
"v_perm_b32 v35, v220, v216, s48\n"
"v_perm_b32 v39, v220, v216, s49\n"
"v_mfma_f32_16x16x32_bf16 a[116:119], v[52:55], a[192:195], a[116:119]\n"
"v_perm_b32 v43, v221, v217, s48\n"
"v_perm_b32 v47, v221, v217, s49\n"
"v_mfma_f32_16x16x32_bf16 a[148:151], v[52:55], a[196:199], a[148:151]\n"
"buffer_load_dwordx4 v[72:75], v2, s[20:23], s60 offen\n"
"v_mfma_f32_16x16x32_bf16 a[24:27], v[56:59], a[180:183], a[24:27]\n"
"v_perm_b32 v52, v198, v194, s49\n"
"v_perm_b32 v53, v206, v202, s49\n"
"v_mfma_f32_16x16x32_bf16 a[56:59], v[56:59], a[184:187], a[56:59]\n"
"v_perm_b32 v54, v214, v210, s49\n"
"v_perm_b32 v51, v222, v218, s48\n"
"v_mfma_f32_16x16x32_bf16 a[88:91], v[56:59], a[188:191], a[88:91]\n"
"v_perm_b32 v55, v222, v218, s49\n"
"ds_read_b128 a[168:171], v8 offset:28672\n"
"v_mfma_f32_16x16x32_bf16 a[120:123], v[56:59], a[192:195], a[120:123]\n"
"buffer_load_dwordx4 v[76:79], v3, s[20:23], s60 offen\n"
"v_mfma_f32_16x16x32_bf16 a[152:155], v[56:59], a[196:199], a[152:155]\n"
"v_perm_b32 v56, v199, v195, s48\n"
"v_perm_b32 v57, v207, v203, s48\n"
"v_mfma_f32_16x16x32_bf16 a[28:31], v[60:63], a[180:183], a[28:31]\n"
"v_perm_b32 v58, v215, v211, s48\n"
"v_perm_b32 v59, v223, v219, s48\n"
"v_mfma_f32_16x16x32_bf16 a[60:63], v[60:63], a[184:187], a[60:63]\n"
"ds_read_b128 a[172:175], v8 offset:32768\n"
"v_mfma_f32_16x16x32_bf16 a[92:95], v[60:63], a[188:191], a[92:95]\n"
"buffer_load_dwordx4 v[80:83], v4, s[20:23], s60 offen\n"
"v_mfma_f32_16x16x32_bf16 a[124:127], v[60:63], a[192:195], a[124:127]\n"
"ds_read_b128 a[176:179], v8 offset:36864\n"
"v_add_u32 v8, 40960, v8\n"
"v_mfma_f32_16x16x32_bf16 a[156:159], v[60:63], a[196:199], a[156:159]\n"
"s_waitcnt lgkmcnt(3)\n"
"v_perm_b32 v60, v199, v195, s49\n"
"v_perm_b32 v61, v207, v203, s49\n"
"v_mfma_f32_16x16x32_bf16 a[0:3], v[32:35], a[160:163], a[0:3]\n"
"v_perm_b32 v62, v215, v211, s49\n"
"v_perm_b32 v63, v223, v219, s49\n"
"v_mfma_f32_16x16x32_bf16 a[32:35], v[32:35], a[164:167], a[32:35]\n"
"s_waitcnt lgkmcnt(2)\n"
"buffer_load_dwordx4 v[84:87], v5, s[20:23], s60 offen\n"
"v_mfma_f32_16x16x32_bf16 a[64:67], v[32:35], a[168:171], a[64:67]\n"
"ds_read_b128 a[180:183], v9 offset:20480\n"
"ds_read_b128 a[184:187], v9 offset:24576\n"
"ds_read_b128 a[188:191], v9 offset:28672\n"
"ds_read_b128 a[192:195], v9 offset:32768\n"
"ds_read_b128 a[196:199], v9 offset:36864\n"
"v_add_u32 v9, 40960, v9\n"
"buffer_load_dwordx4 v[88:91], v6, s[20:23], s60 offen\n"
"s_waitcnt lgkmcnt(6)\n"
"v_mfma_f32_16x16x32_bf16 a[96:99], v[32:35], a[172:175], a[96:99]\n"
"s_waitcnt lgkmcnt(5)\n"
"v_mfma_f32_16x16x32_bf16 a[128:131], v[32:35], a[176:179], a[128:131]\n"
"s_waitcnt vmcnt(11)\n"
"v_perm_b32 v33, v236, v232, s48\n"
"v_mfma_f32_16x16x32_bf16 a[4:7], v[36:39], a[160:163], a[4:7]\n"
"v_mfma_f32_16x16x32_bf16 a[36:39], v[36:39], a[164:167], a[36:39]\n"
"buffer_load_dwordx4 v[92:95], v7, s[20:23], s60 offen\n"
"v_mfma_f32_16x16x32_bf16 a[68:71], v[36:39], a[168:171], a[68:71]\n"
"s_waitcnt vmcnt(10)\n"
"v_perm_b32 v34, v244, v240, s48\n"
"v_mfma_f32_16x16x32_bf16 a[100:103], v[36:39], a[172:175], a[100:103]\n"
"v_mfma_f32_16x16x32_bf16 a[132:135], v[36:39], a[176:179], a[132:135]\n"
"v_perm_b32 v36, v228, v224, s49\n"
"v_perm_b32 v37, v236, v232, s49\n"
"v_mfma_f32_16x16x32_bf16 a[8:11], v[40:43], a[160:163], a[8:11]\n"
"buffer_load_dwordx4 v14, s[8:11], s72 offen lds\n"
"v_mfma_f32_16x16x32_bf16 a[40:43], v[40:43], a[164:167], a[40:43]\n"
"s_add_u32 m0, s73, 45056\n"
"s_add_u32 s72, s72, 16384\n"
"v_perm_b32 v38, v244, v240, s49\n"
"v_mfma_f32_16x16x32_bf16 a[72:75], v[40:43], a[168:171], a[72:75]\n"
"v_mfma_f32_16x16x32_bf16 a[104:107], v[40:43], a[172:175], a[104:107]\n"
"v_mfma_f32_16x16x32_bf16 a[136:139], v[40:43], a[176:179], a[136:139]\n"
"buffer_load_dwordx4 v14, s[8:11], s72 offen lds\n"
"v_mfma_f32_16x16x32_bf16 a[12:15], v[44:47], a[160:163], a[12:15]\n"
"s_add_u32 m0, s73, 49152\n"
"s_add_u32 s72, s72, 16384\n"
"s_waitcnt vmcnt(10) lgkmcnt(0)\n"
"v_perm_b32 v32, v228, v224, s48\n"
"v_mfma_f32_16x16x32_bf16 a[44:47], v[44:47], a[164:167], a[44:47]\n"
"v_perm_b32 v40, v229, v225, s48\n"
"v_perm_b32 v41, v237, v233, s48\n"
"v_mfma_f32_16x16x32_bf16 a[76:79], v[44:47], a[168:171], a[76:79]\n"
"v_perm_b32 v42, v245, v241, s48\n"
"v_perm_b32 v35, v252, v248, s48\n"
"v_mfma_f32_16x16x32_bf16 a[108:111], v[44:47], a[172:175], a[108:111]\n"
"buffer_load_dwordx4 v14, s[8:11], s72 offen lds\n"
"v_mfma_f32_16x16x32_bf16 a[140:143], v[44:47], a[176:179], a[140:143]\n"
"s_add_u32 m0, s73, 53248\n"
"s_add_u32 s72, s72, 16384\n"
"v_perm_b32 v44, v229, v225, s49\n"
"v_mfma_f32_16x16x32_bf16 a[16:19], v[48:51], a[160:163], a[16:19]\n"
"v_perm_b32 v45, v237, v233, s49\n"
"v_perm_b32 v46, v245, v241, s49\n"
"v_mfma_f32_16x16x32_bf16 a[48:51], v[48:51], a[164:167], a[48:51]\n"
"v_perm_b32 v39, v252, v248, s49\n"
"v_perm_b32 v43, v253, v249, s48\n"
"v_mfma_f32_16x16x32_bf16 a[80:83], v[48:51], a[168:171], a[80:83]\n"
"buffer_load_dwordx4 v14, s[8:11], s72 offen lds\n"
"v_mfma_f32_16x16x32_bf16 a[112:115], v[48:51], a[172:175], a[112:115]\n"
"s_add_u32 m0, s73, 57344\n"
"s_add_u32 s72, s72, 16384\n"
"s_lshl_b32 s73, %2, 10\n"
"v_mfma_f32_16x16x32_bf16 a[144:147], v[48:51], a[176:179], a[144:147]\n"
"v_perm_b32 v48, v230, v226, s48\n"
"v_perm_b32 v49, v238, v234, s48\n"
"v_mfma_f32_16x16x32_bf16 a[20:23], v[52:55], a[160:163], a[20:23]\n"
"v_perm_b32 v50, v246, v242, s48\n"
"v_perm_b32 v47, v253, v249, s49\n"
"v_mfma_f32_16x16x32_bf16 a[52:55], v[52:55], a[164:167], a[52:55]\n"
"v_perm_b32 v51, v254, v250, s48\n"
"v_mfma_f32_16x16x32_bf16 a[84:87], v[52:55], a[168:171], a[84:87]\n"
"buffer_load_dwordx4 v14, s[8:11], s72 offen lds\n"
"v_mfma_f32_16x16x32_bf16 a[116:119], v[52:55], a[172:175], a[116:119]\n"
"s_lshr_b32 s72, s66, 12\n"
"s_add_u32 m0, s73, 61440\n"
"s_and_b32 s72, s72, 768\n"
"v_mfma_f32_16x16x32_bf16 a[148:151], v[52:55], a[176:179], a[148:151]\n"
"v_perm_b32 v52, v230, v226, s49\n"
"v_perm_b32 v53, v238, v234, s49\n"
"v_mfma_f32_16x16x32_bf16 a[24:27], v[56:59], a[160:163], a[24:27]\n"
"v_perm_b32 v54, v246, v242, s49\n"
"v_perm_b32 v55, v254, v250, s49\n"
"v_mfma_f32_16x16x32_bf16 a[56:59], v[56:59], a[164:167], a[56:59]\n"
"buffer_load_dwordx4 v14, s[8:11], s72 offen lds\n"
"v_mfma_f32_16x16x32_bf16 a[88:91], v[56:59], a[168:171], a[88:91]\n"
"s_add_u32 m0, s73, 65536\n"
"s_add_u32 s72, s72, 16384\n"
"v_mfma_f32_16x16x32_bf16 a[120:123], v[56:59], a[172:175], a[120:123]\n"
"v_mfma_f32_16x16x32_bf16 a[152:155], v[56:59], a[176:179], a[152:155]\n"
"v_perm_b32 v56, v231, v227, s48\n"
"v_perm_b32 v57, v239, v235, s48\n"
"v_mfma_f32_16x16x32_bf16 a[28:31], v[60:63], a[160:163], a[28:31]\n"
"buffer_load_dwordx4 v14, s[8:11], s72 offen lds\n"
"v_mfma_f32_16x16x32_bf16 a[60:63], v[60:63], a[164:167], a[60:63]\n"
"s_add_u32 m0, s73, 69632\n"
"s_add_u32 s72, s72, 16384\n"
"v_perm_b32 v58, v247, v243, s48\n"
"v_mfma_f32_16x16x32_bf16 a[92:95], v[60:63], a[168:171], a[92:95]\n"
"v_perm_b32 v59, v255, v251, s48\n"
"ds_read_b128 a[160:163], v10 offset:20480\n"
"v_mfma_f32_16x16x32_bf16 a[124:127], v[60:63], a[172:175], a[124:127]\n"
"ds_read_b128 a[164:167], v10 offset:24576\n"
"v_mfma_f32_16x16x32_bf16 a[156:159], v[60:63], a[176:179], a[156:159]\n"
"v_perm_b32 v60, v231, v227, s49\n"
"v_perm_b32 v61, v239, v235, s49\n"
"v_mfma_f32_16x16x32_bf16 a[0:3], v[32:35], a[180:183], a[0:3]\n"
"v_perm_b32 v62, v247, v243, s49\n"
"v_perm_b32 v63, v255, v251, s49\n"
"v_mfma_f32_16x16x32_bf16 a[32:35], v[32:35], a[184:187], a[32:35]\n"
"ds_read_b128 a[168:171], v10 offset:28672\n"
"v_mfma_f32_16x16x32_bf16 a[64:67], v[32:35], a[188:191], a[64:67]\n"
"ds_read_b128 a[172:175], v10 offset:32768\n"
"v_mfma_f32_16x16x32_bf16 a[96:99], v[32:35], a[192:195], a[96:99]\n"
"ds_read_b128 a[176:179], v10 offset:36864\n"
"v_add_u32 v10, 40960, v10\n"
"v_mfma_f32_16x16x32_bf16 a[128:131], v[32:35], a[196:199], a[128:131]\n"
"s_waitcnt vmcnt(11)\n"
"v_perm_b32 v33, v76, v72, s48\n"
"s_waitcnt vmcnt(9)\n"
"v_perm_b32 v34, v84, v80, s48\n"
"v_mfma_f32_16x16x32_bf16 a[4:7], v[36:39], a[180:183], a[4:7]\n"
"v_mfma_f32_16x16x32_bf16 a[36:39], v[36:39], a[184:187], a[36:39]\n"
"v_mfma_f32_16x16x32_bf16 a[68:71], v[36:39], a[188:191], a[68:71]\n"
"s_waitcnt vmcnt(7)\n"
"v_perm_b32 v32, v68, v64, s48\n"
"v_perm_b32 v35, v92, v88, s48\n"
"v_mfma_f32_16x16x32_bf16 a[100:103], v[36:39], a[192:195], a[100:103]\n"
"v_mfma_f32_16x16x32_bf16 a[132:135], v[36:39], a[196:199], a[132:135]\n"
"v_perm_b32 v36, v68, v64, s49\n"
"v_perm_b32 v37, v76, v72, s49\n"
"v_mfma_f32_16x16x32_bf16 a[8:11], v[40:43], a[180:183], a[8:11]\n"
"v_perm_b32 v38, v84, v80, s49\n"
"v_perm_b32 v39, v92, v88, s49\n"
"v_mfma_f32_16x16x32_bf16 a[40:43], v[40:43], a[184:187], a[40:43]\n"
"v_mfma_f32_16x16x32_bf16 a[72:75], v[40:43], a[188:191], a[72:75]\n"
"v_mfma_f32_16x16x32_bf16 a[104:107], v[40:43], a[192:195], a[104:107]\n"
"v_mfma_f32_16x16x32_bf16 a[136:139], v[40:43], a[196:199], a[136:139]\n"
"v_perm_b32 v40, v69, v65, s48\n"
"v_perm_b32 v41, v77, v73, s48\n"
"v_mfma_f32_16x16x32_bf16 a[12:15], v[44:47], a[180:183], a[12:15]\n"
"v_perm_b32 v42, v85, v81, s48\n"
"v_perm_b32 v43, v93, v89, s48\n"
"v_mfma_f32_16x16x32_bf16 a[44:47], v[44:47], a[184:187], a[44:47]\n"
"v_mfma_f32_16x16x32_bf16 a[76:79], v[44:47], a[188:191], a[76:79]\n"
"v_mfma_f32_16x16x32_bf16 a[108:111], v[44:47], a[192:195], a[108:111]\n"
"v_mfma_f32_16x16x32_bf16 a[140:143], v[44:47], a[196:199], a[140:143]\n"
"v_perm_b32 v44, v69, v65, s49\n"
"v_perm_b32 v45, v77, v73, s49\n"
"v_mfma_f32_16x16x32_bf16 a[16:19], v[48:51], a[180:183], a[16:19]\n"
"v_perm_b32 v46, v85, v81, s49\n"
"v_perm_b32 v47, v93, v89, s49\n"
"v_mfma_f32_16x16x32_bf16 a[48:51], v[48:51], a[184:187], a[48:51]\n"
"v_mfma_f32_16x16x32_bf16 a[80:83], v[48:51], a[188:191], a[80:83]\n"
"v_mfma_f32_16x16x32_bf16 a[112:115], v[48:51], a[192:195], a[112:115]\n"
"v_mfma_f32_16x16x32_bf16 a[144:147], v[48:51], a[196:199], a[144:147]\n"
"v_perm_b32 v48, v70, v66, s48\n"
"v_perm_b32 v49, v78, v74, s48\n"
"v_mfma_f32_16x16x32_bf16 a[20:23], v[52:55], a[180:183], a[20:23]\n"
"v_perm_b32 v50, v86, v82, s48\n"
"v_perm_b32 v51, v94, v90, s48\n"
"v_mfma_f32_16x16x32_bf16 a[52:55], v[52:55], a[184:187], a[52:55]\n"
"v_mfma_f32_16x16x32_bf16 a[84:87], v[52:55], a[188:191], a[84:87]\n"
"v_mfma_f32_16x16x32_bf16 a[116:119], v[52:55], a[192:195], a[116:119]\n"
"v_mfma_f32_16x16x32_bf16 a[148:151], v[52:55], a[196:199], a[148:151]\n"
"v_perm_b32 v52, v70, v66, s49\n"
"v_perm_b32 v53, v78, v74, s49\n"
"v_mfma_f32_16x16x32_bf16 a[24:27], v[56:59], a[180:183], a[24:27]\n"
"v_perm_b32 v54, v86, v82, s49\n"
"v_perm_b32 v55, v94, v90, s49\n"
"v_mfma_f32_16x16x32_bf16 a[56:59], v[56:59], a[184:187], a[56:59]\n"
"v_mfma_f32_16x16x32_bf16 a[88:91], v[56:59], a[188:191], a[88:91]\n"
"v_mfma_f32_16x16x32_bf16 a[120:123], v[56:59], a[192:195], a[120:123]\n"
"v_mfma_f32_16x16x32_bf16 a[152:155], v[56:59], a[196:199], a[152:155]\n"
"v_perm_b32 v56, v71, v67, s48\n"
"v_perm_b32 v57, v79, v75, s48\n"
"v_mfma_f32_16x16x32_bf16 a[28:31], v[60:63], a[180:183], a[28:31]\n"
"v_perm_b32 v58, v87, v83, s48\n"
"v_perm_b32 v59, v95, v91, s48\n"
"v_mfma_f32_16x16x32_bf16 a[60:63], v[60:63], a[184:187], a[60:63]\n"
"ds_read_b128 a[180:183], v11 offset:20480\n"
"v_mfma_f32_16x16x32_bf16 a[92:95], v[60:63], a[188:191], a[92:95]\n"
"ds_read_b128 a[184:187], v11 offset:24576\n"
"v_mfma_f32_16x16x32_bf16 a[124:127], v[60:63], a[192:195], a[124:127]\n"
"ds_read_b128 a[188:191], v11 offset:28672\n"
"v_mfma_f32_16x16x32_bf16 a[156:159], v[60:63], a[196:199], a[156:159]\n"
"s_waitcnt lgkmcnt(3)\n"
"v_perm_b32 v60, v71, v67, s49\n"
"v_perm_b32 v61, v79, v75, s49\n"
"v_mfma_f32_16x16x32_bf16 a[0:3], v[32:35], a[160:163], a[0:3]\n"
"v_perm_b32 v62, v87, v83, s49\n"
"v_perm_b32 v63, v95, v91, s49\n"
"v_mfma_f32_16x16x32_bf16 a[32:35], v[32:35], a[164:167], a[32:35]\n"
"ds_read_b128 a[192:195], v11 offset:32768\n"
"v_mfma_f32_16x16x32_bf16 a[64:67], v[32:35], a[168:171], a[64:67]\n"
"ds_read_b128 a[196:199], v11 offset:36864\n"
"v_add_u32 v11, 40960, v11\n"
"v_mfma_f32_16x16x32_bf16 a[96:99], v[32:35], a[172:175], a[96:99]\n"
"v_mfma_f32_16x16x32_bf16 a[128:131], v[32:35], a[176:179], a[128:131]\n"
"v_mfma_f32_16x16x32_bf16 a[4:7], v[36:39], a[160:163], a[4:7]\n"
"buffer_load_dwordx4 v14, s[8:11], s72 offen lds\n"
"v_mfma_f32_16x16x32_bf16 a[36:39], v[36:39], a[164:167], a[36:39]\n"
"s_add_u32 m0, s73, 73728\n"
"s_add_u32 s72, s72, 16384\n"
"v_mfma_f32_16x16x32_bf16 a[68:71], v[36:39], a[168:171], a[68:71]\n"
"v_mfma_f32_16x16x32_bf16 a[100:103], v[36:39], a[172:175], a[100:103]\n"
"v_mfma_f32_16x16x32_bf16 a[132:135], v[36:39], a[176:179], a[132:135]\n"
"v_mfma_f32_16x16x32_bf16 a[8:11], v[40:43], a[160:163], a[8:11]\n"
"buffer_load_dwordx4 v14, s[8:11], s72 offen lds\n"
"v_mfma_f32_16x16x32_bf16 a[40:43], v[40:43], a[164:167], a[40:43]\n"
"s_add_u32 m0, s73, 77824\n"
"s_add_u32 s72, s72, 16384\n"
"v_mfma_f32_16x16x32_bf16 a[72:75], v[40:43], a[168:171], a[72:75]\n"
"v_mfma_f32_16x16x32_bf16 a[104:107], v[40:43], a[172:175], a[104:107]\n"
"v_mfma_f32_16x16x32_bf16 a[136:139], v[40:43], a[176:179], a[136:139]\n"
"buffer_load_dwordx4 v14, s[8:11], s72 offen lds\n"
"v_mfma_f32_16x16x32_bf16 a[12:15], v[44:47], a[160:163], a[12:15]\n"
"s_add_u32 m0, s75, 0\n"
"v_mov_b32 v14, -1\n"
"v_mfma_f32_16x16x32_bf16 a[44:47], v[44:47], a[164:167], a[44:47]\n"
"v_mfma_f32_16x16x32_bf16 a[76:79], v[44:47], a[168:171], a[76:79]\n"
"v_mfma_f32_16x16x32_bf16 a[108:111], v[44:47], a[172:175], a[108:111]\n"
"buffer_load_dwordx4 v12, s[12:15], 0 offen nt lds\n"
"v_mfma_f32_16x16x32_bf16 a[140:143], v[44:47], a[176:179], a[140:143]\n"
"v_mfma_f32_16x16x32_bf16 a[16:19], v[48:51], a[160:163], a[16:19]\n"
"v_mfma_f32_16x16x32_bf16 a[48:51], v[48:51], a[164:167], a[48:51]\n"
"v_mfma_f32_16x16x32_bf16 a[80:83], v[48:51], a[168:171], a[80:83]\n"
"buffer_load_dwordx4 v12, s[12:15], 0 offen offset:1024 nt lds\n"
"v_mfma_f32_16x16x32_bf16 a[112:115], v[48:51], a[172:175], a[112:115]\n"
"v_mfma_f32_16x16x32_bf16 a[144:147], v[48:51], a[176:179], a[144:147]\n"
"v_mfma_f32_16x16x32_bf16 a[20:23], v[52:55], a[160:163], a[20:23]\n"
"v_mfma_f32_16x16x32_bf16 a[52:55], v[52:55], a[164:167], a[52:55]\n"
"v_mfma_f32_16x16x32_bf16 a[84:87], v[52:55], a[168:171], a[84:87]\n"
"buffer_load_dwordx4 v13, s[12:15], 0 offen offset:2048 nt lds\n"
"v_mfma_f32_16x16x32_bf16 a[116:119], v[52:55], a[172:175], a[116:119]\n"
"v_mfma_f32_16x16x32_bf16 a[148:151], v[52:55], a[176:179], a[148:151]\n"
"v_mfma_f32_16x16x32_bf16 a[24:27], v[56:59], a[160:163], a[24:27]\n"
"v_mfma_f32_16x16x32_bf16 a[56:59], v[56:59], a[164:167], a[56:59]\n"
"buffer_load_dwordx4 v13, s[12:15], 0 offen offset:3072 nt lds\n"
"v_mfma_f32_16x16x32_bf16 a[88:91], v[56:59], a[168:171], a[88:91]\n"
"s_add_u32 m0, s75, 16384\n"
"v_mfma_f32_16x16x32_bf16 a[120:123], v[56:59], a[172:175], a[120:123]\n"
"v_mfma_f32_16x16x32_bf16 a[152:155], v[56:59], a[176:179], a[152:155]\n"
"v_mfma_f32_16x16x32_bf16 a[28:31], v[60:63], a[160:163], a[28:31]\n"
"v_mfma_f32_16x16x32_bf16 a[60:63], v[60:63], a[164:167], a[60:63]\n"
"v_mfma_f32_16x16x32_bf16 a[92:95], v[60:63], a[168:171], a[92:95]\n"
"v_mfma_f32_16x16x32_bf16 a[124:127], v[60:63], a[172:175], a[124:127]\n"
"v_mfma_f32_16x16x32_bf16 a[156:159], v[60:63], a[176:179], a[156:159]\n"
"buffer_load_dwordx4 v12, s[12:15], s50 offen nt lds\n"
"buffer_load_dwordx4 v12, s[12:15], s50 offen offset:1024 nt lds\n"
"buffer_load_dwordx4 v13, s[12:15], s50 offen offset:2048 nt lds\n"
"buffer_load_dwordx4 v13, s[12:15], s50 offen offset:3072 nt lds\n"
"s_add_u32 m0, s75, 32768\n"
"s_nop 0\n"
"buffer_load_dwordx4 v12, s[12:15], s51 offen nt lds\n"
"buffer_load_dwordx4 v12, s[12:15], s51 offen offset:1024 nt lds\n"
"buffer_load_dwordx4 v13, s[12:15], s51 offen offset:2048 nt lds\n"
"s_waitcnt vmcnt(7) lgkmcnt(0)\n"
"ds_read_b128 a[200:203], v23 offset:0\n"
"ds_read_b128 a[204:207], v24 offset:0\n"
"ds_read_b128 a[208:211], v25 offset:0\n"
"ds_read_b128 a[212:215], v26 offset:0\n"
"buffer_load_dwordx4 v13, s[12:15], s51 offen offset:3072 nt lds\n"
"s_add_u32 m0, s75, 49152\n"
"s_nop 0\n"
"buffer_load_dwordx4 v12, s[12:15], s52 offen nt lds\n"
"buffer_load_dwordx4 v12, s[12:15], s52 offen offset:1024 nt lds\n"
"buffer_load_dwordx4 v13, s[12:15], s52 offen offset:2048 nt lds\n"
"buffer_load_dwordx4 v13, s[12:15], s52 offen offset:3072 nt lds\n"
"s_add_u32 m0, s75, 65536\n"
"s_nop 0\n"
"buffer_load_dwordx4 v12, s[12:15], s53 offen nt lds\n"
"s_waitcnt vmcnt(9) lgkmcnt(0)\n"
"ds_read_b128 a[216:219], v23 offset:16384\n"
"ds_read_b128 a[220:223], v24 offset:16384\n"
"buffer_load_dwordx4 v12, s[12:15], s53 offen offset:1024 nt lds\n"
"v_lshl_add_u32 v12, v17, 8, v19\n"
"ds_read_b128 a[224:227], v25 offset:16384\n"
"ds_read_b128 a[228:231], v26 offset:16384\n"
"buffer_load_dwordx4 v13, s[12:15], s53 offen offset:2048 nt lds\n"
"buffer_load_dwordx4 v13, s[12:15], s53 offen offset:3072 nt lds\n"
"s_add_u32 m0, s75, 0\n"
"v_xor_b32 v13, 128, v12\n"
"buffer_load_dwordx4 v[96:99], v0, s[20:23], s61 offen\n"
"s_waitcnt vmcnt(9) lgkmcnt(0)\n"
"ds_read_b128 a[232:235], v23 offset:32768\n"
"ds_read_b128 a[236:239], v24 offset:32768\n"
"ds_read_b128 a[240:243], v25 offset:32768\n"
"ds_read_b128 a[244:247], v26 offset:32768\n"
"buffer_load_dwordx4 v[100:103], v1, s[20:23], s61 offen\n"
"buffer_load_dwordx4 v[104:107], v2, s[20:23], s61 offen\n"
"buffer_load_dwordx4 v[108:111], v3, s[20:23], s61 offen\n"
"buffer_load_dwordx4 v[112:115], v4, s[20:23], s61 offen\n"
"s_waitcnt vmcnt(9) lgkmcnt(0)\n"
"ds_read_b128 a[248:251], v23 offset:49152\n"
"ds_read_b128 a[252:255], v24 offset:49152\n"
"ds_read_b128 v[224:227], v25 offset:49152\n"
"ds_read_b128 v[228:231], v26 offset:49152\n"
"v_add_u32 v23, 16384, v23\n"
"v_add_u32 v24, 16384, v24\n"
"v_add_u32 v25, 16384, v25\n"
"v_add_u32 v26, 16384, v26\n"
"buffer_load_dwordx4 v[116:119], v5, s[20:23], s61 offen\n"
"buffer_load_dwordx4 v[120:123], v6, s[20:23], s61 offen\n"
"buffer_load_dwordx4 v[124:127], v7, s[20:23], s61 offen\n"
"s_lshl_b32 s61, s34, 7\n"
"s_add_u32 s54, s61, 128\n"
"s_and_b32 s54, s54, 896\n"
"s_add_u32 s72, s54, 32768\n"
"s_add_u32 s55, s61, 256\n"
"s_and_b32 s55, s55, 896\n"
"s_add_u32 s56, s61, 384\n"
"s_and_b32 s56, s56, 896\n"
"s_add_u32 s57, s61, 512\n"
"s_and_b32 s57, s57, 896\n"
"s_add_u32 s58, s61, 640\n"
"s_lshl_b32 s71, s54, 1\n"
"s_and_b32 s58, s58, 896\n"
"s_add_u32 s59, s61, 768\n"
"s_and_b32 s59, s59, 896\n"
"buffer_load_dwordx4 v[128:131], v0, s[20:23], s62 offen\n"
"s_add_u32 s60, s61, 896\n"
"s_and_b32 s60, s60, 896\n"
"buffer_load_dwordx4 v[132:135], v1, s[20:23], s62 offen\n"
"s_waitcnt vmcnt(10) lgkmcnt(0)\n"
"ds_read_b128 v[232:235], v23 offset:49152\n"
"ds_read_b128 v[236:239], v24 offset:49152\n"
"ds_read_b128 v[240:243], v25 offset:49152\n"
"ds_read_b128 v[244:247], v26 offset:49152\n"
"buffer_load_dwordx4 v[136:139], v2, s[20:23], s62 offen\n"
"buffer_load_dwordx4 v[140:143], v3, s[20:23], s62 offen\n"
"s_waitcnt vmcnt(10)\n"
"v_perm_b32 v36, v100, v96, s49\n"
"v_perm_b32 v40, v101, v97, s48\n"
"v_perm_b32 v44, v101, v97, s49\n"
"v_perm_b32 v48, v102, v98, s48\n"
"v_perm_b32 v52, v102, v98, s49\n"
"v_perm_b32 v56, v103, v99, s48\n"
"v_perm_b32 v60, v103, v99, s49\n"
"buffer_load_dwordx4 v[144:147], v4, s[20:23], s62 offen\n"
"buffer_load_dwordx4 v[148:151], v5, s[20:23], s62 offen\n"
"s_waitcnt vmcnt(10) lgkmcnt(0)\n"
"v_perm_b32 v33, v108, v104, s48\n"
"v_perm_b32 v37, v108, v104, s49\n"
"v_perm_b32 v41, v109, v105, s48\n"
"v_perm_b32 v45, v109, v105, s49\n"
"v_perm_b32 v49, v110, v106, s48\n"
"v_perm_b32 v53, v110, v106, s49\n"
"v_perm_b32 v57, v111, v107, s48\n"
"v_perm_b32 v61, v111, v107, s49\n"
"buffer_load_dwordx4 v[152:155], v6, s[20:23], s62 offen\n"
"buffer_load_dwordx4 v[156:159], v7, s[20:23], s62 offen\n"
"s_waitcnt vmcnt(10)\n"
"v_perm_b32 v34, v116, v112, s48\n"
"v_perm_b32 v38, v116, v112, s49\n"
"v_perm_b32 v42, v117, v113, s48\n"
"v_perm_b32 v46, v117, v113, s49\n"
"v_perm_b32 v50, v118, v114, s48\n"
"v_perm_b32 v54, v118, v114, s49\n"
"v_perm_b32 v58, v119, v115, s48\n"
"v_perm_b32 v62, v119, v115, s49\n"
"s_mul_i32 s62, s54, 640\n"
"buffer_load_dwordx4 v[160:163], v0, s[20:23], s63 offen\n"
"buffer_load_dwordx4 v[164:167], v1, s[20:23], s63 offen\n"
"s_waitcnt vmcnt(10)\n"
"v_perm_b32 v32, v100, v96, s48\n"
"v_perm_b32 v35, v124, v120, s48\n"
"v_perm_b32 v39, v124, v120, s49\n"
"v_perm_b32 v43, v125, v121, s48\n"
"v_mfma_f32_16x16x32_bf16 a[0:3], v[32:35], a[180:183], a[0:3]\n"
"v_perm_b32 v47, v125, v121, s49\n"
"v_perm_b32 v51, v126, v122, s48\n"
"v_mfma_f32_16x16x32_bf16 a[32:35], v[32:35], a[184:187], a[32:35]\n"
"v_perm_b32 v55, v126, v122, s49\n"
"v_perm_b32 v59, v127, v123, s48\n"
"v_mfma_f32_16x16x32_bf16 a[64:67], v[32:35], a[188:191], a[64:67]\n"
"v_perm_b32 v63, v127, v123, s49\n"
"v_mfma_f32_16x16x32_bf16 a[96:99], v[32:35], a[192:195], a[96:99]\n"
"buffer_load_dwordx4 v[168:171], v2, s[20:23], s63 offen\n"
"v_mfma_f32_16x16x32_bf16 a[128:131], v[32:35], a[196:199], a[128:131]\n"
"v_mfma_f32_16x16x32_bf16 a[4:7], v[36:39], a[180:183], a[4:7]\n"
"v_mfma_f32_16x16x32_bf16 a[36:39], v[36:39], a[184:187], a[36:39]\n"
"v_mfma_f32_16x16x32_bf16 a[68:71], v[36:39], a[188:191], a[68:71]\n"
"buffer_load_dwordx4 v[172:175], v3, s[20:23], s63 offen\n"
"v_mfma_f32_16x16x32_bf16 a[100:103], v[36:39], a[192:195], a[100:103]\n"
"v_mfma_f32_16x16x32_bf16 a[132:135], v[36:39], a[196:199], a[132:135]\n"
"s_waitcnt vmcnt(10)\n"
"v_perm_b32 v36, v132, v128, s49\n"
"v_mfma_f32_16x16x32_bf16 a[8:11], v[40:43], a[180:183], a[8:11]\n"
"v_mfma_f32_16x16x32_bf16 a[40:43], v[40:43], a[184:187], a[40:43]\n"
"buffer_load_dwordx4 v[176:179], v4, s[20:23], s63 offen\n"
"v_mfma_f32_16x16x32_bf16 a[72:75], v[40:43], a[188:191], a[72:75]\n"
"v_mfma_f32_16x16x32_bf16 a[104:107], v[40:43], a[192:195], a[104:107]\n"
"v_mfma_f32_16x16x32_bf16 a[136:139], v[40:43], a[196:199], a[136:139]\n"
"v_perm_b32 v40, v133, v129, s48\n"
"v_mfma_f32_16x16x32_bf16 a[12:15], v[44:47], a[180:183], a[12:15]\n"
"buffer_load_dwordx4 v[180:183], v5, s[20:23], s63 offen\n"
"v_mfma_f32_16x16x32_bf16 a[44:47], v[44:47], a[184:187], a[44:47]\n"
"s_waitcnt vmcnt(10)\n"
"v_perm_b32 v33, v140, v136, s48\n"
"v_perm_b32 v37, v140, v136, s49\n"
"v_mfma_f32_16x16x32_bf16 a[76:79], v[44:47], a[188:191], a[76:79]\n"
"v_perm_b32 v41, v141, v137, s48\n"
"v_mfma_f32_16x16x32_bf16 a[108:111], v[44:47], a[192:195], a[108:111]\n"
"v_mfma_f32_16x16x32_bf16 a[140:143], v[44:47], a[196:199], a[140:143]\n"
"buffer_load_dwordx4 v[184:187], v6, s[20:23], s63 offen\n"
"v_mfma_f32_16x16x32_bf16 a[16:19], v[48:51], a[180:183], a[16:19]\n"
"v_perm_b32 v44, v133, v129, s49\n"
"v_perm_b32 v45, v141, v137, s49\n"
"v_mfma_f32_16x16x32_bf16 a[48:51], v[48:51], a[184:187], a[48:51]\n"
"s_barrier\n"
"ds_read_b128 a[160:163], v8 offset:0\n"
"v_mfma_f32_16x16x32_bf16 a[80:83], v[48:51], a[188:191], a[80:83]\n"
"ds_read_b128 a[164:167], v8 offset:4096\n"
"v_mfma_f32_16x16x32_bf16 a[112:115], v[48:51], a[192:195], a[112:115]\n"
"buffer_load_dwordx4 v[188:191], v7, s[20:23], s63 offen\n"
"v_mfma_f32_16x16x32_bf16 a[144:147], v[48:51], a[196:199], a[144:147]\n"
"s_mul_i32 s63, s55, 640\n"
"v_perm_b32 v48, v134, v130, s48\n"
"v_perm_b32 v49, v142, v138, s48\n"
"v_mfma_f32_16x16x32_bf16 a[20:23], v[52:55], a[180:183], a[20:23]\n"
"s_add_u32 s63, s63, 2560\n"
"s_waitcnt vmcnt(10)\n"
"v_perm_b32 v34, v148, v144, s48\n"
"v_perm_b32 v38, v148, v144, s49\n"
"v_mfma_f32_16x16x32_bf16 a[52:55], v[52:55], a[184:187], a[52:55]\n"
"v_perm_b32 v42, v149, v145, s48\n"
"v_perm_b32 v46, v149, v145, s49\n"
"v_mfma_f32_16x16x32_bf16 a[84:87], v[52:55], a[188:191], a[84:87]\n"
"buffer_load_dwordx4 v[192:195], v0, s[20:23], s64 offen\n"
"v_mfma_f32_16x16x32_bf16 a[116:119], v[52:55], a[192:195], a[116:119]\n"
"v_perm_b32 v50, v150, v146, s48\n"
"ds_read_b128 a[168:171], v8 offset:8192\n"
"v_mfma_f32_16x16x32_bf16 a[148:151], v[52:55], a[196:199], a[148:151]\n"
"v_perm_b32 v52, v134, v130, s49\n"
"v_perm_b32 v53, v142, v138, s49\n"
"v_mfma_f32_16x16x32_bf16 a[24:27], v[56:59], a[180:183], a[24:27]\n"
"v_perm_b32 v54, v150, v146, s49\n"
"ds_read_b128 a[172:175], v8 offset:12288\n"
"v_mfma_f32_16x16x32_bf16 a[56:59], v[56:59], a[184:187], a[56:59]\n"
"buffer_load_dwordx4 v[196:199], v1, s[20:23], s64 offen\n"
"v_mfma_f32_16x16x32_bf16 a[88:91], v[56:59], a[188:191], a[88:91]\n"
"s_waitcnt vmcnt(10)\n"
"v_perm_b32 v32, v132, v128, s48\n"
"v_perm_b32 v35, v156, v152, s48\n"
"v_mfma_f32_16x16x32_bf16 a[120:123], v[56:59], a[192:195], a[120:123]\n"
"v_perm_b32 v39, v156, v152, s49\n"
"v_perm_b32 v43, v157, v153, s48\n"
"v_mfma_f32_16x16x32_bf16 a[152:155], v[56:59], a[196:199], a[152:155]\n"
"v_perm_b32 v56, v135, v131, s48\n"
"v_perm_b32 v57, v143, v139, s48\n"
"v_mfma_f32_16x16x32_bf16 a[28:31], v[60:63], a[180:183], a[28:31]\n"
"v_perm_b32 v58, v151, v147, s48\n"
"v_perm_b32 v47, v157, v153, s49\n"
"v_mfma_f32_16x16x32_bf16 a[60:63], v[60:63], a[184:187], a[60:63]\n"
"buffer_load_dwordx4 v[200:203], v2, s[20:23], s64 offen\n"
"v_mfma_f32_16x16x32_bf16 a[92:95], v[60:63], a[188:191], a[92:95]\n"
"v_perm_b32 v51, v158, v154, s48\n"
"v_perm_b32 v55, v158, v154, s49\n"
"v_mfma_f32_16x16x32_bf16 a[124:127], v[60:63], a[192:195], a[124:127]\n"
"v_perm_b32 v59, v159, v155, s48\n"
"ds_read_b128 a[176:179], v8 offset:16384\n"
"v_mfma_f32_16x16x32_bf16 a[156:159], v[60:63], a[196:199], a[156:159]\n"
"s_waitcnt lgkmcnt(3)\n"
"v_perm_b32 v60, v135, v131, s49\n"
"v_perm_b32 v61, v143, v139, s49\n"
"v_mfma_f32_16x16x32_bf16 a[0:3], v[32:35], a[160:163], a[0:3]\n"
"buffer_load_dwordx4 v[204:207], v3, s[20:23], s64 offen\n"
"v_mfma_f32_16x16x32_bf16 a[32:35], v[32:35], a[164:167], a[32:35]\n"
"s_waitcnt lgkmcnt(2)\n"
"v_perm_b32 v62, v151, v147, s49\n"
"v_perm_b32 v63, v159, v155, s49\n"
"v_mfma_f32_16x16x32_bf16 a[64:67], v[32:35], a[168:171], a[64:67]\n"
"s_waitcnt lgkmcnt(1)\n"
"ds_read_b128 a[180:183], v9 offset:0\n"
"v_mfma_f32_16x16x32_bf16 a[96:99], v[32:35], a[172:175], a[96:99]\n"
"ds_read_b128 a[184:187], v9 offset:4096\n"
"ds_read_b128 a[188:191], v9 offset:8192\n"
"buffer_load_dwordx4 v[208:211], v4, s[20:23], s64 offen\n"
"ds_read_b128 a[192:195], v9 offset:12288\n"
"ds_read_b128 a[196:199], v9 offset:16384\n"
"s_waitcnt lgkmcnt(5)\n"
"v_mfma_f32_16x16x32_bf16 a[128:131], v[32:35], a[176:179], a[128:131]\n"
"buffer_load_dwordx4 v[212:215], v5, s[20:23], s64 offen\n"
"v_mfma_f32_16x16x32_bf16 a[4:7], v[36:39], a[160:163], a[4:7]\n"
"s_waitcnt vmcnt(10)\n"
"v_perm_b32 v33, v172, v168, s48\n"
"v_mfma_f32_16x16x32_bf16 a[36:39], v[36:39], a[164:167], a[36:39]\n"
"v_mfma_f32_16x16x32_bf16 a[68:71], v[36:39], a[168:171], a[68:71]\n"
"v_mfma_f32_16x16x32_bf16 a[100:103], v[36:39], a[172:175], a[100:103]\n"
"v_mfma_f32_16x16x32_bf16 a[132:135], v[36:39], a[176:179], a[132:135]\n"
"buffer_load_dwordx4 v[216:219], v6, s[20:23], s64 offen\n"
"v_mfma_f32_16x16x32_bf16 a[8:11], v[40:43], a[160:163], a[8:11]\n"
"v_perm_b32 v36, v164, v160, s49\n"
"v_perm_b32 v37, v172, v168, s49\n"
"v_mfma_f32_16x16x32_bf16 a[40:43], v[40:43], a[164:167], a[40:43]\n"
"v_mfma_f32_16x16x32_bf16 a[72:75], v[40:43], a[168:171], a[72:75]\n"
"v_mfma_f32_16x16x32_bf16 a[104:107], v[40:43], a[172:175], a[104:107]\n"
"buffer_load_dwordx4 v[220:223], v7, s[20:23], s64 offen\n"
"v_mfma_f32_16x16x32_bf16 a[136:139], v[40:43], a[176:179], a[136:139]\n"
"s_mul_i32 s64, s56, 640\n"
"v_perm_b32 v40, v165, v161, s48\n"
"v_perm_b32 v41, v173, v169, s48\n"
"v_mfma_f32_16x16x32_bf16 a[12:15], v[44:47], a[160:163], a[12:15]\n"
"s_add_u32 s64, s64, 5120\n"
"s_waitcnt vmcnt(10) lgkmcnt(0)\n"
"v_perm_b32 v34, v180, v176, s48\n"
"v_perm_b32 v38, v180, v176, s49\n"
"v_mfma_f32_16x16x32_bf16 a[44:47], v[44:47], a[164:167], a[44:47]\n"
"v_perm_b32 v42, v181, v177, s48\n"
"v_mfma_f32_16x16x32_bf16 a[76:79], v[44:47], a[168:171], a[76:79]\n"
"buffer_load_dwordx4 v[64:67], v0, s[20:23], s65 offen\n"
"v_mfma_f32_16x16x32_bf16 a[108:111], v[44:47], a[172:175], a[108:111]\n"
"v_mfma_f32_16x16x32_bf16 a[140:143], v[44:47], a[176:179], a[140:143]\n"
"v_perm_b32 v44, v165, v161, s49\n"
"v_perm_b32 v45, v173, v169, s49\n"
"v_mfma_f32_16x16x32_bf16 a[16:19], v[48:51], a[160:163], a[16:19]\n"
"v_perm_b32 v46, v181, v177, s49\n"
"v_mfma_f32_16x16x32_bf16 a[48:51], v[48:51], a[164:167], a[48:51]\n"
"buffer_load_dwordx4 v[68:71], v1, s[20:23], s65 offen\n"
"v_mfma_f32_16x16x32_bf16 a[80:83], v[48:51], a[168:171], a[80:83]\n"
"s_waitcnt vmcnt(10)\n"
"v_perm_b32 v32, v164, v160, s48\n"
"v_perm_b32 v35, v188, v184, s48\n"
"v_mfma_f32_16x16x32_bf16 a[112:115], v[48:51], a[172:175], a[112:115]\n"
"v_perm_b32 v39, v188, v184, s49\n"
"v_perm_b32 v43, v189, v185, s48\n"
"v_mfma_f32_16x16x32_bf16 a[144:147], v[48:51], a[176:179], a[144:147]\n"
"v_perm_b32 v48, v166, v162, s48\n"
"v_perm_b32 v49, v174, v170, s48\n"
"v_mfma_f32_16x16x32_bf16 a[20:23], v[52:55], a[160:163], a[20:23]\n"
"buffer_load_dwordx4 v[72:75], v2, s[20:23], s65 offen\n"
"v_mfma_f32_16x16x32_bf16 a[52:55], v[52:55], a[164:167], a[52:55]\n"
"v_perm_b32 v50, v182, v178, s48\n"
"v_perm_b32 v47, v189, v185, s49\n"
"v_mfma_f32_16x16x32_bf16 a[84:87], v[52:55], a[168:171], a[84:87]\n"
"v_perm_b32 v51, v190, v186, s48\n"
"v_mfma_f32_16x16x32_bf16 a[116:119], v[52:55], a[172:175], a[116:119]\n"
"v_mfma_f32_16x16x32_bf16 a[148:151], v[52:55], a[176:179], a[148:151]\n"
"buffer_load_dwordx4 v[76:79], v3, s[20:23], s65 offen\n"
"v_mfma_f32_16x16x32_bf16 a[24:27], v[56:59], a[160:163], a[24:27]\n"
"v_perm_b32 v52, v166, v162, s49\n"
"v_perm_b32 v53, v174, v170, s49\n"
"v_mfma_f32_16x16x32_bf16 a[56:59], v[56:59], a[164:167], a[56:59]\n"
"v_perm_b32 v54, v182, v178, s49\n"
"v_perm_b32 v55, v190, v186, s49\n"
"v_mfma_f32_16x16x32_bf16 a[88:91], v[56:59], a[168:171], a[88:91]\n"
"v_mfma_f32_16x16x32_bf16 a[120:123], v[56:59], a[172:175], a[120:123]\n"
"buffer_load_dwordx4 v[80:83], v4, s[20:23], s65 offen\n"
"v_mfma_f32_16x16x32_bf16 a[152:155], v[56:59], a[176:179], a[152:155]\n"
"v_perm_b32 v56, v167, v163, s48\n"
"v_perm_b32 v57, v175, v171, s48\n"
"v_mfma_f32_16x16x32_bf16 a[28:31], v[60:63], a[160:163], a[28:31]\n"
"v_perm_b32 v58, v183, v179, s48\n"
"v_perm_b32 v59, v191, v187, s48\n"
"v_mfma_f32_16x16x32_bf16 a[60:63], v[60:63], a[164:167], a[60:63]\n"
"ds_read_b128 a[160:163], v10 offset:0\n"
"v_mfma_f32_16x16x32_bf16 a[92:95], v[60:63], a[168:171], a[92:95]\n"
"buffer_load_dwordx4 v[84:87], v5, s[20:23], s65 offen\n"
"v_mfma_f32_16x16x32_bf16 a[124:127], v[60:63], a[172:175], a[124:127]\n"
"ds_read_b128 a[164:167], v10 offset:4096\n"
"v_mfma_f32_16x16x32_bf16 a[156:159], v[60:63], a[176:179], a[156:159]\n"
"v_perm_b32 v60, v167, v163, s49\n"
"v_perm_b32 v61, v175, v171, s49\n"
"v_mfma_f32_16x16x32_bf16 a[0:3], v[32:35], a[180:183], a[0:3]\n"
"v_perm_b32 v62, v183, v179, s49\n"
"v_perm_b32 v63, v191, v187, s49\n"
"v_mfma_f32_16x16x32_bf16 a[32:35], v[32:35], a[184:187], a[32:35]\n"
"ds_read_b128 a[168:171], v10 offset:8192\n"
"v_mfma_f32_16x16x32_bf16 a[64:67], v[32:35], a[188:191], a[64:67]\n"
"buffer_load_dwordx4 v[88:91], v6, s[20:23], s65 offen\n"
"v_mfma_f32_16x16x32_bf16 a[96:99], v[32:35], a[192:195], a[96:99]\n"
"ds_read_b128 a[172:175], v10 offset:12288\n"
"v_mfma_f32_16x16x32_bf16 a[128:131], v[32:35], a[196:199], a[128:131]\n"
"s_waitcnt vmcnt(11)\n"
"v_perm_b32 v33, v204, v200, s48\n"
"ds_read_b128 a[176:179], v10 offset:16384\n"
"v_mfma_f32_16x16x32_bf16 a[4:7], v[36:39], a[180:183], a[4:7]\n"
"v_mfma_f32_16x16x32_bf16 a[36:39], v[36:39], a[184:187], a[36:39]\n"
"buffer_load_dwordx4 v[92:95], v7, s[20:23], s65 offen\n"
"v_mfma_f32_16x16x32_bf16 a[68:71], v[36:39], a[188:191], a[68:71]\n"
"s_mul_i32 s65, s57, 640\n"
"s_waitcnt vmcnt(10)\n"
"v_perm_b32 v34, v212, v208, s48\n"
"s_add_u32 s65, s65, 7680\n"
"v_mfma_f32_16x16x32_bf16 a[100:103], v[36:39], a[192:195], a[100:103]\n"
"v_mfma_f32_16x16x32_bf16 a[132:135], v[36:39], a[196:199], a[132:135]\n"
"v_perm_b32 v36, v196, v192, s49\n"
"v_perm_b32 v37, v204, v200, s49\n"
"v_mfma_f32_16x16x32_bf16 a[8:11], v[40:43], a[180:183], a[8:11]\n"
"buffer_load_dwordx4 v[96:99], v0, s[20:23], s66 offen\n"
"v_mfma_f32_16x16x32_bf16 a[40:43], v[40:43], a[184:187], a[40:43]\n"
"v_perm_b32 v38, v212, v208, s49\n"
"v_mfma_f32_16x16x32_bf16 a[72:75], v[40:43], a[188:191], a[72:75]\n"
"v_mfma_f32_16x16x32_bf16 a[104:107], v[40:43], a[192:195], a[104:107]\n"
"v_mfma_f32_16x16x32_bf16 a[136:139], v[40:43], a[196:199], a[136:139]\n"
"buffer_load_dwordx4 v[100:103], v1, s[20:23], s66 offen\n"
"v_mfma_f32_16x16x32_bf16 a[12:15], v[44:47], a[180:183], a[12:15]\n"
"s_waitcnt vmcnt(10) lgkmcnt(2)\n"
"v_perm_b32 v32, v196, v192, s48\n"
"v_perm_b32 v40, v197, v193, s48\n"
"v_mfma_f32_16x16x32_bf16 a[44:47], v[44:47], a[184:187], a[44:47]\n"
"v_perm_b32 v41, v205, v201, s48\n"
"v_perm_b32 v42, v213, v209, s48\n"
"v_mfma_f32_16x16x32_bf16 a[76:79], v[44:47], a[188:191], a[76:79]\n"
"v_perm_b32 v35, v220, v216, s48\n"
"v_perm_b32 v39, v220, v216, s49\n"
"v_mfma_f32_16x16x32_bf16 a[108:111], v[44:47], a[192:195], a[108:111]\n"
"buffer_load_dwordx4 v[104:107], v2, s[20:23], s66 offen\n"
"v_mfma_f32_16x16x32_bf16 a[140:143], v[44:47], a[196:199], a[140:143]\n"
"v_perm_b32 v44, v197, v193, s49\n"
"v_perm_b32 v45, v205, v201, s49\n"
"v_mfma_f32_16x16x32_bf16 a[16:19], v[48:51], a[180:183], a[16:19]\n"
"v_perm_b32 v46, v213, v209, s49\n"
"v_perm_b32 v43, v221, v217, s48\n"
"v_mfma_f32_16x16x32_bf16 a[48:51], v[48:51], a[184:187], a[48:51]\n"
"v_perm_b32 v47, v221, v217, s49\n"
"v_mfma_f32_16x16x32_bf16 a[80:83], v[48:51], a[188:191], a[80:83]\n"
"buffer_load_dwordx4 v[108:111], v3, s[20:23], s66 offen\n"
"v_mfma_f32_16x16x32_bf16 a[112:115], v[48:51], a[192:195], a[112:115]\n"
"v_mfma_f32_16x16x32_bf16 a[144:147], v[48:51], a[196:199], a[144:147]\n"
"v_perm_b32 v48, v198, v194, s48\n"
"v_perm_b32 v49, v206, v202, s48\n"
"v_mfma_f32_16x16x32_bf16 a[20:23], v[52:55], a[180:183], a[20:23]\n"
"v_perm_b32 v50, v214, v210, s48\n"
"v_perm_b32 v51, v222, v218, s48\n"
"v_mfma_f32_16x16x32_bf16 a[52:55], v[52:55], a[184:187], a[52:55]\n"
"buffer_load_dwordx4 v[112:115], v4, s[20:23], s66 offen\n"
"v_mfma_f32_16x16x32_bf16 a[84:87], v[52:55], a[188:191], a[84:87]\n"
"v_mfma_f32_16x16x32_bf16 a[116:119], v[52:55], a[192:195], a[116:119]\n"
"v_mfma_f32_16x16x32_bf16 a[148:151], v[52:55], a[196:199], a[148:151]\n"
"v_perm_b32 v52, v198, v194, s49\n"
"v_perm_b32 v53, v206, v202, s49\n"
"v_mfma_f32_16x16x32_bf16 a[24:27], v[56:59], a[180:183], a[24:27]\n"
"buffer_load_dwordx4 v[116:119], v5, s[20:23], s66 offen\n"
"v_mfma_f32_16x16x32_bf16 a[56:59], v[56:59], a[184:187], a[56:59]\n"
"v_perm_b32 v54, v214, v210, s49\n"
"v_perm_b32 v55, v222, v218, s49\n"
"v_mfma_f32_16x16x32_bf16 a[88:91], v[56:59], a[188:191], a[88:91]\n"
"v_mfma_f32_16x16x32_bf16 a[120:123], v[56:59], a[192:195], a[120:123]\n"
"v_mfma_f32_16x16x32_bf16 a[152:155], v[56:59], a[196:199], a[152:155]\n"
"v_perm_b32 v56, v199, v195, s48\n"
"v_perm_b32 v57, v207, v203, s48\n"
"v_mfma_f32_16x16x32_bf16 a[28:31], v[60:63], a[180:183], a[28:31]\n"
"buffer_load_dwordx4 v[120:123], v6, s[20:23], s66 offen\n"
"v_mfma_f32_16x16x32_bf16 a[60:63], v[60:63], a[184:187], a[60:63]\n"
"v_perm_b32 v58, v215, v211, s48\n"
"v_perm_b32 v59, v223, v219, s48\n"
"v_mfma_f32_16x16x32_bf16 a[92:95], v[60:63], a[188:191], a[92:95]\n"
"ds_read_b128 a[180:183], v11 offset:0\n"
"v_mfma_f32_16x16x32_bf16 a[124:127], v[60:63], a[192:195], a[124:127]\n"
"ds_read_b128 a[184:187], v11 offset:4096\n"
"v_mfma_f32_16x16x32_bf16 a[156:159], v[60:63], a[196:199], a[156:159]\n"
"buffer_load_dwordx4 v[124:127], v7, s[20:23], s66 offen\n"
"v_mfma_f32_16x16x32_bf16 a[0:3], v[32:35], a[160:163], a[0:3]\n"
"s_mul_i32 s66, s58, 640\n"
"v_perm_b32 v60, v199, v195, s49\n"
"v_perm_b32 v61, v207, v203, s49\n"
"v_mfma_f32_16x16x32_bf16 a[32:35], v[32:35], a[164:167], a[32:35]\n"
"s_add_u32 s66, s66, 10240\n"
"v_perm_b32 v62, v215, v211, s49\n"
"v_perm_b32 v63, v223, v219, s49\n"
"v_mfma_f32_16x16x32_bf16 a[64:67], v[32:35], a[168:171], a[64:67]\n"
"s_waitcnt lgkmcnt(2)\n"
"ds_read_b128 a[188:191], v11 offset:8192\n"
"v_mfma_f32_16x16x32_bf16 a[96:99], v[32:35], a[172:175], a[96:99]\n"
"buffer_load_dwordx4 v[128:131], v0, s[20:23], s67 offen\n"
"v_mfma_f32_16x16x32_bf16 a[128:131], v[32:35], a[176:179], a[128:131]\n"
"s_waitcnt vmcnt(13)\n"
"ds_read_b128 a[192:195], v11 offset:12288\n"
"v_perm_b32 v33, v76, v72, s48\n"
"v_mfma_f32_16x16x32_bf16 a[4:7], v[36:39], a[160:163], a[4:7]\n"
"s_waitcnt vmcnt(11)\n"
"ds_read_b128 a[196:199], v11 offset:16384\n"
"v_perm_b32 v34, v84, v80, s48\n"
"v_mfma_f32_16x16x32_bf16 a[36:39], v[36:39], a[164:167], a[36:39]\n"
"v_mfma_f32_16x16x32_bf16 a[68:71], v[36:39], a[168:171], a[68:71]\n"
"buffer_load_dwordx4 v[132:135], v1, s[20:23], s67 offen\n"
"v_mfma_f32_16x16x32_bf16 a[100:103], v[36:39], a[172:175], a[100:103]\n"
"s_waitcnt vmcnt(10)\n"
"v_perm_b32 v32, v68, v64, s48\n"
"v_perm_b32 v35, v92, v88, s48\n"
"v_mfma_f32_16x16x32_bf16 a[132:135], v[36:39], a[176:179], a[132:135]\n"
"v_perm_b32 v37, v76, v72, s49\n"
"v_perm_b32 v38, v84, v80, s49\n"
"v_mfma_f32_16x16x32_bf16 a[8:11], v[40:43], a[160:163], a[8:11]\n"
"v_perm_b32 v36, v68, v64, s49\n"
"v_perm_b32 v39, v92, v88, s49\n"
"v_mfma_f32_16x16x32_bf16 a[40:43], v[40:43], a[164:167], a[40:43]\n"
"buffer_load_dwordx4 v[136:139], v2, s[20:23], s67 offen\n"
"v_mfma_f32_16x16x32_bf16 a[72:75], v[40:43], a[168:171], a[72:75]\n"
"v_mfma_f32_16x16x32_bf16 a[104:107], v[40:43], a[172:175], a[104:107]\n"
"v_mfma_f32_16x16x32_bf16 a[136:139], v[40:43], a[176:179], a[136:139]\n"
"v_perm_b32 v43, v93, v89, s48\n"
"v_perm_b32 v41, v77, v73, s48\n"
"v_mfma_f32_16x16x32_bf16 a[12:15], v[44:47], a[160:163], a[12:15]\n"
"buffer_load_dwordx4 v[140:143], v3, s[20:23], s67 offen\n"
"v_mfma_f32_16x16x32_bf16 a[44:47], v[44:47], a[164:167], a[44:47]\n"
"v_perm_b32 v42, v85, v81, s48\n"
"v_perm_b32 v40, v69, v65, s48\n"
"v_mfma_f32_16x16x32_bf16 a[76:79], v[44:47], a[168:171], a[76:79]\n"
"v_mfma_f32_16x16x32_bf16 a[108:111], v[44:47], a[172:175], a[108:111]\n"
"v_mfma_f32_16x16x32_bf16 a[140:143], v[44:47], a[176:179], a[140:143]\n"
"buffer_load_dwordx4 v[144:147], v4, s[20:23], s67 offen\n"
"v_mfma_f32_16x16x32_bf16 a[16:19], v[48:51], a[160:163], a[16:19]\n"
"v_perm_b32 v47, v93, v89, s49\n"
"v_lshrrev_b32 v93, 3, v16\n"
"v_mfma_f32_16x16x32_bf16 a[48:51], v[48:51], a[164:167], a[48:51]\n"
"v_lshlrev_b32 v93, 12, v93\n"
"v_perm_b32 v45, v77, v73, s49\n"
"v_mfma_f32_16x16x32_bf16 a[80:83], v[48:51], a[168:171], a[80:83]\n"
"v_perm_b32 v46, v85, v81, s49\n"
"v_perm_b32 v44, v69, v65, s49\n"
"v_mfma_f32_16x16x32_bf16 a[112:115], v[48:51], a[172:175], a[112:115]\n"
"buffer_load_dwordx4 v[148:151], v5, s[20:23], s67 offen\n"
"v_mfma_f32_16x16x32_bf16 a[144:147], v[48:51], a[176:179], a[144:147]\n"
"v_perm_b32 v51, v94, v90, s48\n"
"v_perm_b32 v49, v78, v74, s48\n"
"v_mfma_f32_16x16x32_bf16 a[20:23], v[52:55], a[160:163], a[20:23]\n"
"v_perm_b32 v50, v86, v82, s48\n"
"v_perm_b32 v48, v70, v66, s48\n"
"v_mfma_f32_16x16x32_bf16 a[52:55], v[52:55], a[164:167], a[52:55]\n"
"v_mfma_f32_16x16x32_bf16 a[84:87], v[52:55], a[168:171], a[84:87]\n"
"v_mfma_f32_16x16x32_bf16 a[116:119], v[52:55], a[172:175], a[116:119]\n"
"buffer_load_dwordx4 v[152:155], v6, s[20:23], s67 offen\n"
"v_mfma_f32_16x16x32_bf16 a[148:151], v[52:55], a[176:179], a[148:151]\n"
"v_perm_b32 v55, v94, v90, s49\n"
"v_and_b32 v94, 7, v16\n"
"v_mfma_f32_16x16x32_bf16 a[24:27], v[56:59], a[160:163], a[24:27]\n"
"v_perm_b32 v53, v78, v74, s49\n"
"v_perm_b32 v54, v86, v82, s49\n"
"v_mfma_f32_16x16x32_bf16 a[56:59], v[56:59], a[164:167], a[56:59]\n"
"v_perm_b32 v52, v70, v66, s49\n"
"v_mfma_f32_16x16x32_bf16 a[88:91], v[56:59], a[168:171], a[88:91]\n"
"buffer_load_dwordx4 v[156:159], v7, s[20:23], s67 offen\n"
"v_mfma_f32_16x16x32_bf16 a[120:123], v[56:59], a[172:175], a[120:123]\n"
"s_mul_i32 s67, s59, 640\n"
"s_add_u32 s67, s67, 12800\n"
"v_mfma_f32_16x16x32_bf16 a[152:155], v[56:59], a[176:179], a[152:155]\n"
"v_perm_b32 v57, v79, v75, s48\n"
"v_perm_b32 v58, v87, v83, s48\n"
"v_mfma_f32_16x16x32_bf16 a[28:31], v[60:63], a[160:163], a[28:31]\n"
"v_perm_b32 v56, v71, v67, s48\n"
"v_perm_b32 v59, v95, v91, s48\n"
"v_mfma_f32_16x16x32_bf16 a[60:63], v[60:63], a[164:167], a[60:63]\n"
"buffer_load_dwordx4 v[160:163], v0, s[20:23], s68 offen\n"
"v_mfma_f32_16x16x32_bf16 a[92:95], v[60:63], a[168:171], a[92:95]\n"
"v_mfma_f32_16x16x32_bf16 a[124:127], v[60:63], a[172:175], a[124:127]\n"
"v_mfma_f32_16x16x32_bf16 a[156:159], v[60:63], a[176:179], a[156:159]\n"
"s_waitcnt lgkmcnt(0)\n"
"v_perm_b32 v61, v79, v75, s49\n"
"v_perm_b32 v62, v87, v83, s49\n"
"v_mfma_f32_16x16x32_bf16 a[0:3], v[32:35], a[180:183], a[0:3]\n"
"buffer_load_dwordx4 v[164:167], v1, s[20:23], s68 offen\n"
"v_mfma_f32_16x16x32_bf16 a[32:35], v[32:35], a[184:187], a[32:35]\n"
"v_perm_b32 v60, v71, v67, s49\n"
"v_perm_b32 v63, v95, v91, s49\n"
"v_mfma_f32_16x16x32_bf16 a[64:67], v[32:35], a[188:191], a[64:67]\n"
"v_mfma_f32_16x16x32_bf16 a[96:99], v[32:35], a[192:195], a[96:99]\n"
"v_mfma_f32_16x16x32_bf16 a[128:131], v[32:35], a[196:199], a[128:131]\n"
"buffer_load_dwordx4 v[168:171], v2, s[20:23], s68 offen\n"
"v_mfma_f32_16x16x32_bf16 a[4:7], v[36:39], a[180:183], a[4:7]\n"
"s_waitcnt vmcnt(15)\n"
"v_perm_b32 v33, v108, v104, s48\n"
"s_waitcnt vmcnt(13)\n"
"v_perm_b32 v34, v116, v112, s48\n"
"v_mfma_f32_16x16x32_bf16 a[36:39], v[36:39], a[184:187], a[36:39]\n"
"s_waitcnt vmcnt(11)\n"
"v_perm_b32 v32, v100, v96, s48\n"
"v_perm_b32 v35, v124, v120, s48\n"
"v_mfma_f32_16x16x32_bf16 a[68:71], v[36:39], a[188:191], a[68:71]\n"
"v_mfma_f32_16x16x32_bf16 a[100:103], v[36:39], a[192:195], a[100:103]\n"
"buffer_load_dwordx4 v[172:175], v3, s[20:23], s68 offen\n"
"v_mfma_f32_16x16x32_bf16 a[132:135], v[36:39], a[196:199], a[132:135]\n"
"v_perm_b32 v37, v108, v104, s49\n"
"v_perm_b32 v38, v116, v112, s49\n"
"v_mfma_f32_16x16x32_bf16 a[8:11], v[40:43], a[180:183], a[8:11]\n"
"v_perm_b32 v36, v100, v96, s49\n"
"v_perm_b32 v39, v124, v120, s49\n"
"v_mfma_f32_16x16x32_bf16 a[40:43], v[40:43], a[184:187], a[40:43]\n"
"v_mfma_f32_16x16x32_bf16 a[72:75], v[40:43], a[188:191], a[72:75]\n"
"buffer_load_dwordx4 v[176:179], v4, s[20:23], s68 offen\n"
"v_mfma_f32_16x16x32_bf16 a[104:107], v[40:43], a[192:195], a[104:107]\n"
"v_mfma_f32_16x16x32_bf16 a[136:139], v[40:43], a[196:199], a[136:139]\n"
"v_perm_b32 v41, v109, v105, s48\n"
"v_perm_b32 v42, v117, v113, s48\n"
"v_mfma_f32_16x16x32_bf16 a[12:15], v[44:47], a[180:183], a[12:15]\n"
"v_perm_b32 v40, v101, v97, s48\n"
"v_perm_b32 v43, v125, v121, s48\n"
"v_mfma_f32_16x16x32_bf16 a[44:47], v[44:47], a[184:187], a[44:47]\n"
"buffer_load_dwordx4 v[180:183], v5, s[20:23], s68 offen\n"
"v_mfma_f32_16x16x32_bf16 a[76:79], v[44:47], a[188:191], a[76:79]\n"
"v_mfma_f32_16x16x32_bf16 a[108:111], v[44:47], a[192:195], a[108:111]\n"
"v_mfma_f32_16x16x32_bf16 a[140:143], v[44:47], a[196:199], a[140:143]\n"
"s_barrier\n"
"ds_read_b128 a[160:163], v8 offset:20480\n"
"v_perm_b32 v45, v109, v105, s49\n"
"v_mfma_f32_16x16x32_bf16 a[16:19], v[48:51], a[180:183], a[16:19]\n"
"ds_read_b128 a[164:167], v8 offset:24576\n"
"v_perm_b32 v46, v117, v113, s49\n"
"v_mfma_f32_16x16x32_bf16 a[48:51], v[48:51], a[184:187], a[48:51]\n"
"buffer_load_dwordx4 v[184:187], v6, s[20:23], s68 offen\n"
"v_mfma_f32_16x16x32_bf16 a[80:83], v[48:51], a[188:191], a[80:83]\n"
"ds_read_b128 a[168:171], v8 offset:28672\n"
"v_perm_b32 v44, v101, v97, s49\n"
"v_mfma_f32_16x16x32_bf16 a[112:115], v[48:51], a[192:195], a[112:115]\n"
"ds_read_b128 a[172:175], v8 offset:32768\n"
"v_perm_b32 v47, v125, v121, s49\n"
"v_mfma_f32_16x16x32_bf16 a[144:147], v[48:51], a[196:199], a[144:147]\n"
"ds_read_b128 a[176:179], v8 offset:36864\n"
"v_lshl_add_u32 v8, v94, 4, v93\n"
"v_mfma_f32_16x16x32_bf16 a[20:23], v[52:55], a[180:183], a[20:23]\n"
"buffer_load_dwordx4 v[188:191], v7, s[20:23], s68 offen\n"
"v_mfma_f32_16x16x32_bf16 a[52:55], v[52:55], a[184:187], a[52:55]\n"
"s_mul_i32 s68, s60, 640\n"
"v_perm_b32 v49, v110, v106, s48\n"
"v_perm_b32 v50, v118, v114, s48\n"
"v_mfma_f32_16x16x32_bf16 a[84:87], v[52:55], a[188:191], a[84:87]\n"
"s_add_u32 s68, s68, 15360\n"
"v_perm_b32 v48, v102, v98, s48\n"
"v_perm_b32 v51, v126, v122, s48\n"
"v_mfma_f32_16x16x32_bf16 a[116:119], v[52:55], a[192:195], a[116:119]\n"
"v_bfe_u32 v93, v16, 2, 2\n"
"v_xor_b32 v94, %2, v93\n"
"v_mfma_f32_16x16x32_bf16 a[148:151], v[52:55], a[196:199], a[148:151]\n"
"buffer_load_dwordx4 v[192:195], v0, s[20:23], s69 offen\n"
"v_mfma_f32_16x16x32_bf16 a[24:27], v[56:59], a[180:183], a[24:27]\n"
"v_perm_b32 v53, v110, v106, s49\n"
"v_perm_b32 v54, v118, v114, s49\n"
"v_mfma_f32_16x16x32_bf16 a[56:59], v[56:59], a[184:187], a[56:59]\n"
"v_perm_b32 v52, v102, v98, s49\n"
"v_lshlrev_b32 v0, 4, v16\n"
"v_mfma_f32_16x16x32_bf16 a[88:91], v[56:59], a[188:191], a[88:91]\n"
"v_perm_b32 v55, v126, v122, s49\n"
"v_lshlrev_b32 v93, 10, v93\n"
"v_mfma_f32_16x16x32_bf16 a[120:123], v[56:59], a[192:195], a[120:123]\n"
"buffer_load_dwordx4 v[196:199], v1, s[20:23], s69 offen\n"
"v_mfma_f32_16x16x32_bf16 a[152:155], v[56:59], a[196:199], a[152:155]\n"
"v_perm_b32 v57, v111, v107, s48\n"
"v_perm_b32 v58, v119, v115, s48\n"
"v_mfma_f32_16x16x32_bf16 a[28:31], v[60:63], a[180:183], a[28:31]\n"
"ds_read_b128 a[180:183], v9 offset:20480\n"
"v_perm_b32 v56, v103, v99, s48\n"
"v_mfma_f32_16x16x32_bf16 a[60:63], v[60:63], a[184:187], a[60:63]\n"
"ds_read_b128 a[184:187], v9 offset:24576\n"
"v_perm_b32 v59, v127, v123, s48\n"
"v_mfma_f32_16x16x32_bf16 a[92:95], v[60:63], a[188:191], a[92:95]\n"
"buffer_load_dwordx4 v[200:203], v2, s[20:23], s69 offen\n"
"v_mfma_f32_16x16x32_bf16 a[124:127], v[60:63], a[192:195], a[124:127]\n"
"ds_read_b128 a[188:191], v9 offset:28672\n"
"v_lshl_add_u32 v93, v94, 5, v93\n"
"v_mfma_f32_16x16x32_bf16 a[156:159], v[60:63], a[196:199], a[156:159]\n"
"s_waitcnt lgkmcnt(3)\n"
"ds_read_b128 a[192:195], v9 offset:32768\n"
"v_perm_b32 v61, v111, v107, s49\n"
"v_mfma_f32_16x16x32_bf16 a[0:3], v[32:35], a[160:163], a[0:3]\n"
"ds_read_b128 a[196:199], v9 offset:36864\n"
"v_perm_b32 v62, v119, v115, s49\n"
"v_mfma_f32_16x16x32_bf16 a[32:35], v[32:35], a[164:167], a[32:35]\n"
"buffer_load_dwordx4 v[204:207], v3, s[20:23], s69 offen\n"
"v_mfma_f32_16x16x32_bf16 a[64:67], v[32:35], a[168:171], a[64:67]\n"
"v_perm_b32 v60, v103, v99, s49\n"
"v_perm_b32 v63, v127, v123, s49\n"
"v_mfma_f32_16x16x32_bf16 a[96:99], v[32:35], a[172:175], a[96:99]\n"
"v_lshl_add_u32 v93, v17, 7, v93\n"
"v_and_b32 v94, 3, v16\n"
"v_mfma_f32_16x16x32_bf16 a[128:131], v[32:35], a[176:179], a[128:131]\n"
"s_waitcnt vmcnt(16)\n"
"v_perm_b32 v33, v140, v136, s48\n"
"s_waitcnt vmcnt(14)\n"
"v_perm_b32 v34, v148, v144, s48\n"
"v_mfma_f32_16x16x32_bf16 a[4:7], v[36:39], a[160:163], a[4:7]\n"
"buffer_load_dwordx4 v[208:211], v4, s[20:23], s69 offen\n"
"v_mfma_f32_16x16x32_bf16 a[36:39], v[36:39], a[164:167], a[36:39]\n"
"s_waitcnt vmcnt(13)\n"
"v_perm_b32 v32, v132, v128, s48\n"
"v_perm_b32 v35, v156, v152, s48\n"
"v_mfma_f32_16x16x32_bf16 a[68:71], v[36:39], a[168:171], a[68:71]\n"
"v_lshl_add_u32 v93, v94, 3, v93\n"
"v_mfma_f32_16x16x32_bf16 a[100:103], v[36:39], a[172:175], a[100:103]\n"
"v_mfma_f32_16x16x32_bf16 a[132:135], v[36:39], a[176:179], a[132:135]\n"
"buffer_load_dwordx4 v[212:215], v5, s[20:23], s69 offen\n"
"v_mfma_f32_16x16x32_bf16 a[8:11], v[40:43], a[160:163], a[8:11]\n"
"v_perm_b32 v37, v140, v136, s49\n"
"v_perm_b32 v38, v148, v144, s49\n"
"v_mfma_f32_16x16x32_bf16 a[40:43], v[40:43], a[164:167], a[40:43]\n"
"v_perm_b32 v36, v132, v128, s49\n"
"v_perm_b32 v39, v156, v152, s49\n"
"v_mfma_f32_16x16x32_bf16 a[72:75], v[40:43], a[168:171], a[72:75]\n"
"v_mfma_f32_16x16x32_bf16 a[104:107], v[40:43], a[172:175], a[104:107]\n"
"v_mfma_f32_16x16x32_bf16 a[136:139], v[40:43], a[176:179], a[136:139]\n"
"buffer_load_dwordx4 v[216:219], v6, s[20:23], s69 offen\n"
"v_mfma_f32_16x16x32_bf16 a[12:15], v[44:47], a[160:163], a[12:15]\n"
"v_perm_b32 v41, v141, v137, s48\n"
"v_perm_b32 v42, v149, v145, s48\n"
"v_mfma_f32_16x16x32_bf16 a[44:47], v[44:47], a[164:167], a[44:47]\n"
"v_perm_b32 v40, v133, v129, s48\n"
"v_perm_b32 v43, v157, v153, s48\n"
"v_mfma_f32_16x16x32_bf16 a[76:79], v[44:47], a[168:171], a[76:79]\n"
"v_mfma_f32_16x16x32_bf16 a[108:111], v[44:47], a[172:175], a[108:111]\n"
"buffer_load_dwordx4 v[220:223], v7, s[20:23], s69 offen\n"
"v_mfma_f32_16x16x32_bf16 a[140:143], v[44:47], a[176:179], a[140:143]\n"
"s_lshl_b32 s69, %2, 17\n"
"v_add_u32 v8, s69, v8\n"
"s_lshl_b32 s69, %2, 12\n"
"v_mfma_f32_16x16x32_bf16 a[16:19], v[48:51], a[160:163], a[16:19]\n"
"v_xor_b32 v9, 32, v8\n"
"v_perm_b32 v45, v141, v137, s49\n"
"v_mfma_f32_16x16x32_bf16 a[48:51], v[48:51], a[164:167], a[48:51]\n"
"v_perm_b32 v46, v149, v145, s49\n"
"v_lshrrev_b32 v7, 2, v16\n"
"v_mfma_f32_16x16x32_bf16 a[80:83], v[48:51], a[168:171], a[80:83]\n"
"buffer_load_dwordx4 v12, s[16:19], 0 offen nt lds\n"
"v_mfma_f32_16x16x32_bf16 a[112:115], v[48:51], a[172:175], a[112:115]\n"
"v_and_b32 v6, 7, v7\n"
"v_lshlrev_b32 v6, 5, v6\n"
"v_mfma_f32_16x16x32_bf16 a[144:147], v[48:51], a[176:179], a[144:147]\n"
"v_perm_b32 v49, v142, v138, s48\n"
"v_perm_b32 v50, v150, v146, s48\n"
"v_mfma_f32_16x16x32_bf16 a[20:23], v[52:55], a[160:163], a[20:23]\n"
"v_and_b32 v7, 3, v7\n"
"v_lshl_add_u32 v6, v7, 8, v6\n"
"v_mfma_f32_16x16x32_bf16 a[52:55], v[52:55], a[164:167], a[52:55]\n"
"buffer_load_dwordx4 v13, s[16:19], 0 offen offset:1024 nt lds\n"
"v_mfma_f32_16x16x32_bf16 a[84:87], v[52:55], a[168:171], a[84:87]\n"
"v_lshl_add_u32 v6, v17, 10, v6\n"
"v_and_b32 v7, 3, v16\n"
"v_mfma_f32_16x16x32_bf16 a[116:119], v[52:55], a[172:175], a[116:119]\n"
"v_lshl_add_u32 v6, v7, 3, v6\n"
"v_add_u32 v6, s75, v6\n"
"v_mfma_f32_16x16x32_bf16 a[148:151], v[52:55], a[176:179], a[148:151]\n"
"v_perm_b32 v53, v142, v138, s49\n"
"v_perm_b32 v54, v150, v146, s49\n"
"v_mfma_f32_16x16x32_bf16 a[24:27], v[56:59], a[160:163], a[24:27]\n"
"buffer_load_dwordx4 v12, s[16:19], 0 offen offset:2048 nt lds\n"
"v_mfma_f32_16x16x32_bf16 a[56:59], v[56:59], a[164:167], a[56:59]\n"
"v_add_u32 v7, 16384, v6\n"
"v_perm_b32 v44, v133, v129, s49\n"
"v_mfma_f32_16x16x32_bf16 a[88:91], v[56:59], a[168:171], a[88:91]\n"
"v_perm_b32 v48, v134, v130, s48\n"
"v_perm_b32 v52, v134, v130, s49\n"
"v_mfma_f32_16x16x32_bf16 a[120:123], v[56:59], a[172:175], a[120:123]\n"
"v_perm_b32 v47, v157, v153, s49\n"
"v_perm_b32 v51, v158, v154, s48\n"
"v_mfma_f32_16x16x32_bf16 a[152:155], v[56:59], a[176:179], a[152:155]\n"
"buffer_load_dwordx4 v13, s[16:19], 0 offen offset:3072 nt lds\n"
"v_mfma_f32_16x16x32_bf16 a[28:31], v[60:63], a[160:163], a[28:31]\n"
"s_add_u32 m0, s75, 16384\n"
"ds_read_b128 a[160:163], v10 offset:20480\n"
"v_perm_b32 v57, v143, v139, s48\n"
"v_mfma_f32_16x16x32_bf16 a[60:63], v[60:63], a[164:167], a[60:63]\n"
"ds_read_b128 a[164:167], v10 offset:24576\n"
"v_perm_b32 v58, v151, v147, s48\n"
"v_mfma_f32_16x16x32_bf16 a[92:95], v[60:63], a[168:171], a[92:95]\n"
"s_waitcnt vmcnt(10) lgkmcnt(2)\n"
"ds_read_b128 a[168:171], v10 offset:28672\n"
"v_perm_b32 v108, v197, v193, s49\n"
"v_mfma_f32_16x16x32_bf16 a[124:127], v[60:63], a[172:175], a[124:127]\n"
"buffer_load_dwordx4 v12, s[16:19], s50 offen nt lds\n"
"v_mfma_f32_16x16x32_bf16 a[156:159], v[60:63], a[176:179], a[156:159]\n"
"ds_read_b128 a[172:175], v10 offset:32768\n"
"v_perm_b32 v61, v143, v139, s49\n"
"v_mfma_f32_16x16x32_bf16 a[0:3], v[32:35], a[180:183], a[0:3]\n"
"ds_read_b128 a[176:179], v10 offset:36864\n"
"v_xor_b32 v10, 64, v8\n"
"v_mfma_f32_16x16x32_bf16 a[32:35], v[32:35], a[184:187], a[32:35]\n"
"v_perm_b32 v62, v151, v147, s49\n"
"v_perm_b32 v116, v198, v194, s49\n"
"v_mfma_f32_16x16x32_bf16 a[64:67], v[32:35], a[188:191], a[64:67]\n"
"buffer_load_dwordx4 v13, s[16:19], s50 offen offset:1024 nt lds\n"
"v_mfma_f32_16x16x32_bf16 a[96:99], v[32:35], a[192:195], a[96:99]\n"
"s_waitcnt vmcnt(10)\n"
"v_perm_b32 v97, v204, v200, s48\n"
"v_perm_b32 v109, v205, v201, s49\n"
"v_mfma_f32_16x16x32_bf16 a[128:131], v[32:35], a[196:199], a[128:131]\n"
"v_perm_b32 v33, v172, v168, s48\n"
"v_perm_b32 v34, v180, v176, s48\n"
"v_mfma_f32_16x16x32_bf16 a[4:7], v[36:39], a[180:183], a[4:7]\n"
"v_perm_b32 v117, v206, v202, s49\n"
"v_perm_b32 v32, v164, v160, s48\n"
"v_mfma_f32_16x16x32_bf16 a[36:39], v[36:39], a[184:187], a[36:39]\n"
"v_perm_b32 v125, v207, v203, s49\n"
"v_perm_b32 v35, v188, v184, s48\n"
"v_mfma_f32_16x16x32_bf16 a[68:71], v[36:39], a[188:191], a[68:71]\n"
"buffer_load_dwordx4 v12, s[16:19], s50 offen offset:2048 nt lds\n"
"v_mfma_f32_16x16x32_bf16 a[100:103], v[36:39], a[192:195], a[100:103]\n"
"v_perm_b32 v56, v135, v131, s48\n"
"v_perm_b32 v55, v158, v154, s49\n"
"v_mfma_f32_16x16x32_bf16 a[132:135], v[36:39], a[196:199], a[132:135]\n"
"v_perm_b32 v37, v172, v168, s49\n"
"v_perm_b32 v38, v180, v176, s49\n"
"v_mfma_f32_16x16x32_bf16 a[8:11], v[40:43], a[180:183], a[8:11]\n"
"v_perm_b32 v36, v164, v160, s49\n"
"v_perm_b32 v39, v188, v184, s49\n"
"v_mfma_f32_16x16x32_bf16 a[40:43], v[40:43], a[184:187], a[40:43]\n"
"s_waitcnt vmcnt(9) lgkmcnt(2)\n"
"v_perm_b32 v98, v212, v208, s48\n"
"v_perm_b32 v106, v213, v209, s48\n"
"v_mfma_f32_16x16x32_bf16 a[72:75], v[40:43], a[188:191], a[72:75]\n"
"v_perm_b32 v118, v214, v210, s49\n"
"v_perm_b32 v59, v159, v155, s48\n"
"v_mfma_f32_16x16x32_bf16 a[104:107], v[40:43], a[192:195], a[104:107]\n"
"v_perm_b32 v60, v135, v131, s49\n"
"v_perm_b32 v100, v196, v192, s49\n"
"v_mfma_f32_16x16x32_bf16 a[136:139], v[40:43], a[196:199], a[136:139]\n"
"v_perm_b32 v41, v173, v169, s48\n"
"v_perm_b32 v42, v181, v177, s48\n"
"v_mfma_f32_16x16x32_bf16 a[12:15], v[44:47], a[180:183], a[12:15]\n"
"v_perm_b32 v40, v165, v161, s48\n"
"v_perm_b32 v43, v189, v185, s48\n"
"v_mfma_f32_16x16x32_bf16 a[44:47], v[44:47], a[184:187], a[44:47]\n"
"v_perm_b32 v104, v197, v193, s48\n"
"v_perm_b32 v112, v198, v194, s48\n"
"v_mfma_f32_16x16x32_bf16 a[76:79], v[44:47], a[188:191], a[76:79]\n"
"v_perm_b32 v120, v199, v195, s48\n"
"v_perm_b32 v124, v199, v195, s49\n"
"v_mfma_f32_16x16x32_bf16 a[108:111], v[44:47], a[192:195], a[108:111]\n"
"v_perm_b32 v101, v204, v200, s49\n"
"v_perm_b32 v105, v205, v201, s48\n"
"v_mfma_f32_16x16x32_bf16 a[140:143], v[44:47], a[196:199], a[140:143]\n"
"v_perm_b32 v45, v173, v169, s49\n"
"v_perm_b32 v46, v181, v177, s49\n"
"v_mfma_f32_16x16x32_bf16 a[16:19], v[48:51], a[180:183], a[16:19]\n"
"s_waitcnt vmcnt(7) lgkmcnt(0)\n"
"v_perm_b32 v96, v196, v192, s48\n"
"v_perm_b32 v99, v220, v216, s48\n"
"v_mfma_f32_16x16x32_bf16 a[48:51], v[48:51], a[184:187], a[48:51]\n"
"v_perm_b32 v107, v221, v217, s48\n"
"v_perm_b32 v119, v222, v218, s49\n"
"v_mfma_f32_16x16x32_bf16 a[80:83], v[48:51], a[188:191], a[80:83]\n"
"v_perm_b32 v44, v165, v161, s49\n"
"v_perm_b32 v47, v189, v185, s49\n"
"v_mfma_f32_16x16x32_bf16 a[112:115], v[48:51], a[192:195], a[112:115]\n"
"v_perm_b32 v113, v206, v202, s48\n"
"v_perm_b32 v121, v207, v203, s48\n"
"v_mfma_f32_16x16x32_bf16 a[144:147], v[48:51], a[196:199], a[144:147]\n"
"v_perm_b32 v49, v174, v170, s48\n"
"v_perm_b32 v50, v182, v178, s48\n"
"v_mfma_f32_16x16x32_bf16 a[20:23], v[52:55], a[180:183], a[20:23]\n"
"v_perm_b32 v48, v166, v162, s48\n"
"v_perm_b32 v51, v190, v186, s48\n"
"v_mfma_f32_16x16x32_bf16 a[52:55], v[52:55], a[184:187], a[52:55]\n"
"v_perm_b32 v102, v212, v208, s49\n"
"v_perm_b32 v110, v213, v209, s49\n"
"v_mfma_f32_16x16x32_bf16 a[84:87], v[52:55], a[188:191], a[84:87]\n"
"v_perm_b32 v114, v214, v210, s48\n"
"v_perm_b32 v122, v215, v211, s48\n"
"v_mfma_f32_16x16x32_bf16 a[116:119], v[52:55], a[192:195], a[116:119]\n"
"v_perm_b32 v126, v215, v211, s49\n"
"v_perm_b32 v103, v220, v216, s49\n"
"v_mfma_f32_16x16x32_bf16 a[148:151], v[52:55], a[196:199], a[148:151]\n"
"v_perm_b32 v53, v174, v170, s49\n"
"v_perm_b32 v54, v182, v178, s49\n"
"v_mfma_f32_16x16x32_bf16 a[24:27], v[56:59], a[180:183], a[24:27]\n"
"v_perm_b32 v52, v166, v162, s49\n"
"v_perm_b32 v55, v190, v186, s49\n"
"v_mfma_f32_16x16x32_bf16 a[56:59], v[56:59], a[184:187], a[56:59]\n"
"v_perm_b32 v111, v221, v217, s49\n"
"v_perm_b32 v115, v222, v218, s48\n"
"v_mfma_f32_16x16x32_bf16 a[88:91], v[56:59], a[188:191], a[88:91]\n"
"v_perm_b32 v123, v223, v219, s48\n"
"v_perm_b32 v127, v223, v219, s49\n"
"v_mfma_f32_16x16x32_bf16 a[120:123], v[56:59], a[192:195], a[120:123]\n"
"v_perm_b32 v63, v159, v155, s49\n"
"v_add_u32 v92, s69, v93\n"
"v_mfma_f32_16x16x32_bf16 a[152:155], v[56:59], a[196:199], a[152:155]\n"
"v_perm_b32 v57, v175, v171, s48\n"
"v_perm_b32 v58, v183, v179, s48\n"
"v_mfma_f32_16x16x32_bf16 a[28:31], v[60:63], a[180:183], a[28:31]\n"
"ds_read_b128 a[180:183], v11 offset:20480\n"
"v_perm_b32 v56, v167, v163, s48\n"
"v_mfma_f32_16x16x32_bf16 a[60:63], v[60:63], a[184:187], a[60:63]\n"
"ds_read_b128 a[184:187], v11 offset:24576\n"
"v_perm_b32 v59, v191, v187, s48\n"
"v_mfma_f32_16x16x32_bf16 a[92:95], v[60:63], a[188:191], a[92:95]\n"
"ds_read_b128 a[188:191], v11 offset:28672\n"
"v_xor_b32 v93, 4096, v92\n"
"v_mfma_f32_16x16x32_bf16 a[124:127], v[60:63], a[192:195], a[124:127]\n"
"ds_read_b128 a[192:195], v11 offset:32768\n"
"v_xor_b32 v94, 8192, v92\n"
"v_mfma_f32_16x16x32_bf16 a[156:159], v[60:63], a[196:199], a[156:159]\n"
"ds_read_b128 a[196:199], v11 offset:36864\n"
"v_xor_b32 v11, 96, v8\n"
"v_mfma_f32_16x16x32_bf16 a[0:3], v[32:35], a[160:163], a[0:3]\n"
"v_perm_b32 v61, v175, v171, s49\n"
"v_perm_b32 v62, v183, v179, s49\n"
"v_mfma_f32_16x16x32_bf16 a[32:35], v[32:35], a[164:167], a[32:35]\n"
"v_perm_b32 v60, v167, v163, s49\n"
"v_perm_b32 v63, v191, v187, s49\n"
"v_mfma_f32_16x16x32_bf16 a[64:67], v[32:35], a[168:171], a[64:67]\n"
"v_xor_b32 v95, 12288, v92\n"
"v_lshrrev_b32 v188, 5, v16\n"
"v_mfma_f32_16x16x32_bf16 a[96:99], v[32:35], a[172:175], a[96:99]\n"
"v_lshl_add_u32 v1, v188, 24, v0\n"
"v_xor_b32 v191, 7, v16\n"
"v_mfma_f32_16x16x32_bf16 a[128:131], v[32:35], a[176:179], a[128:131]\n"
"v_lshlrev_b32 v191, 20, v191\n"
"v_lshrrev_b32 v190, 3, v16\n"
"v_mfma_f32_16x16x32_bf16 a[4:7], v[36:39], a[160:163], a[4:7]\n"
"v_lshlrev_b32 v190, 20, v190\n"
"v_lshl_add_u32 v190, v16, 2, v190\n"
"v_mfma_f32_16x16x32_bf16 a[36:39], v[36:39], a[164:167], a[36:39]\n"
"v_lshlrev_b32 v188, 3, v188\n"
"v_and_b32 v189, 31, v16\n"
"v_mfma_f32_16x16x32_bf16 a[68:71], v[36:39], a[168:171], a[68:71]\n"
"v_lshl_add_u32 v188, v189, 4, v188\n"
"v_lshlrev_b32 v189, 11, v18\n"
"v_mfma_f32_16x16x32_bf16 a[100:103], v[36:39], a[172:175], a[100:103]\n"
"v_lshl_add_u32 v189, v17, 3, v189\n"
"v_mfma_f32_16x16x32_bf16 a[132:135], v[36:39], a[176:179], a[132:135]\n"
"v_mfma_f32_16x16x32_bf16 a[8:11], v[40:43], a[160:163], a[8:11]\n"
"v_mfma_f32_16x16x32_bf16 a[40:43], v[40:43], a[164:167], a[40:43]\n"
"v_mfma_f32_16x16x32_bf16 a[72:75], v[40:43], a[168:171], a[72:75]\n"
"v_mfma_f32_16x16x32_bf16 a[104:107], v[40:43], a[172:175], a[104:107]\n"
"v_mfma_f32_16x16x32_bf16 a[136:139], v[40:43], a[176:179], a[136:139]\n"
"v_mfma_f32_16x16x32_bf16 a[12:15], v[44:47], a[160:163], a[12:15]\n"
"v_mfma_f32_16x16x32_bf16 a[44:47], v[44:47], a[164:167], a[44:47]\n"
"v_mfma_f32_16x16x32_bf16 a[76:79], v[44:47], a[168:171], a[76:79]\n"
"v_mfma_f32_16x16x32_bf16 a[108:111], v[44:47], a[172:175], a[108:111]\n"
"v_mfma_f32_16x16x32_bf16 a[140:143], v[44:47], a[176:179], a[140:143]\n"
"v_mfma_f32_16x16x32_bf16 a[16:19], v[48:51], a[160:163], a[16:19]\n"
"v_mfma_f32_16x16x32_bf16 a[48:51], v[48:51], a[164:167], a[48:51]\n"
"v_mfma_f32_16x16x32_bf16 a[80:83], v[48:51], a[168:171], a[80:83]\n"
"v_mfma_f32_16x16x32_bf16 a[112:115], v[48:51], a[172:175], a[112:115]\n"
"v_mfma_f32_16x16x32_bf16 a[144:147], v[48:51], a[176:179], a[144:147]\n"
"v_mfma_f32_16x16x32_bf16 a[20:23], v[52:55], a[160:163], a[20:23]\n"
"v_mfma_f32_16x16x32_bf16 a[52:55], v[52:55], a[164:167], a[52:55]\n"
"v_mfma_f32_16x16x32_bf16 a[84:87], v[52:55], a[168:171], a[84:87]\n"
"v_mfma_f32_16x16x32_bf16 a[116:119], v[52:55], a[172:175], a[116:119]\n"
"v_mfma_f32_16x16x32_bf16 a[148:151], v[52:55], a[176:179], a[148:151]\n"
"s_waitcnt lgkmcnt(0)\n"
"s_barrier\n"
"v_mfma_f32_16x16x32_bf16 a[24:27], v[56:59], a[160:163], a[24:27]\n"
"v_mfma_f32_16x16x32_bf16 a[56:59], v[56:59], a[164:167], a[56:59]\n"
"v_mfma_f32_16x16x32_bf16 a[88:91], v[56:59], a[168:171], a[88:91]\n"
"v_mfma_f32_16x16x32_bf16 a[120:123], v[56:59], a[172:175], a[120:123]\n"
"v_mfma_f32_16x16x32_bf16 a[152:155], v[56:59], a[176:179], a[152:155]\n"
"v_mfma_f32_16x16x32_bf16 a[28:31], v[60:63], a[160:163], a[28:31]\n"
"v_mfma_f32_16x16x32_bf16 a[60:63], v[60:63], a[164:167], a[60:63]\n"
"v_mfma_f32_16x16x32_bf16 a[92:95], v[60:63], a[168:171], a[92:95]\n"
"v_mfma_f32_16x16x32_bf16 a[124:127], v[60:63], a[172:175], a[124:127]\n"
"v_mfma_f32_16x16x32_bf16 a[156:159], v[60:63], a[176:179], a[156:159]\n"
"v_mfma_f32_16x16x32_bf16 a[0:3], v[96:99], a[180:183], a[0:3]\n"
"v_mfma_f32_16x16x32_bf16 a[4:7], v[100:103], a[180:183], a[4:7]\n"
"v_mfma_f32_16x16x32_bf16 a[8:11], v[104:107], a[180:183], a[8:11]\n"
"v_mfma_f32_16x16x32_bf16 a[12:15], v[108:111], a[180:183], a[12:15]\n"
"v_mfma_f32_16x16x32_bf16 a[16:19], v[112:115], a[180:183], a[16:19]\n"
"v_mfma_f32_16x16x32_bf16 a[20:23], v[116:119], a[180:183], a[20:23]\n"
"v_mfma_f32_16x16x32_bf16 a[24:27], v[120:123], a[180:183], a[24:27]\n"
"v_mfma_f32_16x16x32_bf16 a[28:31], v[124:127], a[180:183], a[28:31]\n"
"v_mfma_f32_16x16x32_bf16 a[32:35], v[96:99], a[184:187], a[32:35]\n"
"v_accvgpr_read_b32 v21, a0\n"
"v_accvgpr_read_b32 v22, a4\n"
"v_mfma_f32_16x16x32_bf16 a[36:39], v[100:103], a[184:187], a[36:39]\n"
"v_cvt_pk_bf16_f32 v32, v21, v22\n"
"v_accvgpr_read_b32 v21, a1\n"
"v_mfma_f32_16x16x32_bf16 a[40:43], v[104:107], a[184:187], a[40:43]\n"
"v_accvgpr_read_b32 v22, a5\n"
"v_cvt_pk_bf16_f32 v36, v21, v22\n"
"v_mfma_f32_16x16x32_bf16 a[44:47], v[108:111], a[184:187], a[44:47]\n"
"v_accvgpr_read_b32 v21, a2\n"
"v_accvgpr_read_b32 v22, a6\n"
"v_mfma_f32_16x16x32_bf16 a[48:51], v[112:115], a[184:187], a[48:51]\n"
"v_accvgpr_read_b32 v23, a8\n"
"v_accvgpr_read_b32 v24, a12\n"
"v_mfma_f32_16x16x32_bf16 a[52:55], v[116:119], a[184:187], a[52:55]\n"
"v_cvt_pk_bf16_f32 v40, v21, v22\n"
"v_cvt_pk_bf16_f32 v33, v23, v24\n"
"v_mfma_f32_16x16x32_bf16 a[56:59], v[120:123], a[184:187], a[56:59]\n"
"v_accvgpr_read_b32 v21, a3\n"
"v_accvgpr_read_b32 v23, a9\n"
"v_mfma_f32_16x16x32_bf16 a[60:63], v[124:127], a[184:187], a[60:63]\n"
"buffer_load_dwordx4 v13, s[16:19], s50 offen offset:3072 nt lds\n"
"v_mfma_f32_16x16x32_bf16 a[64:67], v[96:99], a[188:191], a[64:67]\n"
"s_add_u32 m0, s75, 32768\n"
"v_accvgpr_read_b32 v24, a13\n"
"v_cvt_pk_bf16_f32 v37, v23, v24\n"
"v_mfma_f32_16x16x32_bf16 a[68:71], v[100:103], a[188:191], a[68:71]\n"
"v_accvgpr_read_b32 v22, a7\n"
"v_accvgpr_read_b32 v23, a10\n"
"v_mfma_f32_16x16x32_bf16 a[72:75], v[104:107], a[188:191], a[72:75]\n"
"v_accvgpr_read_b32 v24, a14\n"
"v_accvgpr_read_b32 v25, a16\n"
"v_mfma_f32_16x16x32_bf16 a[76:79], v[108:111], a[188:191], a[76:79]\n"
"buffer_load_dwordx4 v12, s[16:19], s51 offen nt lds\n"
"v_mfma_f32_16x16x32_bf16 a[80:83], v[112:115], a[188:191], a[80:83]\n"
"v_accvgpr_read_b32 v26, a20\n"
"v_cvt_pk_bf16_f32 v41, v23, v24\n"
"v_mfma_f32_16x16x32_bf16 a[84:87], v[116:119], a[188:191], a[84:87]\n"
"v_cvt_pk_bf16_f32 v34, v25, v26\n"
"v_accvgpr_read_b32 v23, a11\n"
"v_mfma_f32_16x16x32_bf16 a[88:91], v[120:123], a[188:191], a[88:91]\n"
"v_accvgpr_read_b32 v25, a17\n"
"v_accvgpr_read_b32 v26, a21\n"
"v_mfma_f32_16x16x32_bf16 a[92:95], v[124:127], a[188:191], a[92:95]\n"
"buffer_load_dwordx4 v13, s[16:19], s51 offen offset:1024 nt lds\n"
"v_mfma_f32_16x16x32_bf16 a[96:99], v[96:99], a[192:195], a[96:99]\n"
"v_cvt_pk_bf16_f32 v38, v25, v26\n"
"v_accvgpr_read_b32 v24, a15\n"
"v_mfma_f32_16x16x32_bf16 a[100:103], v[100:103], a[192:195], a[100:103]\n"
"v_accvgpr_read_b32 v25, a18\n"
"v_accvgpr_read_b32 v26, a22\n"
"v_mfma_f32_16x16x32_bf16 a[104:107], v[104:107], a[192:195], a[104:107]\n"
"v_accvgpr_read_b32 v27, a24\n"
"v_accvgpr_read_b32 v28, a28\n"
"v_mfma_f32_16x16x32_bf16 a[108:111], v[108:111], a[192:195], a[108:111]\n"
"buffer_load_dwordx4 v12, s[16:19], s51 offen offset:2048 nt lds\n"
"v_mfma_f32_16x16x32_bf16 a[112:115], v[112:115], a[192:195], a[112:115]\n"
"v_cvt_pk_bf16_f32 v42, v25, v26\n"
"v_cvt_pk_bf16_f32 v35, v27, v28\n"
"v_mfma_f32_16x16x32_bf16 a[116:119], v[116:119], a[192:195], a[116:119]\n"
"v_accvgpr_read_b32 v25, a19\n"
"v_accvgpr_read_b32 v27, a25\n"
"v_mfma_f32_16x16x32_bf16 a[120:123], v[120:123], a[192:195], a[120:123]\n"
"v_accvgpr_read_b32 v28, a29\n"
"v_cvt_pk_bf16_f32 v39, v27, v28\n"
"v_mfma_f32_16x16x32_bf16 a[124:127], v[124:127], a[192:195], a[124:127]\n"
"buffer_load_dwordx4 v13, s[16:19], s51 offen offset:3072 nt lds\n"
"v_mfma_f32_16x16x32_bf16 a[128:131], v[96:99], a[196:199], a[128:131]\n"
"s_add_u32 m0, s75, 49152\n"
"v_accvgpr_read_b32 v26, a23\n"
"v_accvgpr_read_b32 v27, a26\n"
"v_mfma_f32_16x16x32_bf16 a[132:135], v[100:103], a[196:199], a[132:135]\n"
"v_accvgpr_read_b32 v28, a30\n"
"v_cvt_pk_bf16_f32 v44, v21, v22\n"
"v_mfma_f32_16x16x32_bf16 a[136:139], v[104:107], a[196:199], a[136:139]\n"
"v_cvt_pk_bf16_f32 v43, v27, v28\n"
"v_accvgpr_read_b32 v21, a32\n"
"v_mfma_f32_16x16x32_bf16 a[140:143], v[108:111], a[196:199], a[140:143]\n"
"v_accvgpr_read_b32 v22, a36\n"
"v_accvgpr_read_b32 v27, a27\n"
"v_mfma_f32_16x16x32_bf16 a[144:147], v[112:115], a[196:199], a[144:147]\n"
"buffer_load_dwordx4 v12, s[16:19], s52 offen nt lds\n"
"v_mfma_f32_16x16x32_bf16 a[148:151], v[116:119], a[196:199], a[148:151]\n"
"v_cvt_pk_bf16_f32 v48, v21, v22\n"
"v_accvgpr_read_b32 v21, a33\n"
"v_mfma_f32_16x16x32_bf16 a[152:155], v[120:123], a[196:199], a[152:155]\n"
"v_accvgpr_read_b32 v22, a37\n"
"v_accvgpr_read_b32 v28, a31\n"
"v_mfma_f32_16x16x32_bf16 a[156:159], v[124:127], a[196:199], a[156:159]\n"
"v_cvt_pk_bf16_f32 v52, v21, v22\n"
"v_cvt_pk_bf16_f32 v45, v23, v24\n"
"v_mfma_f32_16x16x32_bf16 v[128:131], a[200:203], v[32:35], 0\n"
"buffer_load_dwordx4 v13, s[16:19], s52 offen offset:1024 nt lds\n"
"v_mfma_f32_16x16x32_bf16 v[132:135], a[216:219], v[32:35], 0\n"
"v_accvgpr_read_b32 v21, a34\n"
"v_accvgpr_read_b32 v22, a38\n"
"v_mfma_f32_16x16x32_bf16 v[136:139], a[232:235], v[32:35], 0\n"
"v_accvgpr_read_b32 v23, a40\n"
"v_accvgpr_read_b32 v24, a44\n"
"v_mfma_f32_16x16x32_bf16 v[140:143], a[248:251], v[32:35], 0\n"
"v_cvt_pk_bf16_f32 v56, v21, v22\n"
"v_cvt_pk_bf16_f32 v49, v23, v24\n"
"v_mfma_f32_16x16x32_bf16 v[144:147], v[232:235], v[32:35], 0\n"
"v_accvgpr_read_b32 v21, a35\n"
"v_accvgpr_read_b32 v23, a41\n"
"v_mfma_f32_16x16x32_bf16 v[128:131], a[204:207], v[36:39], v[128:131]\n"
"v_accvgpr_read_b32 v24, a45\n"
"v_cvt_pk_bf16_f32 v53, v23, v24\n"
"v_mfma_f32_16x16x32_bf16 v[132:135], a[220:223], v[36:39], v[132:135]\n"
"v_accvgpr_read_b32 v22, a39\n"
"v_cvt_pk_bf16_f32 v46, v25, v26\n"
"v_mfma_f32_16x16x32_bf16 v[136:139], a[236:239], v[36:39], v[136:139]\n"
"v_accvgpr_read_b32 v23, a42\n"
"v_accvgpr_read_b32 v24, a46\n"
"v_mfma_f32_16x16x32_bf16 v[140:143], a[252:255], v[36:39], v[140:143]\n"
"v_accvgpr_read_b32 v25, a48\n"
"v_accvgpr_read_b32 v26, a52\n"
"v_mfma_f32_16x16x32_bf16 v[144:147], v[236:239], v[36:39], v[144:147]\n"
"v_cvt_pk_bf16_f32 v57, v23, v24\n"
"v_cvt_pk_bf16_f32 v50, v25, v26\n"
"v_mfma_f32_16x16x32_bf16 v[128:131], a[208:211], v[40:43], v[128:131]\n"
"v_accvgpr_read_b32 v23, a43\n"
"v_accvgpr_read_b32 v25, a49\n"
"v_mfma_f32_16x16x32_bf16 v[132:135], a[224:227], v[40:43], v[132:135]\n"
"v_accvgpr_read_b32 v26, a53\n"
"v_cvt_pk_bf16_f32 v54, v25, v26\n"
"v_mfma_f32_16x16x32_bf16 v[136:139], a[240:243], v[40:43], v[136:139]\n"
"v_accvgpr_read_b32 v24, a47\n"
"v_cvt_pk_bf16_f32 v47, v27, v28\n"
"v_mfma_f32_16x16x32_bf16 v[140:143], v[224:227], v[40:43], v[140:143]\n"
"v_accvgpr_read_b32 v25, a50\n"
"v_accvgpr_read_b32 v26, a54\n"
"v_mfma_f32_16x16x32_bf16 v[144:147], v[240:243], v[40:43], v[144:147]\n"
"v_accvgpr_read_b32 v27, a56\n"
"v_accvgpr_read_b32 v28, a60\n"
"v_mfma_f32_16x16x32_bf16 v[128:131], a[212:215], v[44:47], v[128:131]\n"
"v_cvt_pk_bf16_f32 v58, v25, v26\n"
"v_cvt_pk_bf16_f32 v51, v27, v28\n"
"v_mfma_f32_16x16x32_bf16 v[132:135], a[228:231], v[44:47], v[132:135]\n"
"v_accvgpr_read_b32 v25, a51\n"
"v_accvgpr_read_b32 v27, a57\n"
"v_mfma_f32_16x16x32_bf16 v[136:139], a[244:247], v[44:47], v[136:139]\n"
"v_accvgpr_read_b32 v28, a61\n"
"v_cvt_pk_bf16_f32 v55, v27, v28\n"
"v_mfma_f32_16x16x32_bf16 v[140:143], v[228:231], v[44:47], v[140:143]\n"
"v_max3_f32 v19, v128, v129, v130\n"
"v_accvgpr_read_b32 v26, a55\n"
"v_mfma_f32_16x16x32_bf16 v[144:147], v[244:247], v[44:47], v[144:147]\n"
"v_max3_f32 v20, v131, v132, v133\n"
"v_max3_f32 v19, v19, v134, v135\n"
"v_mfma_f32_16x16x32_bf16 v[148:151], a[200:203], v[48:51], 0\n"
"v_max3_f32 v20, v20, v136, v137\n"
"v_max3_f32 v19, v19, v138, v139\n"
"v_mfma_f32_16x16x32_bf16 v[152:155], a[216:219], v[48:51], 0\n"
"v_max3_f32 v20, v20, v140, v141\n"
"v_max3_f32 v19, v19, v142, v143\n"
"v_mfma_f32_16x16x32_bf16 v[156:159], a[232:235], v[48:51], 0\n"
"v_max3_f32 v20, v20, v144, v145\n"
"v_max3_f32 v19, v19, v146, v147\n"
"v_mfma_f32_16x16x32_bf16 v[160:163], a[248:251], v[48:51], 0\n"
"v_max_f32 v19, v19, v20\n"
"v_mov_b32 v20, v19\n"
"v_mfma_f32_16x16x32_bf16 v[164:167], v[232:235], v[48:51], 0\n"
"v_accvgpr_read_b32 v27, a58\n"
"v_accvgpr_read_b32 v28, a62\n"
"v_mfma_f32_16x16x32_bf16 v[148:151], a[204:207], v[52:55], v[148:151]\n"
"v_permlane16_swap_b32 v19, v20\n"
"v_mfma_f32_16x16x32_bf16 v[152:155], a[220:223], v[52:55], v[152:155]\n"
"v_max_f32 v19, v19, v20\n"
"v_mov_b32 v20, v19\n"
"v_mfma_f32_16x16x32_bf16 v[156:159], a[236:239], v[52:55], v[156:159]\n"
"v_cvt_pk_bf16_f32 v59, v27, v28\n"
"v_accvgpr_read_b32 v27, a59\n"
"v_mfma_f32_16x16x32_bf16 v[160:163], a[252:255], v[52:55], v[160:163]\n"
"v_permlane32_swap_b32 v19, v20\n"
"v_mfma_f32_16x16x32_bf16 v[164:167], v[236:239], v[52:55], v[164:167]\n"
"v_max_f32 v19, v19, v20\n"
"v_mul_f32 v20, s74, v19\n"
"v_mfma_f32_16x16x32_bf16 v[148:151], a[208:211], v[56:59], v[148:151]\n"
"v_pk_fma_f32 v[128:129], v[128:129], s[74:75], v[20:21] op_sel_hi:[1,0,0] neg_lo:[0,0,1] neg_hi:[0,0,1]\n"
"v_pk_fma_f32 v[130:131], v[130:131], s[74:75], v[20:21] op_sel_hi:[1,0,0] neg_lo:[0,0,1] neg_hi:[0,0,1]\n"
"v_mfma_f32_16x16x32_bf16 v[152:155], a[224:227], v[56:59], v[152:155]\n"
"v_pk_fma_f32 v[132:133], v[132:133], s[74:75], v[20:21] op_sel_hi:[1,0,0] neg_lo:[0,0,1] neg_hi:[0,0,1]\n"
"v_pk_fma_f32 v[134:135], v[134:135], s[74:75], v[20:21] op_sel_hi:[1,0,0] neg_lo:[0,0,1] neg_hi:[0,0,1]\n"
"v_mfma_f32_16x16x32_bf16 v[156:159], a[240:243], v[56:59], v[156:159]\n"
"v_pk_fma_f32 v[136:137], v[136:137], s[74:75], v[20:21] op_sel_hi:[1,0,0] neg_lo:[0,0,1] neg_hi:[0,0,1]\n"
"v_pk_fma_f32 v[138:139], v[138:139], s[74:75], v[20:21] op_sel_hi:[1,0,0] neg_lo:[0,0,1] neg_hi:[0,0,1]\n"
"v_mfma_f32_16x16x32_bf16 v[160:163], v[224:227], v[56:59], v[160:163]\n"
"v_pk_fma_f32 v[140:141], v[140:141], s[74:75], v[20:21] op_sel_hi:[1,0,0] neg_lo:[0,0,1] neg_hi:[0,0,1]\n"
"v_pk_fma_f32 v[142:143], v[142:143], s[74:75], v[20:21] op_sel_hi:[1,0,0] neg_lo:[0,0,1] neg_hi:[0,0,1]\n"
"v_mfma_f32_16x16x32_bf16 v[164:167], v[240:243], v[56:59], v[164:167]\n"
"v_pk_fma_f32 v[144:145], v[144:145], s[74:75], v[20:21] op_sel_hi:[1,0,0] neg_lo:[0,0,1] neg_hi:[0,0,1]\n"
"v_pk_fma_f32 v[146:147], v[146:147], s[74:75], v[20:21] op_sel_hi:[1,0,0] neg_lo:[0,0,1] neg_hi:[0,0,1]\n"
"v_exp_f32 v128, v128\n"
"v_exp_f32 v129, v129\n"
"v_exp_f32 v130, v130\n"
"v_exp_f32 v131, v131\n"
"v_exp_f32 v132, v132\n"
"v_exp_f32 v133, v133\n"
"v_exp_f32 v134, v134\n"
"v_exp_f32 v135, v135\n"
"v_exp_f32 v136, v136\n"
"v_exp_f32 v137, v137\n"
"v_exp_f32 v138, v138\n"
"v_exp_f32 v139, v139\n"
"v_exp_f32 v140, v140\n"
"v_exp_f32 v141, v141\n"
"v_exp_f32 v142, v142\n"
"v_exp_f32 v143, v143\n"
"v_exp_f32 v144, v144\n"
"v_exp_f32 v145, v145\n"
"v_exp_f32 v146, v146\n"
"v_exp_f32 v147, v147\n"
"v_pk_add_f32 v[168:169], v[128:129], v[130:131]\n"
"v_pk_add_f32 v[170:171], v[132:133], v[134:135]\n"
"v_pk_add_f32 v[168:169], v[168:169], v[136:137]\n"
"v_pk_add_f32 v[170:171], v[170:171], v[138:139]\n"
"v_pk_add_f32 v[168:169], v[168:169], v[140:141]\n"
"v_pk_add_f32 v[170:171], v[170:171], v[142:143]\n"
"v_pk_add_f32 v[168:169], v[168:169], v[144:145]\n"
"v_pk_add_f32 v[170:171], v[170:171], v[146:147]\n"
"v_pk_add_f32 v[168:169], v[168:169], v[170:171]\n"
"v_add_f32 v30, v168, v169\n"
"v_mov_b32 v29, v30\n"
"v_accvgpr_read_b32 v28, a63\n"
"v_cvt_pk_bf16_f32 v61, v23, v24\n"
"v_permlane16_swap_b32 v30, v29\n"
"v_add_f32 v30, v30, v29\n"
"v_mov_b32 v29, v30\n"
"v_accvgpr_read_b32 v23, a72\n"
"v_accvgpr_read_b32 v24, a76\n"
"v_permlane32_swap_b32 v30, v29\n"
"v_add_f32 v30, v30, v29\n"
"v_rcp_f32 v30, v30\n"
"v_cvt_pk_bf16_f32 v33, v23, v24\n"
"v_pk_mul_f32 v[128:129], v[128:129], v[30:31] op_sel_hi:[1,0]\n"
"v_pk_mul_f32 v[130:131], v[130:131], v[30:31] op_sel_hi:[1,0]\n"
"v_pk_mul_f32 v[132:133], v[132:133], v[30:31] op_sel_hi:[1,0]\n"
"v_pk_mul_f32 v[134:135], v[134:135], v[30:31] op_sel_hi:[1,0]\n"
"v_pk_mul_f32 v[136:137], v[136:137], v[30:31] op_sel_hi:[1,0]\n"
"v_pk_mul_f32 v[138:139], v[138:139], v[30:31] op_sel_hi:[1,0]\n"
"v_pk_mul_f32 v[140:141], v[140:141], v[30:31] op_sel_hi:[1,0]\n"
"v_pk_mul_f32 v[142:143], v[142:143], v[30:31] op_sel_hi:[1,0]\n"
"v_pk_mul_f32 v[144:145], v[144:145], v[30:31] op_sel_hi:[1,0]\n"
"v_pk_mul_f32 v[146:147], v[146:147], v[30:31] op_sel_hi:[1,0]\n"
"v_accvgpr_read_b32 v23, a73\n"
"v_accvgpr_read_b32 v24, a77\n"
"v_cvt_pk_bf16_f32 v37, v23, v24\n"
"v_accvgpr_read_b32 v23, a74\n"
"v_accvgpr_read_b32 v24, a78\n"
"v_cvt_pk_bf16_f32 v41, v23, v24\n"
"v_accvgpr_read_b32 v23, a75\n"
"v_accvgpr_read_b32 v24, a79\n"
"v_cvt_pk_bf16_f32 v45, v23, v24\n"
"v_cvt_pk_bf16_f32 v60, v21, v22\n"
"v_accvgpr_read_b32 v23, a104\n"
"v_accvgpr_read_b32 v24, a108\n"
"v_accvgpr_read_b32 v21, a64\n"
"v_accvgpr_read_b32 v22, a68\n"
"v_cvt_pk_bf16_f32 v49, v23, v24\n"
"v_cvt_pk_bf16_f32 v32, v21, v22\n"
"v_accvgpr_read_b32 v23, a105\n"
"v_accvgpr_read_b32 v24, a109\n"
"v_accvgpr_read_b32 v21, a65\n"
"v_accvgpr_read_b32 v22, a69\n"
"v_cvt_pk_bf16_f32 v53, v23, v24\n"
"v_cvt_pk_bf16_f32 v36, v21, v22\n"
"v_accvgpr_read_b32 v23, a106\n"
"v_accvgpr_read_b32 v24, a110\n"
"v_accvgpr_read_b32 v21, a66\n"
"v_accvgpr_read_b32 v22, a70\n"
"v_cvt_pk_bf16_f32 v57, v23, v24\n"
"v_cvt_pk_bf16_f32 v40, v21, v22\n"
"v_accvgpr_read_b32 v23, a107\n"
"v_accvgpr_read_b32 v24, a111\n"
"v_accvgpr_read_b32 v21, a67\n"
"v_accvgpr_read_b32 v22, a71\n"
"v_cvt_pk_bf16_f32 v44, v21, v22\n"
"v_accvgpr_read_b32 v21, a96\n"
"v_accvgpr_read_b32 v22, a100\n"
"v_cvt_pk_bf16_f32 v48, v21, v22\n"
"v_accvgpr_read_b32 v21, a97\n"
"v_accvgpr_read_b32 v22, a101\n"
"v_cvt_pk_bf16_f32 v52, v21, v22\n"
"v_accvgpr_read_b32 v21, a98\n"
"v_accvgpr_read_b32 v22, a102\n"
"v_cvt_pk_bf16_f32 v56, v21, v22\n"
"v_accvgpr_read_b32 v21, a99\n"
"v_accvgpr_read_b32 v22, a103\n"
"v_cvt_pk_bf16_f32 v62, v25, v26\n"
"v_accvgpr_read_b32 v25, a80\n"
"v_accvgpr_read_b32 v26, a84\n"
"v_cvt_pk_bf16_f32 v63, v27, v28\n"
"v_cvt_pk_bf16_f32 v34, v25, v26\n"
"v_accvgpr_read_b32 v27, a88\n"
"v_mfma_f32_16x16x32_bf16 v[148:151], a[212:215], v[60:63], v[148:151]\n"
"v_accvgpr_read_b32 v28, a92\n"
"v_accvgpr_read_b32 v25, a81\n"
"v_mfma_f32_16x16x32_bf16 v[152:155], a[228:231], v[60:63], v[152:155]\n"
"v_accvgpr_read_b32 v26, a85\n"
"v_cvt_pk_bf16_f32 v35, v27, v28\n"
"v_mfma_f32_16x16x32_bf16 v[156:159], a[244:247], v[60:63], v[156:159]\n"
"v_cvt_pk_bf16_f32 v38, v25, v26\n"
"v_accvgpr_read_b32 v27, a89\n"
"v_mfma_f32_16x16x32_bf16 v[160:163], v[228:231], v[60:63], v[160:163]\n"
"v_max3_f32 v19, v148, v149, v150\n"
"v_accvgpr_read_b32 v28, a93\n"
"v_mfma_f32_16x16x32_bf16 v[164:167], v[244:247], v[60:63], v[164:167]\n"
"v_max3_f32 v20, v151, v152, v153\n"
"v_max3_f32 v19, v19, v154, v155\n"
"v_max3_f32 v20, v20, v156, v157\n"
"v_max3_f32 v19, v19, v158, v159\n"
"v_cvt_pk_bf16_f32 v61, v23, v24\n"
"v_max3_f32 v20, v20, v160, v161\n"
"v_max3_f32 v19, v19, v162, v163\n"
"v_accvgpr_read_b32 v23, a136\n"
"v_max3_f32 v20, v20, v164, v165\n"
"v_max3_f32 v19, v19, v166, v167\n"
"v_max_f32 v19, v19, v20\n"
"v_mov_b32 v20, v19\n"
"v_accvgpr_read_b32 v24, a140\n"
"v_cvt_pk_bf16_f32 v60, v21, v22\n"
"v_permlane16_swap_b32 v19, v20\n"
"v_max_f32 v19, v19, v20\n"
"v_mov_b32 v20, v19\n"
"v_accvgpr_read_b32 v21, a128\n"
"v_accvgpr_read_b32 v22, a132\n"
"v_permlane32_swap_b32 v19, v20\n"
"v_max_f32 v19, v19, v20\n"
"v_mul_f32 v20, s74, v19\n"
"v_pk_fma_f32 v[148:149], v[148:149], s[74:75], v[20:21] op_sel_hi:[1,0,0] neg_lo:[0,0,1] neg_hi:[0,0,1]\n"
"v_pk_fma_f32 v[150:151], v[150:151], s[74:75], v[20:21] op_sel_hi:[1,0,0] neg_lo:[0,0,1] neg_hi:[0,0,1]\n"
"v_pk_fma_f32 v[152:153], v[152:153], s[74:75], v[20:21] op_sel_hi:[1,0,0] neg_lo:[0,0,1] neg_hi:[0,0,1]\n"
"v_pk_fma_f32 v[154:155], v[154:155], s[74:75], v[20:21] op_sel_hi:[1,0,0] neg_lo:[0,0,1] neg_hi:[0,0,1]\n"
"v_pk_fma_f32 v[156:157], v[156:157], s[74:75], v[20:21] op_sel_hi:[1,0,0] neg_lo:[0,0,1] neg_hi:[0,0,1]\n"
"v_pk_fma_f32 v[158:159], v[158:159], s[74:75], v[20:21] op_sel_hi:[1,0,0] neg_lo:[0,0,1] neg_hi:[0,0,1]\n"
"v_pk_fma_f32 v[160:161], v[160:161], s[74:75], v[20:21] op_sel_hi:[1,0,0] neg_lo:[0,0,1] neg_hi:[0,0,1]\n"
"v_pk_fma_f32 v[162:163], v[162:163], s[74:75], v[20:21] op_sel_hi:[1,0,0] neg_lo:[0,0,1] neg_hi:[0,0,1]\n"
"v_pk_fma_f32 v[164:165], v[164:165], s[74:75], v[20:21] op_sel_hi:[1,0,0] neg_lo:[0,0,1] neg_hi:[0,0,1]\n"
"v_pk_fma_f32 v[166:167], v[166:167], s[74:75], v[20:21] op_sel_hi:[1,0,0] neg_lo:[0,0,1] neg_hi:[0,0,1]\n"
"v_exp_f32 v148, v148\n"
"v_exp_f32 v149, v149\n"
"v_exp_f32 v150, v150\n"
"v_exp_f32 v151, v151\n"
"v_exp_f32 v152, v152\n"
"v_exp_f32 v153, v153\n"
"v_exp_f32 v154, v154\n"
"v_exp_f32 v155, v155\n"
"v_exp_f32 v156, v156\n"
"v_exp_f32 v157, v157\n"
"v_exp_f32 v158, v158\n"
"v_exp_f32 v159, v159\n"
"v_exp_f32 v160, v160\n"
"v_exp_f32 v161, v161\n"
"v_exp_f32 v162, v162\n"
"v_exp_f32 v163, v163\n"
"v_exp_f32 v164, v164\n"
"buffer_load_dwordx4 v12, s[16:19], s52 offen offset:2048 nt lds\n"
"v_exp_f32 v165, v165\n"
"v_exp_f32 v166, v166\n"
"v_exp_f32 v167, v167\n"
"v_pk_add_f32 v[178:179], v[148:149], v[150:151]\n"
"v_pk_add_f32 v[180:181], v[152:153], v[154:155]\n"
"v_pk_add_f32 v[178:179], v[178:179], v[156:157]\n"
"v_pk_add_f32 v[180:181], v[180:181], v[158:159]\n"
"v_pk_add_f32 v[178:179], v[178:179], v[160:161]\n"
"v_pk_add_f32 v[180:181], v[180:181], v[162:163]\n"
"v_pk_add_f32 v[178:179], v[178:179], v[164:165]\n"
"buffer_load_dwordx4 v13, s[16:19], s52 offen offset:3072 nt lds\n"
"s_add_u32 m0, s75, 65536\n"
"v_pk_add_f32 v[180:181], v[180:181], v[166:167]\n"
"v_pk_add_f32 v[178:179], v[178:179], v[180:181]\n"
"v_add_f32 v30, v178, v179\n"
"v_mov_b32 v29, v30\n"
"v_accvgpr_read_b32 v25, a82\n"
"v_accvgpr_read_b32 v26, a86\n"
"v_permlane16_swap_b32 v30, v29\n"
"v_add_f32 v30, v30, v29\n"
"v_mov_b32 v29, v30\n"
"v_cvt_pk_bf16_f32 v39, v27, v28\n"
"buffer_load_dwordx4 v12, s[16:19], s53 offen nt lds\n"
"v_permlane32_swap_b32 v30, v29\n"
"v_add_f32 v30, v30, v29\n"
"v_rcp_f32 v30, v30\n"
"v_cvt_pk_bf16_f32 v42, v25, v26\n"
"v_pk_mul_f32 v[148:149], v[148:149], v[30:31] op_sel_hi:[1,0]\n"
"v_pk_mul_f32 v[150:151], v[150:151], v[30:31] op_sel_hi:[1,0]\n"
"v_pk_mul_f32 v[152:153], v[152:153], v[30:31] op_sel_hi:[1,0]\n"
"v_pk_mul_f32 v[154:155], v[154:155], v[30:31] op_sel_hi:[1,0]\n"
"v_pk_mul_f32 v[156:157], v[156:157], v[30:31] op_sel_hi:[1,0]\n"
"v_pk_mul_f32 v[158:159], v[158:159], v[30:31] op_sel_hi:[1,0]\n"
"v_pk_mul_f32 v[160:161], v[160:161], v[30:31] op_sel_hi:[1,0]\n"
"buffer_load_dwordx4 v13, s[16:19], s53 offen offset:1024 nt lds\n"
"v_pk_mul_f32 v[162:163], v[162:163], v[30:31] op_sel_hi:[1,0]\n"
"v_pk_mul_f32 v[164:165], v[164:165], v[30:31] op_sel_hi:[1,0]\n"
"v_pk_mul_f32 v[166:167], v[166:167], v[30:31] op_sel_hi:[1,0]\n"
"v_accvgpr_read_b32 v27, a90\n"
"v_accvgpr_read_b32 v28, a94\n"
"v_accvgpr_read_b32 v25, a83\n"
"v_accvgpr_read_b32 v26, a87\n"
"v_cvt_pk_bf16_f32 v43, v27, v28\n"
"v_cvt_pk_bf16_f32 v46, v25, v26\n"
"v_accvgpr_read_b32 v27, a91\n"
"v_accvgpr_read_b32 v28, a95\n"
"v_accvgpr_read_b32 v25, a112\n"
"buffer_load_dwordx4 v12, s[16:19], s53 offen offset:2048 nt lds\n"
"v_mov_b32 v12, -1\n"
"v_accvgpr_read_b32 v26, a116\n"
"v_cvt_pk_bf16_f32 v47, v27, v28\n"
"v_cvt_pk_bf16_f32 v50, v25, v26\n"
"v_accvgpr_read_b32 v27, a120\n"
"v_accvgpr_read_b32 v28, a124\n"
"v_accvgpr_read_b32 v25, a113\n"
"v_accvgpr_read_b32 v26, a117\n"
"v_cvt_pk_bf16_f32 v51, v27, v28\n"
"v_cvt_pk_bf16_f32 v54, v25, v26\n"
"v_accvgpr_read_b32 v27, a121\n"
"v_accvgpr_read_b32 v28, a125\n"
"v_accvgpr_read_b32 v25, a114\n"
"v_accvgpr_read_b32 v26, a118\n"
"buffer_load_dwordx4 v13, s[16:19], s53 offen offset:3072 nt lds\n"
"s_add_u32 m0, s69, 0\n"
"v_mov_b32 v13, -1\n"
"v_cvt_pk_bf16_f32 v55, v27, v28\n"
"v_cvt_pk_bf16_f32 v58, v25, v26\n"
"v_accvgpr_read_b32 v27, a122\n"
"v_accvgpr_read_b32 v28, a126\n"
"v_accvgpr_read_b32 v25, a115\n"
"v_accvgpr_read_b32 v26, a119\n"
"v_cvt_pk_bf16_f32 v59, v27, v28\n"
"v_cvt_pk_bf16_f32 v62, v25, v26\n"
"v_accvgpr_read_b32 v27, a123\n"
"v_accvgpr_read_b32 v28, a127\n"
"v_accvgpr_read_b32 v25, a144\n"
"buffer_load_dwordx4 v8, s[24:27], s54 offen lds\n"
"v_accvgpr_read_b32 v26, a148\n"
"v_cvt_pk_bf16_f32 v63, v27, v28\n"
"v_accvgpr_read_b32 v27, a152\n"
"v_accvgpr_read_b32 v28, a156\n"
"v_cvt_pk_bf16_f32 v168, v128, v129\n"
"v_cvt_pk_bf16_f32 v169, v130, v131\n"
"v_mfma_f32_16x16x32_bf16 v[128:131], a[200:203], v[32:35], 0\n"
"v_cvt_pk_bf16_f32 v170, v132, v133\n"
"v_cvt_pk_bf16_f32 v171, v134, v135\n"
"v_mfma_f32_16x16x32_bf16 v[132:135], a[216:219], v[32:35], 0\n"
"v_cvt_pk_bf16_f32 v172, v136, v137\n"
"v_cvt_pk_bf16_f32 v173, v138, v139\n"
"v_mfma_f32_16x16x32_bf16 v[136:139], a[232:235], v[32:35], 0\n"
"v_cvt_pk_bf16_f32 v174, v140, v141\n"
"v_cvt_pk_bf16_f32 v175, v142, v143\n"
"v_mfma_f32_16x16x32_bf16 v[140:143], a[248:251], v[32:35], 0\n"
"v_cvt_pk_bf16_f32 v176, v144, v145\n"
"v_cvt_pk_bf16_f32 v177, v146, v147\n"
"v_mfma_f32_16x16x32_bf16 v[144:147], v[232:235], v[32:35], 0\n"
"v_cvt_pk_bf16_f32 v33, v23, v24\n"
"v_accvgpr_read_b32 v23, a137\n"
"v_mfma_f32_16x16x32_bf16 v[128:131], a[204:207], v[36:39], v[128:131]\n"
"v_accvgpr_read_b32 v24, a141\n"
"v_cvt_pk_bf16_f32 v32, v21, v22\n"
"v_mfma_f32_16x16x32_bf16 v[132:135], a[220:223], v[36:39], v[132:135]\n"
"v_accvgpr_read_b32 v21, a129\n"
"v_accvgpr_read_b32 v22, a133\n"
"v_mfma_f32_16x16x32_bf16 v[136:139], a[236:239], v[36:39], v[136:139]\n"
"v_cvt_pk_bf16_f32 v34, v25, v26\n"
"v_accvgpr_read_b32 v25, a145\n"
"v_mfma_f32_16x16x32_bf16 v[140:143], a[252:255], v[36:39], v[140:143]\n"
"v_accvgpr_read_b32 v26, a149\n"
"v_cvt_pk_bf16_f32 v35, v27, v28\n"
"v_mfma_f32_16x16x32_bf16 v[144:147], v[236:239], v[36:39], v[144:147]\n"
"v_cvt_pk_bf16_f32 v37, v23, v24\n"
"v_accvgpr_read_b32 v23, a138\n"
"v_mfma_f32_16x16x32_bf16 v[128:131], a[208:211], v[40:43], v[128:131]\n"
"v_accvgpr_read_b32 v24, a142\n"
"v_cvt_pk_bf16_f32 v36, v21, v22\n"
"v_mfma_f32_16x16x32_bf16 v[132:135], a[224:227], v[40:43], v[132:135]\n"
"v_accvgpr_read_b32 v21, a130\n"
"v_accvgpr_read_b32 v22, a134\n"
"v_mfma_f32_16x16x32_bf16 v[136:139], a[240:243], v[40:43], v[136:139]\n"
"v_cvt_pk_bf16_f32 v38, v25, v26\n"
"v_accvgpr_read_b32 v27, a153\n"
"v_mfma_f32_16x16x32_bf16 v[140:143], v[224:227], v[40:43], v[140:143]\n"
"v_accvgpr_read_b32 v28, a157\n"
"v_accvgpr_read_b32 v25, a146\n"
"v_mfma_f32_16x16x32_bf16 v[144:147], v[240:243], v[40:43], v[144:147]\n"
"v_cvt_pk_bf16_f32 v41, v23, v24\n"
"v_accvgpr_read_b32 v23, a139\n"
"v_mfma_f32_16x16x32_bf16 v[128:131], a[212:215], v[44:47], v[128:131]\n"
"v_accvgpr_read_b32 v24, a143\n"
"v_cvt_pk_bf16_f32 v40, v21, v22\n"
"v_mfma_f32_16x16x32_bf16 v[132:135], a[228:231], v[44:47], v[132:135]\n"
"v_accvgpr_read_b32 v21, a131\n"
"v_accvgpr_read_b32 v22, a135\n"
"v_mfma_f32_16x16x32_bf16 v[136:139], a[244:247], v[44:47], v[136:139]\n"
"v_accvgpr_read_b32 v26, a150\n"
"v_cvt_pk_bf16_f32 v39, v27, v28\n"
"v_mfma_f32_16x16x32_bf16 v[140:143], v[228:231], v[44:47], v[140:143]\n"
"v_max3_f32 v19, v128, v129, v130\n"
"v_cvt_pk_bf16_f32 v42, v25, v26\n"
"v_mfma_f32_16x16x32_bf16 v[144:147], v[244:247], v[44:47], v[144:147]\n"
"v_max3_f32 v20, v131, v132, v133\n"
"v_max3_f32 v19, v19, v134, v135\n"
"v_max3_f32 v20, v20, v136, v137\n"
"v_max3_f32 v19, v19, v138, v139\n"
"v_cvt_pk_bf16_f32 v45, v23, v24\n"
"v_max3_f32 v20, v20, v140, v141\n"
"v_max3_f32 v19, v19, v142, v143\n"
"v_add_u32 v24, s75, v0\n"
"v_max3_f32 v20, v20, v144, v145\n"
"v_max3_f32 v19, v19, v146, v147\n"
"v_max_f32 v19, v19, v20\n"
"v_mov_b32 v20, v19\n"
"v_cvt_pk_bf16_f32 v44, v21, v22\n"
"v_xor_b32 v21, 4096, v24\n"
"v_permlane16_swap_b32 v19, v20\n"
"v_max_f32 v19, v19, v20\n"
"v_mov_b32 v20, v19\n"
"v_xor_b32 v22, 8192, v24\n"
"v_xor_b32 v23, 12288, v24\n"
"v_permlane32_swap_b32 v19, v20\n"
"v_max_f32 v19, v19, v20\n"
"v_mul_f32 v20, s74, v19\n"
"v_pk_fma_f32 v[128:129], v[128:129], s[74:75], v[20:21] op_sel_hi:[1,0,0] neg_lo:[0,0,1] neg_hi:[0,0,1]\n"
"v_pk_fma_f32 v[130:131], v[130:131], s[74:75], v[20:21] op_sel_hi:[1,0,0] neg_lo:[0,0,1] neg_hi:[0,0,1]\n"
"v_pk_fma_f32 v[132:133], v[132:133], s[74:75], v[20:21] op_sel_hi:[1,0,0] neg_lo:[0,0,1] neg_hi:[0,0,1]\n"
"v_pk_fma_f32 v[134:135], v[134:135], s[74:75], v[20:21] op_sel_hi:[1,0,0] neg_lo:[0,0,1] neg_hi:[0,0,1]\n"
"v_pk_fma_f32 v[136:137], v[136:137], s[74:75], v[20:21] op_sel_hi:[1,0,0] neg_lo:[0,0,1] neg_hi:[0,0,1]\n"
"v_pk_fma_f32 v[138:139], v[138:139], s[74:75], v[20:21] op_sel_hi:[1,0,0] neg_lo:[0,0,1] neg_hi:[0,0,1]\n"
"v_pk_fma_f32 v[140:141], v[140:141], s[74:75], v[20:21] op_sel_hi:[1,0,0] neg_lo:[0,0,1] neg_hi:[0,0,1]\n"
"v_pk_fma_f32 v[142:143], v[142:143], s[74:75], v[20:21] op_sel_hi:[1,0,0] neg_lo:[0,0,1] neg_hi:[0,0,1]\n"
"v_pk_fma_f32 v[144:145], v[144:145], s[74:75], v[20:21] op_sel_hi:[1,0,0] neg_lo:[0,0,1] neg_hi:[0,0,1]\n"
"v_pk_fma_f32 v[146:147], v[146:147], s[74:75], v[20:21] op_sel_hi:[1,0,0] neg_lo:[0,0,1] neg_hi:[0,0,1]\n"
"v_exp_f32 v128, v128\n"
"v_exp_f32 v129, v129\n"
"v_exp_f32 v130, v130\n"
"v_exp_f32 v131, v131\n"
"v_exp_f32 v132, v132\n"
"v_exp_f32 v133, v133\n"
"v_exp_f32 v134, v134\n"
"v_exp_f32 v135, v135\n"
"v_exp_f32 v136, v136\n"
"v_exp_f32 v137, v137\n"
"v_exp_f32 v138, v138\n"
"v_exp_f32 v139, v139\n"
"v_exp_f32 v140, v140\n"
"v_exp_f32 v141, v141\n"
"v_exp_f32 v142, v142\n"
"v_exp_f32 v143, v143\n"
"v_exp_f32 v144, v144\n"
"v_exp_f32 v145, v145\n"
"v_exp_f32 v146, v146\n"
"v_exp_f32 v147, v147\n"
"v_pk_add_f32 v[96:97], v[128:129], v[130:131]\n"
"v_pk_add_f32 v[98:99], v[132:133], v[134:135]\n"
"v_pk_add_f32 v[96:97], v[96:97], v[136:137]\n"
"v_pk_add_f32 v[98:99], v[98:99], v[138:139]\n"
"v_pk_add_f32 v[96:97], v[96:97], v[140:141]\n"
"v_pk_add_f32 v[98:99], v[98:99], v[142:143]\n"
"v_pk_add_f32 v[96:97], v[96:97], v[144:145]\n"
"v_pk_add_f32 v[98:99], v[98:99], v[146:147]\n"
"v_pk_add_f32 v[96:97], v[96:97], v[98:99]\n"
"v_add_f32 v30, v96, v97\n"
"v_mov_b32 v29, v30\n"
"v_accvgpr_read_b32 v27, a154\n"
"v_accvgpr_read_b32 v28, a158\n"
"v_permlane16_swap_b32 v30, v29\n"
"v_add_f32 v30, v30, v29\n"
"v_mov_b32 v29, v30\n"
"v_accvgpr_read_b32 v25, a147\n"
"v_cvt_pk_bf16_f32 v43, v27, v28\n"
"v_permlane32_swap_b32 v30, v29\n"
"v_add_f32 v30, v30, v29\n"
"v_rcp_f32 v30, v30\n"
"v_accvgpr_read_b32 v26, a151\n"
"v_pk_mul_f32 v[128:129], v[128:129], v[30:31] op_sel_hi:[1,0]\n"
"v_pk_mul_f32 v[130:131], v[130:131], v[30:31] op_sel_hi:[1,0]\n"
"v_pk_mul_f32 v[132:133], v[132:133], v[30:31] op_sel_hi:[1,0]\n"
"v_pk_mul_f32 v[134:135], v[134:135], v[30:31] op_sel_hi:[1,0]\n"
"v_pk_mul_f32 v[136:137], v[136:137], v[30:31] op_sel_hi:[1,0]\n"
"v_pk_mul_f32 v[138:139], v[138:139], v[30:31] op_sel_hi:[1,0]\n"
"v_pk_mul_f32 v[140:141], v[140:141], v[30:31] op_sel_hi:[1,0]\n"
"v_pk_mul_f32 v[142:143], v[142:143], v[30:31] op_sel_hi:[1,0]\n"
"v_pk_mul_f32 v[144:145], v[144:145], v[30:31] op_sel_hi:[1,0]\n"
"v_pk_mul_f32 v[146:147], v[146:147], v[30:31] op_sel_hi:[1,0]\n"
"v_accvgpr_read_b32 v27, a155\n"
"v_accvgpr_read_b32 v28, a159\n"
"v_cvt_pk_bf16_f32 v178, v148, v149\n"
"v_cvt_pk_bf16_f32 v179, v150, v151\n"
"v_mfma_f32_16x16x32_bf16 v[148:151], a[200:203], v[48:51], 0\n"
"v_cvt_pk_bf16_f32 v180, v152, v153\n"
"v_cvt_pk_bf16_f32 v181, v154, v155\n"
"v_mfma_f32_16x16x32_bf16 v[152:155], a[216:219], v[48:51], 0\n"
"v_cvt_pk_bf16_f32 v182, v156, v157\n"
"v_cvt_pk_bf16_f32 v183, v158, v159\n"
"v_mfma_f32_16x16x32_bf16 v[156:159], a[232:235], v[48:51], 0\n"
"v_cvt_pk_bf16_f32 v104, v144, v145\n"
"v_cvt_pk_bf16_f32 v105, v146, v147\n"
"v_cvt_pk_bf16_f32 v96, v128, v129\n"
"v_cvt_pk_bf16_f32 v97, v130, v131\n"
"v_cvt_pk_bf16_f32 v98, v132, v133\n"
"v_cvt_pk_bf16_f32 v99, v134, v135\n"
"v_cvt_pk_bf16_f32 v100, v136, v137\n"
"v_cvt_pk_bf16_f32 v101, v138, v139\n"
"v_cvt_pk_bf16_f32 v102, v140, v141\n"
"v_cvt_pk_bf16_f32 v103, v142, v143\n"
"v_cvt_pk_bf16_f32 v184, v160, v161\n"
"v_cvt_pk_bf16_f32 v185, v162, v163\n"
"v_mfma_f32_16x16x32_bf16 v[160:163], a[248:251], v[48:51], 0\n"
"v_cvt_pk_bf16_f32 v186, v164, v165\n"
"v_cvt_pk_bf16_f32 v187, v166, v167\n"
"v_mfma_f32_16x16x32_bf16 v[164:167], v[232:235], v[48:51], 0\n"
"v_cvt_pk_bf16_f32 v46, v25, v26\n"
"v_cvt_pk_bf16_f32 v47, v27, v28\n"
"v_mfma_f32_16x16x32_bf16 v[148:151], a[204:207], v[52:55], v[148:151]\n"
"v_mfma_f32_16x16x32_bf16 v[152:155], a[220:223], v[52:55], v[152:155]\n"
"v_mfma_f32_16x16x32_bf16 v[156:159], a[236:239], v[52:55], v[156:159]\n"
"v_mfma_f32_16x16x32_bf16 v[160:163], a[252:255], v[52:55], v[160:163]\n"
"v_mfma_f32_16x16x32_bf16 v[164:167], v[236:239], v[52:55], v[164:167]\n"
"v_mfma_f32_16x16x32_bf16 v[148:151], a[208:211], v[56:59], v[148:151]\n"
"v_mfma_f32_16x16x32_bf16 v[152:155], a[224:227], v[56:59], v[152:155]\n"
"v_mfma_f32_16x16x32_bf16 v[156:159], a[240:243], v[56:59], v[156:159]\n"
"v_mfma_f32_16x16x32_bf16 v[160:163], v[224:227], v[56:59], v[160:163]\n"
"v_mfma_f32_16x16x32_bf16 v[164:167], v[240:243], v[56:59], v[164:167]\n"
"v_mfma_f32_16x16x32_bf16 v[148:151], a[212:215], v[60:63], v[148:151]\n"
"v_mfma_f32_16x16x32_bf16 v[152:155], a[228:231], v[60:63], v[152:155]\n"
"v_mfma_f32_16x16x32_bf16 v[156:159], a[244:247], v[60:63], v[156:159]\n"
"v_mfma_f32_16x16x32_bf16 v[160:163], v[228:231], v[60:63], v[160:163]\n"
"v_mfma_f32_16x16x32_bf16 v[164:167], v[244:247], v[60:63], v[164:167]\n"
"v_mfma_f32_16x16x32_bf16 v[128:131], a[200:203], v[32:35], 0\n"
"v_mfma_f32_16x16x32_bf16 v[132:135], a[216:219], v[32:35], 0\n"
"v_mfma_f32_16x16x32_bf16 v[136:139], a[232:235], v[32:35], 0\n"
"v_mfma_f32_16x16x32_bf16 v[140:143], a[248:251], v[32:35], 0\n"
"v_max3_f32 v19, v148, v149, v150\n"
"v_max3_f32 v20, v151, v152, v153\n"
"v_mfma_f32_16x16x32_bf16 v[144:147], v[232:235], v[32:35], 0\n"
"v_max3_f32 v19, v19, v154, v155\n"
"v_max3_f32 v20, v20, v156, v157\n"
"v_mfma_f32_16x16x32_bf16 v[128:131], a[204:207], v[36:39], v[128:131]\n"
"v_max3_f32 v19, v19, v158, v159\n"
"v_max3_f32 v20, v20, v160, v161\n"
"v_mfma_f32_16x16x32_bf16 v[132:135], a[220:223], v[36:39], v[132:135]\n"
"v_max3_f32 v19, v19, v162, v163\n"
"v_max3_f32 v20, v20, v164, v165\n"
"v_mfma_f32_16x16x32_bf16 v[136:139], a[236:239], v[36:39], v[136:139]\n"
"v_max3_f32 v19, v19, v166, v167\n"
"v_max_f32 v19, v19, v20\n"
"v_mfma_f32_16x16x32_bf16 v[140:143], a[252:255], v[36:39], v[140:143]\n"
"v_mov_b32 v20, v19\n"
"v_mfma_f32_16x16x32_bf16 v[144:147], v[236:239], v[36:39], v[144:147]\n"
"v_mfma_f32_16x16x32_bf16 v[128:131], a[208:211], v[40:43], v[128:131]\n"
"v_permlane16_swap_b32 v19, v20\n"
"v_mfma_f32_16x16x32_bf16 v[132:135], a[224:227], v[40:43], v[132:135]\n"
"v_max_f32 v19, v19, v20\n"
"v_mov_b32 v20, v19\n"
"v_mfma_f32_16x16x32_bf16 v[136:139], a[240:243], v[40:43], v[136:139]\n"
"v_mfma_f32_16x16x32_bf16 v[140:143], v[224:227], v[40:43], v[140:143]\n"
"v_permlane32_swap_b32 v19, v20\n"
"v_mfma_f32_16x16x32_bf16 v[144:147], v[240:243], v[40:43], v[144:147]\n"
"v_max_f32 v19, v19, v20\n"
"v_mul_f32 v20, s74, v19\n"
"v_mfma_f32_16x16x32_bf16 v[128:131], a[212:215], v[44:47], v[128:131]\n"
"buffer_load_dwordx4 v9, s[24:27], s54 offen offset:1024 lds\n"
"v_mfma_f32_16x16x32_bf16 v[132:135], a[228:231], v[44:47], v[132:135]\n"
"v_pk_fma_f32 v[148:149], v[148:149], s[74:75], v[20:21] op_sel_hi:[1,0,0] neg_lo:[0,0,1] neg_hi:[0,0,1]\n"
"v_pk_fma_f32 v[150:151], v[150:151], s[74:75], v[20:21] op_sel_hi:[1,0,0] neg_lo:[0,0,1] neg_hi:[0,0,1]\n"
"v_mfma_f32_16x16x32_bf16 v[136:139], a[244:247], v[44:47], v[136:139]\n"
"v_pk_fma_f32 v[152:153], v[152:153], s[74:75], v[20:21] op_sel_hi:[1,0,0] neg_lo:[0,0,1] neg_hi:[0,0,1]\n"
"v_pk_fma_f32 v[154:155], v[154:155], s[74:75], v[20:21] op_sel_hi:[1,0,0] neg_lo:[0,0,1] neg_hi:[0,0,1]\n"
"v_mfma_f32_16x16x32_bf16 v[140:143], v[228:231], v[44:47], v[140:143]\n"
"v_pk_fma_f32 v[156:157], v[156:157], s[74:75], v[20:21] op_sel_hi:[1,0,0] neg_lo:[0,0,1] neg_hi:[0,0,1]\n"
"v_pk_fma_f32 v[158:159], v[158:159], s[74:75], v[20:21] op_sel_hi:[1,0,0] neg_lo:[0,0,1] neg_hi:[0,0,1]\n"
"v_mfma_f32_16x16x32_bf16 v[144:147], v[244:247], v[44:47], v[144:147]\n"
"buffer_load_dwordx4 v10, s[24:27], s54 offen offset:2048 lds\n"
"v_pk_fma_f32 v[160:161], v[160:161], s[74:75], v[20:21] op_sel_hi:[1,0,0] neg_lo:[0,0,1] neg_hi:[0,0,1]\n"
"v_pk_fma_f32 v[162:163], v[162:163], s[74:75], v[20:21] op_sel_hi:[1,0,0] neg_lo:[0,0,1] neg_hi:[0,0,1]\n"
"v_pk_fma_f32 v[164:165], v[164:165], s[74:75], v[20:21] op_sel_hi:[1,0,0] neg_lo:[0,0,1] neg_hi:[0,0,1]\n"
"v_pk_fma_f32 v[166:167], v[166:167], s[74:75], v[20:21] op_sel_hi:[1,0,0] neg_lo:[0,0,1] neg_hi:[0,0,1]\n"
"v_max3_f32 v19, v128, v129, v130\n"
"v_max3_f32 v20, v131, v132, v133\n"
"v_max3_f32 v19, v19, v134, v135\n"
"v_max3_f32 v20, v20, v136, v137\n"
"v_max3_f32 v19, v19, v138, v139\n"
"v_exp_f32 v148, v148\n"
"v_exp_f32 v149, v149\n"
"v_exp_f32 v150, v150\n"
"buffer_load_dwordx4 v11, s[24:27], s54 offen offset:3072 lds\n"
"s_add_u32 m0, s69, 16384\n"
"v_exp_f32 v151, v151\n"
"v_exp_f32 v152, v152\n"
"v_exp_f32 v153, v153\n"
"v_exp_f32 v154, v154\n"
"v_exp_f32 v155, v155\n"
"buffer_load_dwordx4 v8, s[24:27], s72 offen lds\n"
"v_max3_f32 v20, v20, v140, v141\n"
"v_max3_f32 v19, v19, v142, v143\n"
"v_exp_f32 v156, v156\n"
"v_exp_f32 v157, v157\n"
"v_exp_f32 v158, v158\n"
"v_exp_f32 v159, v159\n"
"v_max3_f32 v20, v20, v144, v145\n"
"v_max3_f32 v19, v19, v146, v147\n"
"v_exp_f32 v160, v160\n"
"buffer_load_dwordx4 v9, s[24:27], s72 offen offset:1024 lds\n"
"v_exp_f32 v161, v161\n"
"v_exp_f32 v162, v162\n"
"v_exp_f32 v163, v163\n"
"v_max_f32 v19, v19, v20\n"
"v_exp_f32 v164, v164\n"
"v_exp_f32 v165, v165\n"
"v_exp_f32 v166, v166\n"
"buffer_load_dwordx4 v10, s[24:27], s72 offen offset:2048 lds\n"
"v_exp_f32 v167, v167\n"
"v_pk_add_f32 v[106:107], v[148:149], v[150:151]\n"
"v_pk_add_f32 v[108:109], v[152:153], v[154:155]\n"
"v_mov_b32 v20, v19\n"
"v_pk_add_f32 v[106:107], v[106:107], v[156:157]\n"
"v_pk_add_f32 v[108:109], v[108:109], v[158:159]\n"
"v_pk_add_f32 v[106:107], v[106:107], v[160:161]\n"
"v_pk_add_f32 v[108:109], v[108:109], v[162:163]\n"
"v_pk_add_f32 v[106:107], v[106:107], v[164:165]\n"
"v_pk_add_f32 v[108:109], v[108:109], v[166:167]\n"
"v_permlane16_swap_b32 v19, v20\n"
"buffer_load_dwordx4 v11, s[24:27], s72 offen offset:3072 lds\n"
"s_add_u32 m0, s69, 32768\n"
"s_add_u32 s72, s54, 65536\n"
"v_pk_add_f32 v[106:107], v[106:107], v[108:109]\n"
"v_add_f32 v30, v106, v107\n"
"v_max_f32 v19, v19, v20\n"
"v_mov_b32 v29, v30\n"
"v_mov_b32 v20, v19\n"
"s_nop 0\n"
"v_permlane16_swap_b32 v30, v29\n"
"v_add_f32 v30, v30, v29\n"
"buffer_load_dwordx4 v8, s[24:27], s72 offen lds\n"
"v_permlane32_swap_b32 v19, v20\n"
"v_max_f32 v19, v19, v20\n"
"v_mov_b32 v29, v30\n"
"v_mul_f32 v20, s74, v19\n"
"v_pk_fma_f32 v[128:129], v[128:129], s[74:75], v[20:21] op_sel_hi:[1,0,0] neg_lo:[0,0,1] neg_hi:[0,0,1]\n"
"v_pk_fma_f32 v[130:131], v[130:131], s[74:75], v[20:21] op_sel_hi:[1,0,0] neg_lo:[0,0,1] neg_hi:[0,0,1]\n"
"v_pk_fma_f32 v[132:133], v[132:133], s[74:75], v[20:21] op_sel_hi:[1,0,0] neg_lo:[0,0,1] neg_hi:[0,0,1]\n"
"v_pk_fma_f32 v[134:135], v[134:135], s[74:75], v[20:21] op_sel_hi:[1,0,0] neg_lo:[0,0,1] neg_hi:[0,0,1]\n"
"v_exp_f32 v128, v128\n"
"v_exp_f32 v129, v129\n"
"buffer_load_dwordx4 v9, s[24:27], s72 offen offset:1024 lds\n"
"v_exp_f32 v130, v130\n"
"v_exp_f32 v131, v131\n"
"v_exp_f32 v132, v132\n"
"v_exp_f32 v133, v133\n"
"v_exp_f32 v134, v134\n"
"v_exp_f32 v135, v135\n"
"buffer_load_dwordx4 v10, s[24:27], s72 offen offset:2048 lds\n"
"v_pk_fma_f32 v[136:137], v[136:137], s[74:75], v[20:21] op_sel_hi:[1,0,0] neg_lo:[0,0,1] neg_hi:[0,0,1]\n"
"v_pk_fma_f32 v[138:139], v[138:139], s[74:75], v[20:21] op_sel_hi:[1,0,0] neg_lo:[0,0,1] neg_hi:[0,0,1]\n"
"v_permlane32_swap_b32 v30, v29\n"
"v_exp_f32 v136, v136\n"
"v_exp_f32 v137, v137\n"
"v_exp_f32 v138, v138\n"
"v_exp_f32 v139, v139\n"
"v_pk_fma_f32 v[140:141], v[140:141], s[74:75], v[20:21] op_sel_hi:[1,0,0] neg_lo:[0,0,1] neg_hi:[0,0,1]\n"
"buffer_load_dwordx4 v11, s[24:27], s72 offen offset:3072 lds\n"
"s_add_u32 m0, s69, 49152\n"
"s_add_u32 s72, s54, 98304\n"
"v_pk_fma_f32 v[142:143], v[142:143], s[74:75], v[20:21] op_sel_hi:[1,0,0] neg_lo:[0,0,1] neg_hi:[0,0,1]\n"
"v_exp_f32 v140, v140\n"
"v_exp_f32 v141, v141\n"
"v_exp_f32 v142, v142\n"
"v_exp_f32 v143, v143\n"
"v_pk_fma_f32 v[144:145], v[144:145], s[74:75], v[20:21] op_sel_hi:[1,0,0] neg_lo:[0,0,1] neg_hi:[0,0,1]\n"
"buffer_load_dwordx4 v8, s[24:27], s72 offen lds\n"
"v_pk_fma_f32 v[146:147], v[146:147], s[74:75], v[20:21] op_sel_hi:[1,0,0] neg_lo:[0,0,1] neg_hi:[0,0,1]\n"
"v_add_f32 v30, v30, v29\n"
"v_exp_f32 v144, v144\n"
"v_exp_f32 v145, v145\n"
"v_exp_f32 v146, v146\n"
"v_exp_f32 v147, v147\n"
"v_pk_add_f32 v[116:117], v[128:129], v[130:131]\n"
"v_pk_add_f32 v[118:119], v[132:133], v[134:135]\n"
"v_rcp_f32 v30, v30\n"
"buffer_load_dwordx4 v9, s[24:27], s72 offen offset:1024 lds\n"
"v_pk_add_f32 v[116:117], v[116:117], v[136:137]\n"
"v_pk_add_f32 v[118:119], v[118:119], v[138:139]\n"
"v_pk_add_f32 v[116:117], v[116:117], v[140:141]\n"
"v_pk_add_f32 v[118:119], v[118:119], v[142:143]\n"
"v_pk_add_f32 v[116:117], v[116:117], v[144:145]\n"
"v_pk_add_f32 v[118:119], v[118:119], v[146:147]\n"
"v_pk_mul_f32 v[148:149], v[148:149], v[30:31] op_sel_hi:[1,0]\n"
"v_pk_mul_f32 v[150:151], v[150:151], v[30:31] op_sel_hi:[1,0]\n"
"v_pk_mul_f32 v[152:153], v[152:153], v[30:31] op_sel_hi:[1,0]\n"
"v_pk_mul_f32 v[154:155], v[154:155], v[30:31] op_sel_hi:[1,0]\n"
"v_pk_mul_f32 v[156:157], v[156:157], v[30:31] op_sel_hi:[1,0]\n"
"buffer_load_dwordx4 v10, s[24:27], s72 offen offset:2048 lds\n"
"v_pk_mul_f32 v[158:159], v[158:159], v[30:31] op_sel_hi:[1,0]\n"
"v_pk_mul_f32 v[160:161], v[160:161], v[30:31] op_sel_hi:[1,0]\n"
"v_pk_mul_f32 v[162:163], v[162:163], v[30:31] op_sel_hi:[1,0]\n"
"v_pk_mul_f32 v[164:165], v[164:165], v[30:31] op_sel_hi:[1,0]\n"
"v_pk_mul_f32 v[166:167], v[166:167], v[30:31] op_sel_hi:[1,0]\n"
"v_pk_add_f32 v[116:117], v[116:117], v[118:119]\n"
"v_add_f32 v30, v116, v117\n"
"v_mov_b32 v29, v30\n"
"v_cvt_pk_bf16_f32 v106, v148, v149\n"
"v_cvt_pk_bf16_f32 v107, v150, v151\n"
"v_permlane16_swap_b32 v30, v29\n"
"v_add_f32 v30, v30, v29\n"
"buffer_load_dwordx4 v11, s[24:27], s72 offen offset:3072 lds\n"
"s_add_u32 m0, s69, 65536\n"
"v_mov_b32 v29, v30\n"
"s_add_u32 s72, s55, 32768\n"
"v_cvt_pk_bf16_f32 v108, v152, v153\n"
"v_permlane32_swap_b32 v30, v29\n"
"v_add_f32 v30, v30, v29\n"
"v_rcp_f32 v30, v30\n"
"v_cvt_pk_bf16_f32 v109, v154, v155\n"
"v_pk_mul_f32 v[146:147], v[146:147], v[30:31] op_sel_hi:[1,0]\n"
"v_cvt_pk_bf16_f32 v125, v146, v147\n"
"buffer_load_dwordx4 v8, s[24:27], s55 offen lds\n"
"s_waitcnt vmcnt(33)\n"
"ds_read_b64_tr_b16 a[0:1], v6 offset:0\n"
"s_waitcnt vmcnt(29)\n"
"ds_read_b64_tr_b16 a[2:3], v6 offset:16384\n"
"s_waitcnt vmcnt(25)\n"
"ds_read_b64_tr_b16 a[4:5], v6 offset:32768\n"
"s_waitcnt vmcnt(21)\n"
"ds_read_b64_tr_b16 a[6:7], v6 offset:49152\n"
"s_waitcnt vmcnt(17)\n"
"ds_read_b64_tr_b16 a[184:185], v7 offset:49152\n"
"v_xor_b32 v6, 32, v6\n"
"ds_read_b64_tr_b16 a[8:9], v6 offset:0\n"
"ds_read_b64_tr_b16 a[10:11], v6 offset:16384\n"
"ds_read_b64_tr_b16 a[12:13], v6 offset:32768\n"
"ds_read_b64_tr_b16 a[14:15], v6 offset:49152\n"
"v_add_u32 v7, 16384, v6\n"
"ds_read_b64_tr_b16 a[186:187], v7 offset:49152\n"
"v_xor_b32 v6, 96, v6\n"
"buffer_load_dwordx4 v9, s[24:27], s55 offen offset:1024 lds\n"
"ds_read_b64_tr_b16 a[16:17], v6 offset:0\n"
"ds_read_b64_tr_b16 a[18:19], v6 offset:16384\n"
"ds_read_b64_tr_b16 a[20:21], v6 offset:32768\n"
"ds_read_b64_tr_b16 a[22:23], v6 offset:49152\n"
"v_add_u32 v7, 16384, v6\n"
"ds_read_b64_tr_b16 a[188:189], v7 offset:49152\n"
"v_xor_b32 v6, 32, v6\n"
"ds_read_b64_tr_b16 a[24:25], v6 offset:0\n"
"ds_read_b64_tr_b16 a[26:27], v6 offset:16384\n"
"ds_read_b64_tr_b16 a[28:29], v6 offset:32768\n"
"ds_read_b64_tr_b16 a[30:31], v6 offset:49152\n"
"v_add_u32 v7, 16384, v6\n"
"buffer_load_dwordx4 v10, s[24:27], s55 offen offset:2048 lds\n"
"ds_read_b64_tr_b16 a[190:191], v7 offset:49152\n"
"v_xor_b32 v6, 224, v6\n"
"ds_read_b64_tr_b16 a[32:33], v6 offset:0\n"
"s_waitcnt lgkmcnt(15)\n"
"v_mfma_f32_16x16x32_bf16 v[32:35], a[0:3], v[168:171], 0\n"
"ds_read_b64_tr_b16 a[34:35], v6 offset:16384\n"
"ds_read_b64_tr_b16 a[36:37], v6 offset:32768\n"
"ds_read_b64_tr_b16 a[38:39], v6 offset:49152\n"
"v_add_u32 v7, 16384, v6\n"
"s_waitcnt lgkmcnt(15)\n"
"v_mfma_f32_16x16x32_bf16 v[36:39], a[8:11], v[168:171], 0\n"
"ds_read_b64_tr_b16 a[192:193], v7 offset:49152\n"
"v_xor_b32 v6, 32, v6\n"
"v_mfma_f32_16x16x32_bf16 v[32:35], a[4:7], v[172:175], v[32:35]\n"
"buffer_load_dwordx4 v11, s[24:27], s55 offen offset:3072 lds\n"
"v_mfma_f32_16x16x32_bf16 v[36:39], a[12:15], v[172:175], v[36:39]\n"
"s_add_u32 m0, s69, 131072\n"
"ds_read_b64_tr_b16 a[40:41], v6 offset:0\n"
"ds_read_b64_tr_b16 a[42:43], v6 offset:16384\n"
"v_mfma_f32_16x16x16_bf16 v[32:35], a[184:185], v[176:177], v[32:35]\n"
"s_waitcnt lgkmcnt(13)\n"
"ds_read_b64_tr_b16 a[44:45], v6 offset:32768\n"
"ds_read_b64_tr_b16 a[46:47], v6 offset:49152\n"
"v_mfma_f32_16x16x16_bf16 v[36:39], a[186:187], v[176:177], v[36:39]\n"
"v_add_u32 v7, 16384, v6\n"
"ds_read_b64_tr_b16 a[194:195], v7 offset:49152\n"
"v_mfma_f32_16x16x32_bf16 v[40:43], a[16:19], v[168:171], 0\n"
"s_waitcnt lgkmcnt(13)\n"
"v_xor_b32 v6, 96, v6\n"
"ds_read_b64_tr_b16 a[48:49], v6 offset:0\n"
"v_mfma_f32_16x16x32_bf16 v[44:47], a[24:27], v[168:171], 0\n"
"ds_read_b64_tr_b16 a[50:51], v6 offset:16384\n"
"ds_read_b64_tr_b16 a[52:53], v6 offset:32768\n"
"v_mfma_f32_16x16x32_bf16 v[40:43], a[20:23], v[172:175], v[40:43]\n"
"s_waitcnt lgkmcnt(14)\n"
"ds_read_b64_tr_b16 a[54:55], v6 offset:49152\n"
"v_add_u32 v7, 16384, v6\n"
"v_mfma_f32_16x16x32_bf16 v[44:47], a[28:31], v[172:175], v[44:47]\n"
"ds_read_b64_tr_b16 a[196:197], v7 offset:49152\n"
"v_xor_b32 v6, 32, v6\n"
"v_mfma_f32_16x16x16_bf16 v[40:43], a[188:189], v[176:177], v[40:43]\n"
"s_waitcnt lgkmcnt(13)\n"
"buffer_store_dwordx4 v[12:15], v0, s[44:47], s52 offen offset:512 sc1\n"
"v_mfma_f32_16x16x16_bf16 v[44:47], a[190:191], v[176:177], v[44:47]\n"
"ds_read_b64_tr_b16 a[56:57], v6 offset:0\n"
"ds_read_b64_tr_b16 a[58:59], v6 offset:16384\n"
"v_mfma_f32_16x16x32_bf16 v[48:51], a[32:35], v[168:171], 0\n"
"s_waitcnt lgkmcnt(10)\n"
"ds_read_b64_tr_b16 a[60:61], v6 offset:32768\n"
"ds_read_b64_tr_b16 a[62:63], v6 offset:49152\n"
"v_mfma_f32_16x16x32_bf16 v[52:55], a[40:43], v[168:171], 0\n"
"v_add_u32 v7, 16384, v6\n"
"ds_read_b64_tr_b16 a[198:199], v7 offset:49152\n"
"v_mfma_f32_16x16x32_bf16 v[48:51], a[36:39], v[172:175], v[48:51]\n"
"s_waitcnt lgkmcnt(11)\n"
"v_cvt_pk_bf16_f32 v192, v32, v33\n"
"v_cvt_pk_bf16_f32 v193, v34, v35\n"
"v_mfma_f32_16x16x32_bf16 v[52:55], a[44:47], v[172:175], v[52:55]\n"
"v_cvt_pk_bf16_f32 v194, v36, v37\n"
"v_cvt_pk_bf16_f32 v195, v38, v39\n"
"v_mfma_f32_16x16x16_bf16 v[48:51], a[192:193], v[176:177], v[48:51]\n"
"s_waitcnt lgkmcnt(9)\n"
"buffer_store_dwordx4 v[12:15], v0, s[44:47], s52 offen offset:1536 sc1\n"
"v_mfma_f32_16x16x16_bf16 v[52:55], a[194:195], v[176:177], v[52:55]\n"
"s_waitcnt lgkmcnt(6)\n"
"ds_write_b128 v24, v[192:195]\n"
"v_mfma_f32_16x16x32_bf16 v[56:59], a[48:51], v[168:171], 0\n"
"s_waitcnt lgkmcnt(4)\n"
"v_cvt_pk_bf16_f32 v196, v40, v41\n"
"v_cvt_pk_bf16_f32 v197, v42, v43\n"
"v_mfma_f32_16x16x32_bf16 v[60:63], a[56:59], v[168:171], 0\n"
"v_cvt_pk_bf16_f32 v198, v44, v45\n"
"v_cvt_pk_bf16_f32 v199, v46, v47\n"
"v_mfma_f32_16x16x32_bf16 v[56:59], a[52:55], v[172:175], v[56:59]\n"
"s_waitcnt lgkmcnt(2)\n"
"ds_write_b128 v24, v[196:199] offset:1024\n"
"v_mfma_f32_16x16x32_bf16 v[60:63], a[60:63], v[172:175], v[60:63]\n"
"v_cvt_pk_bf16_f32 v200, v48, v49\n"
"v_cvt_pk_bf16_f32 v201, v50, v51\n"
"v_mfma_f32_16x16x16_bf16 v[56:59], a[196:197], v[176:177], v[56:59]\n"
"s_waitcnt lgkmcnt(2)\n"
"buffer_store_dwordx4 v[12:15], v0, s[44:47], s52 offen offset:2560 sc1\n"
"v_mfma_f32_16x16x16_bf16 v[60:63], a[198:199], v[176:177], v[60:63]\n"
"v_cvt_pk_bf16_f32 v202, v52, v53\n"
"v_cvt_pk_bf16_f32 v203, v54, v55\n"
"v_mfma_f32_16x16x32_bf16 v[32:35], a[0:3], v[178:181], 0\n"
"ds_write_b128 v24, v[200:203] offset:2048\n"
"v_mfma_f32_16x16x32_bf16 v[36:39], a[8:11], v[178:181], 0\n"
"v_cvt_pk_bf16_f32 v110, v156, v157\n"
"v_cvt_pk_bf16_f32 v204, v56, v57\n"
"v_mfma_f32_16x16x32_bf16 v[32:35], a[4:7], v[182:185], v[32:35]\n"
"v_cvt_pk_bf16_f32 v205, v58, v59\n"
"v_cvt_pk_bf16_f32 v206, v60, v61\n"
"v_mfma_f32_16x16x32_bf16 v[36:39], a[12:15], v[182:185], v[36:39]\n"
"v_cvt_pk_bf16_f32 v207, v62, v63\n"
"v_cvt_pk_bf16_f32 v111, v158, v159\n"
"v_mfma_f32_16x16x16_bf16 v[32:35], a[184:185], v[186:187], v[32:35]\n"
"ds_write_b128 v24, v[204:207] offset:3072\n"
"v_mfma_f32_16x16x16_bf16 v[36:39], a[186:187], v[186:187], v[36:39]\n"
"v_cvt_pk_bf16_f32 v112, v160, v161\n"
"v_cvt_pk_bf16_f32 v113, v162, v163\n"
"v_mfma_f32_16x16x32_bf16 v[40:43], a[16:19], v[178:181], 0\n"
"v_cvt_pk_bf16_f32 v114, v164, v165\n"
"v_cvt_pk_bf16_f32 v115, v166, v167\n"
"v_mfma_f32_16x16x32_bf16 v[44:47], a[24:27], v[178:181], 0\n"
"buffer_store_dwordx4 v[12:15], v0, s[44:47], s52 offen offset:3584 sc1\n"
"v_mfma_f32_16x16x32_bf16 v[40:43], a[20:23], v[182:185], v[40:43]\n"
"v_cvt_pk_bf16_f32 v208, v32, v33\n"
"v_cvt_pk_bf16_f32 v209, v34, v35\n"
"v_mfma_f32_16x16x32_bf16 v[44:47], a[28:31], v[182:185], v[44:47]\n"
"v_cvt_pk_bf16_f32 v210, v36, v37\n"
"v_cvt_pk_bf16_f32 v211, v38, v39\n"
"v_mfma_f32_16x16x16_bf16 v[40:43], a[188:189], v[186:187], v[40:43]\n"
"ds_write_b128 v24, v[208:211] offset:16384\n"
"v_mfma_f32_16x16x16_bf16 v[44:47], a[190:191], v[186:187], v[44:47]\n"
"v_pk_mul_f32 v[128:129], v[128:129], v[30:31] op_sel_hi:[1,0]\n"
"v_pk_mul_f32 v[130:131], v[130:131], v[30:31] op_sel_hi:[1,0]\n"
"v_mfma_f32_16x16x32_bf16 v[48:51], a[32:35], v[178:181], 0\n"
"v_pk_mul_f32 v[132:133], v[132:133], v[30:31] op_sel_hi:[1,0]\n"
"v_pk_mul_f32 v[134:135], v[134:135], v[30:31] op_sel_hi:[1,0]\n"
"v_mfma_f32_16x16x32_bf16 v[52:55], a[40:43], v[178:181], 0\n"
"v_cvt_pk_bf16_f32 v212, v40, v41\n"
"v_cvt_pk_bf16_f32 v213, v42, v43\n"
"v_mfma_f32_16x16x32_bf16 v[48:51], a[36:39], v[182:185], v[48:51]\n"
"buffer_store_dwordx4 v[12:15], v0, s[44:47], s53 offen offset:512 sc1\n"
"v_mfma_f32_16x16x32_bf16 v[52:55], a[44:47], v[182:185], v[52:55]\n"
"buffer_load_dwordx4 v8, s[24:27], s72 offen lds\n"
"v_mfma_f32_16x16x16_bf16 v[48:51], a[192:193], v[186:187], v[48:51]\n"
"v_cvt_pk_bf16_f32 v214, v44, v45\n"
"v_cvt_pk_bf16_f32 v215, v46, v47\n"
"v_mfma_f32_16x16x16_bf16 v[52:55], a[194:195], v[186:187], v[52:55]\n"
"ds_write_b128 v24, v[212:215] offset:17408\n"
"v_mfma_f32_16x16x32_bf16 v[56:59], a[48:51], v[178:181], 0\n"
"v_cvt_pk_bf16_f32 v116, v128, v129\n"
"v_cvt_pk_bf16_f32 v117, v130, v131\n"
"v_mfma_f32_16x16x32_bf16 v[60:63], a[56:59], v[178:181], 0\n"
"v_cvt_pk_bf16_f32 v216, v48, v49\n"
"v_cvt_pk_bf16_f32 v217, v50, v51\n"
"v_mfma_f32_16x16x32_bf16 v[56:59], a[52:55], v[182:185], v[56:59]\n"
"buffer_load_dwordx4 v9, s[24:27], s72 offen offset:1024 lds\n"
"v_mfma_f32_16x16x32_bf16 v[60:63], a[60:63], v[182:185], v[60:63]\n"
"buffer_load_dwordx4 v10, s[24:27], s72 offen offset:2048 lds\n"
"v_mfma_f32_16x16x16_bf16 v[56:59], a[196:197], v[186:187], v[56:59]\n"
"v_cvt_pk_bf16_f32 v218, v52, v53\n"
"v_cvt_pk_bf16_f32 v219, v54, v55\n"
"v_mfma_f32_16x16x16_bf16 v[60:63], a[198:199], v[186:187], v[60:63]\n"
"ds_write_b128 v24, v[216:219] offset:18432\n"
"v_mfma_f32_16x16x32_bf16 v[32:35], a[0:3], v[96:99], 0\n"
"v_cvt_pk_bf16_f32 v118, v132, v133\n"
"v_cvt_pk_bf16_f32 v119, v134, v135\n"
"v_mfma_f32_16x16x32_bf16 v[36:39], a[8:11], v[96:99], 0\n"
"v_cvt_pk_bf16_f32 v220, v56, v57\n"
"v_cvt_pk_bf16_f32 v221, v58, v59\n"
"v_mfma_f32_16x16x32_bf16 v[32:35], a[4:7], v[100:103], v[32:35]\n"
"v_cvt_pk_bf16_f32 v222, v60, v61\n"
"v_cvt_pk_bf16_f32 v223, v62, v63\n"
"v_mfma_f32_16x16x32_bf16 v[36:39], a[12:15], v[100:103], v[36:39]\n"
"buffer_load_dwordx4 v11, s[24:27], s72 offen offset:3072 lds\n"
"v_mfma_f32_16x16x16_bf16 v[32:35], a[184:185], v[104:105], v[32:35]\n"
"s_add_u32 m0, s69, 147456\n"
"s_add_u32 s72, s55, 65536\n"
"v_pk_mul_f32 v[136:137], v[136:137], v[30:31] op_sel_hi:[1,0]\n"
"v_mfma_f32_16x16x16_bf16 v[36:39], a[186:187], v[104:105], v[36:39]\n"
"buffer_load_dwordx4 v8, s[24:27], s72 offen lds\n"
"v_mfma_f32_16x16x32_bf16 v[40:43], a[16:19], v[96:99], 0\n"
"ds_write_b128 v24, v[220:223] offset:19456\n"
"v_mfma_f32_16x16x32_bf16 v[44:47], a[24:27], v[96:99], 0\n"
"s_waitcnt lgkmcnt(0)\n"
"s_barrier\n"
"ds_read_b128 a[64:67], v21\n"
"v_cvt_pk_bf16_f32 v224, v32, v33\n"
"v_mfma_f32_16x16x32_bf16 v[40:43], a[20:23], v[100:103], v[40:43]\n"
"ds_read_b128 a[68:71], v21 offset:16384\n"
"v_cvt_pk_bf16_f32 v225, v34, v35\n"
"v_mfma_f32_16x16x32_bf16 v[44:47], a[28:31], v[100:103], v[44:47]\n"
"ds_read_b128 a[72:75], v22\n"
"v_cvt_pk_bf16_f32 v226, v36, v37\n"
"v_mfma_f32_16x16x16_bf16 v[40:43], a[188:189], v[104:105], v[40:43]\n"
"buffer_load_dwordx4 v9, s[24:27], s72 offen offset:1024 lds\n"
"v_mfma_f32_16x16x16_bf16 v[44:47], a[190:191], v[104:105], v[44:47]\n"
"buffer_load_dwordx4 v10, s[24:27], s72 offen offset:2048 lds\n"
"v_mfma_f32_16x16x32_bf16 v[48:51], a[32:35], v[96:99], 0\n"
"ds_read_b128 a[76:79], v22 offset:16384\n"
"v_cvt_pk_bf16_f32 v227, v38, v39\n"
"v_mfma_f32_16x16x32_bf16 v[52:55], a[40:43], v[96:99], 0\n"
"ds_read_b128 a[80:83], v23\n"
"v_cvt_pk_bf16_f32 v228, v40, v41\n"
"v_mfma_f32_16x16x32_bf16 v[48:51], a[36:39], v[100:103], v[48:51]\n"
"buffer_load_dwordx4 v11, s[24:27], s72 offen offset:3072 lds\n"
"v_mfma_f32_16x16x32_bf16 v[52:55], a[44:47], v[100:103], v[52:55]\n"
"s_add_u32 m0, s69, 114688\n"
"ds_read_b128 a[84:87], v23 offset:16384\n"
"v_cvt_pk_bf16_f32 v229, v42, v43\n"
"v_mfma_f32_16x16x16_bf16 v[48:51], a[192:193], v[104:105], v[48:51]\n"
"s_add_u32 s72, s55, 98304\n"
"ds_read_b128 a[88:91], v21 offset:1024\n"
"v_cvt_pk_bf16_f32 v230, v44, v45\n"
"v_mfma_f32_16x16x16_bf16 v[52:55], a[194:195], v[104:105], v[52:55]\n"
"ds_read_b128 a[92:95], v21 offset:17408\n"
"v_cvt_pk_bf16_f32 v231, v46, v47\n"
"v_mfma_f32_16x16x32_bf16 v[56:59], a[48:51], v[96:99], 0\n"
"ds_read_b128 a[96:99], v22 offset:1024\n"
"v_cvt_pk_bf16_f32 v232, v48, v49\n"
"v_mfma_f32_16x16x32_bf16 v[60:63], a[56:59], v[96:99], 0\n"
"ds_read_b128 a[100:103], v22 offset:17408\n"
"v_cvt_pk_bf16_f32 v233, v50, v51\n"
"v_mfma_f32_16x16x32_bf16 v[56:59], a[52:55], v[100:103], v[56:59]\n"
"ds_read_b128 a[104:107], v23 offset:1024\n"
"v_cvt_pk_bf16_f32 v234, v52, v53\n"
"v_mfma_f32_16x16x32_bf16 v[60:63], a[60:63], v[100:103], v[60:63]\n"
"ds_read_b128 a[108:111], v23 offset:17408\n"
"v_cvt_pk_bf16_f32 v235, v54, v55\n"
"v_mfma_f32_16x16x16_bf16 v[56:59], a[196:197], v[104:105], v[56:59]\n"
"ds_read_b128 a[112:115], v21 offset:2048\n"
"v_pk_mul_f32 v[138:139], v[138:139], v[30:31] op_sel_hi:[1,0]\n"
"v_mfma_f32_16x16x16_bf16 v[60:63], a[198:199], v[104:105], v[60:63]\n"
"ds_read_b128 a[116:119], v21 offset:18432\n"
"v_pk_mul_f32 v[140:141], v[140:141], v[30:31] op_sel_hi:[1,0]\n"
"v_mfma_f32_16x16x32_bf16 v[32:35], a[0:3], v[106:109], 0\n"
"ds_read_b128 a[120:123], v22 offset:2048\n"
"v_pk_mul_f32 v[142:143], v[142:143], v[30:31] op_sel_hi:[1,0]\n"
"v_mfma_f32_16x16x32_bf16 v[36:39], a[8:11], v[106:109], 0\n"
"ds_read_b128 a[124:127], v22 offset:18432\n"
"v_cvt_pk_bf16_f32 v236, v56, v57\n"
"v_mfma_f32_16x16x32_bf16 v[32:35], a[4:7], v[110:113], v[32:35]\n"
"ds_read_b128 a[128:131], v23 offset:2048\n"
"v_cvt_pk_bf16_f32 v237, v58, v59\n"
"v_mfma_f32_16x16x32_bf16 v[36:39], a[12:15], v[110:113], v[36:39]\n"
"ds_read_b128 a[132:135], v23 offset:18432\n"
"v_cvt_pk_bf16_f32 v238, v60, v61\n"
"v_mfma_f32_16x16x16_bf16 v[32:35], a[184:185], v[114:115], v[32:35]\n"
"ds_read_b128 a[136:139], v21 offset:3072\n"
"v_cvt_pk_bf16_f32 v239, v62, v63\n"
"v_mfma_f32_16x16x16_bf16 v[36:39], a[186:187], v[114:115], v[36:39]\n"
"ds_read_b128 a[140:143], v21 offset:19456\n"
"v_cvt_pk_bf16_f32 v120, v136, v137\n"
"v_mfma_f32_16x16x32_bf16 v[40:43], a[16:19], v[106:109], 0\n"
"ds_read_b128 a[144:147], v22 offset:3072\n"
"v_cvt_pk_bf16_f32 v121, v138, v139\n"
"v_mfma_f32_16x16x32_bf16 v[44:47], a[24:27], v[106:109], 0\n"
"ds_read_b128 a[148:151], v22 offset:19456\n"
"v_cvt_pk_bf16_f32 v240, v32, v33\n"
"v_mfma_f32_16x16x32_bf16 v[40:43], a[20:23], v[110:113], v[40:43]\n"
"ds_read_b128 a[152:155], v23 offset:3072\n"
"v_cvt_pk_bf16_f32 v241, v34, v35\n"
"v_mfma_f32_16x16x32_bf16 v[44:47], a[28:31], v[110:113], v[44:47]\n"
"ds_read_b128 a[156:159], v23 offset:19456\n"
"v_cvt_pk_bf16_f32 v242, v36, v37\n"
"v_mfma_f32_16x16x16_bf16 v[40:43], a[188:189], v[114:115], v[40:43]\n"
"ds_write_b128 v24, v[224:227] offset:32768\n"
"v_mfma_f32_16x16x16_bf16 v[44:47], a[190:191], v[114:115], v[44:47]\n"
"ds_write_b128 v24, v[228:231] offset:33792\n"
"v_mfma_f32_16x16x32_bf16 v[48:51], a[32:35], v[106:109], 0\n"
"ds_write_b128 v24, v[232:235] offset:34816\n"
"v_mfma_f32_16x16x32_bf16 v[52:55], a[40:43], v[106:109], 0\n"
"ds_write_b128 v24, v[236:239] offset:35840\n"
"v_mfma_f32_16x16x32_bf16 v[48:51], a[36:39], v[110:113], v[48:51]\n"
"s_waitcnt vmcnt(17) lgkmcnt(0)\n"
"v_cvt_pk_bf16_f32 v243, v38, v39\n"
"s_barrier\n"
"v_mfma_f32_16x16x32_bf16 v[52:55], a[44:47], v[110:113], v[52:55]\n"
"v_cvt_pk_bf16_f32 v244, v40, v41\n"
"v_cvt_pk_bf16_f32 v245, v42, v43\n"
"v_mfma_f32_16x16x16_bf16 v[48:51], a[192:193], v[114:115], v[48:51]\n"
"v_cvt_pk_bf16_f32 v246, v44, v45\n"
"v_cvt_pk_bf16_f32 v247, v46, v47\n"
"v_mfma_f32_16x16x16_bf16 v[52:55], a[194:195], v[114:115], v[52:55]\n"
"ds_write_b128 v24, v[240:243]\n"
"v_mfma_f32_16x16x32_bf16 v[56:59], a[48:51], v[106:109], 0\n"
"ds_write_b128 v24, v[244:247] offset:1024\n"
"v_mfma_f32_16x16x32_bf16 v[60:63], a[56:59], v[106:109], 0\n"
"v_cvt_pk_bf16_f32 v122, v140, v141\n"
"v_cvt_pk_bf16_f32 v248, v48, v49\n"
"v_mfma_f32_16x16x32_bf16 v[56:59], a[52:55], v[110:113], v[56:59]\n"
"v_cvt_pk_bf16_f32 v249, v50, v51\n"
"v_cvt_pk_bf16_f32 v250, v52, v53\n"
"v_mfma_f32_16x16x32_bf16 v[60:63], a[60:63], v[110:113], v[60:63]\n"
"v_cvt_pk_bf16_f32 v251, v54, v55\n"
"v_cvt_pk_bf16_f32 v123, v142, v143\n"
"v_mfma_f32_16x16x16_bf16 v[56:59], a[196:197], v[114:115], v[56:59]\n"
"ds_write_b128 v24, v[248:251] offset:2048\n"
"v_mfma_f32_16x16x16_bf16 v[60:63], a[198:199], v[114:115], v[60:63]\n"
"v_pk_mul_f32 v[144:145], v[144:145], v[30:31] op_sel_hi:[1,0]\n"
"v_cvt_pk_bf16_f32 v124, v144, v145\n"
"v_mfma_f32_16x16x32_bf16 v[32:35], a[0:3], v[116:119], 0\n"
"v_lshlrev_b32 v184, 2, v17\n"
"v_sub_u32 v184, v18, v184\n"
"v_mfma_f32_16x16x32_bf16 v[36:39], a[8:11], v[116:119], 0\n"
"v_cvt_pk_bf16_f32 v252, v56, v57\n"
"v_cvt_pk_bf16_f32 v253, v58, v59\n"
"v_mfma_f32_16x16x32_bf16 v[32:35], a[4:7], v[120:123], v[32:35]\n"
"v_cvt_pk_bf16_f32 v254, v60, v61\n"
"v_cvt_pk_bf16_f32 v255, v62, v63\n"
"v_mfma_f32_16x16x32_bf16 v[36:39], a[12:15], v[120:123], v[36:39]\n"
"ds_write_b128 v24, v[252:255] offset:3072\n"
"v_mfma_f32_16x16x16_bf16 v[32:35], a[184:185], v[124:125], v[32:35]\n"
"ds_read_b128 a[160:163], v21 offset:32768\n"
"v_and_b32 v185, 1, v184\n"
"v_mfma_f32_16x16x16_bf16 v[36:39], a[186:187], v[124:125], v[36:39]\n"
"ds_read_b128 a[164:167], v21 offset:33792\n"
"v_lshlrev_b32 v185, 4, v185\n"
"v_mfma_f32_16x16x32_bf16 v[40:43], a[16:19], v[116:119], 0\n"
"ds_read_b128 a[168:171], v21 offset:34816\n"
"v_mov_b32 v186, 0x3f80\n"
"v_mfma_f32_16x16x32_bf16 v[44:47], a[24:27], v[116:119], 0\n"
"ds_read_b128 a[172:175], v21 offset:35840\n"
"v_cvt_pk_bf16_f32 v64, v32, v33\n"
"v_mfma_f32_16x16x32_bf16 v[40:43], a[20:23], v[120:123], v[40:43]\n"
"ds_read_b128 a[176:179], v22 offset:32768\n"
"v_cvt_pk_bf16_f32 v65, v34, v35\n"
"v_mfma_f32_16x16x32_bf16 v[44:47], a[28:31], v[120:123], v[44:47]\n"
"ds_read_b128 a[180:183], v22 offset:33792\n"
"v_cvt_pk_bf16_f32 v66, v36, v37\n"
"v_mfma_f32_16x16x16_bf16 v[40:43], a[188:189], v[124:125], v[40:43]\n"
"ds_read_b128 a[200:203], v22 offset:34816\n"
"v_cvt_pk_bf16_f32 v67, v38, v39\n"
"v_mfma_f32_16x16x16_bf16 v[44:47], a[190:191], v[124:125], v[44:47]\n"
"ds_read_b128 a[204:207], v22 offset:35840\n"
"v_lshlrev_b32 v186, v185, v186\n"
"v_mfma_f32_16x16x32_bf16 v[48:51], a[32:35], v[116:119], 0\n"
"ds_read_b128 a[208:211], v23 offset:32768\n"
"v_lshrrev_b32 v187, 1, v184\n"
"v_mfma_f32_16x16x32_bf16 v[52:55], a[40:43], v[116:119], 0\n"
"ds_read_b128 a[212:215], v23 offset:33792\n"
"v_cvt_pk_bf16_f32 v68, v40, v41\n"
"v_mfma_f32_16x16x32_bf16 v[48:51], a[36:39], v[120:123], v[48:51]\n"
"ds_read_b128 a[216:219], v23 offset:34816\n"
"v_cvt_pk_bf16_f32 v69, v42, v43\n"
"v_mfma_f32_16x16x32_bf16 v[52:55], a[44:47], v[120:123], v[52:55]\n"
"ds_read_b128 a[220:223], v23 offset:35840\n"
"ds_read_b64_tr_b16 v[80:81], v92\n"
"v_mfma_f32_16x16x16_bf16 v[48:51], a[192:193], v[124:125], v[48:51]\n"
"ds_read_b64_tr_b16 v[82:83], v92 offset:512\n"
"ds_read_b64_tr_b16 v[84:85], v92 offset:16384\n"
"v_mfma_f32_16x16x16_bf16 v[52:55], a[194:195], v[124:125], v[52:55]\n"
"ds_read_b64_tr_b16 v[86:87], v92 offset:16896\n"
"ds_read_b64_tr_b16 v[88:89], v92 offset:32768\n"
"v_mfma_f32_16x16x32_bf16 v[56:59], a[48:51], v[116:119], 0\n"
"ds_read_b64_tr_b16 v[90:91], v92 offset:33280\n"
"ds_read_b64_tr_b16 v[168:169], v92 offset:49152\n"
"v_mfma_f32_16x16x32_bf16 v[60:63], a[56:59], v[116:119], 0\n"
"ds_read_b64_tr_b16 v[170:171], v92 offset:49664\n"
"ds_read_b64_tr_b16 v[172:173], v93\n"
"v_mfma_f32_16x16x32_bf16 v[56:59], a[52:55], v[120:123], v[56:59]\n"
"ds_read_b64_tr_b16 v[174:175], v93 offset:512\n"
"ds_read_b64_tr_b16 v[176:177], v93 offset:16384\n"
"v_mfma_f32_16x16x32_bf16 v[60:63], a[60:63], v[120:123], v[60:63]\n"
"ds_read_b64_tr_b16 v[178:179], v93 offset:16896\n"
"ds_read_b64_tr_b16 v[180:181], v93 offset:32768\n"
"v_mfma_f32_16x16x16_bf16 v[56:59], a[196:197], v[124:125], v[56:59]\n"
"ds_read_b64_tr_b16 v[182:183], v93 offset:33280\n"
"v_cvt_pk_bf16_f32 v70, v44, v45\n"
"v_mfma_f32_16x16x16_bf16 v[60:63], a[198:199], v[124:125], v[60:63]\n"
"ds_write_b128 v24, v[64:67] offset:16384\n"
"s_waitcnt lgkmcnt(13)\n"
"v_mfma_f32_16x16x32_bf16 v[128:131], v[80:83], v[192:195], 0\n"
"v_cvt_pk_bf16_f32 v71, v46, v47\n"
"v_cvt_pk_bf16_f32 v72, v48, v49\n"
"v_mfma_f32_16x16x32_bf16 v[132:135], v[80:83], v[208:211], 0\n"
"ds_write_b128 v24, v[68:71] offset:17408\n"
"v_mfma_f32_16x16x32_bf16 v[136:139], v[80:83], v[224:227], 0\n"
"v_cvt_pk_bf16_f32 v73, v50, v51\n"
"v_cvt_pk_bf16_f32 v74, v52, v53\n"
"v_mfma_f32_16x16x32_bf16 v[140:143], v[80:83], v[240:243], 0\n"
"v_cvt_pk_bf16_f32 v75, v54, v55\n"
"v_cvt_pk_bf16_f32 v76, v56, v57\n"
"v_mfma_f32_16x16x32_bf16 v[144:147], v[80:83], v[64:67], 0\n"
"s_waitcnt lgkmcnt(9)\n"
"ds_write_b128 v24, v[72:75] offset:18432\n"
"v_mfma_f32_16x16x32_bf16 v[128:131], v[84:87], v[196:199], v[128:131]\n"
"v_cvt_pk_bf16_f32 v77, v58, v59\n"
"v_cvt_pk_bf16_f32 v78, v60, v61\n"
"v_mfma_f32_16x16x32_bf16 v[132:135], v[84:87], v[212:215], v[132:135]\n"
"v_cvt_pk_bf16_f32 v79, v62, v63\n"
"v_add_u32 v92, 16384, v92\n"
"v_mfma_f32_16x16x32_bf16 v[136:139], v[84:87], v[228:231], v[136:139]\n"
"ds_write_b128 v24, v[76:79] offset:19456\n"
"v_mfma_f32_16x16x32_bf16 v[140:143], v[84:87], v[244:247], v[140:143]\n"
"s_waitcnt lgkmcnt(0)\n"
"s_barrier\n"
"buffer_load_dwordx4 v8, s[24:27], s72 offen lds\n"
"v_mfma_f32_16x16x32_bf16 v[144:147], v[84:87], v[68:71], v[144:147]\n"
"buffer_load_dwordx4 v9, s[24:27], s72 offen offset:1024 lds\n"
"v_mfma_f32_16x16x32_bf16 v[128:131], v[88:91], v[200:203], v[128:131]\n"
"buffer_load_dwordx4 v10, s[24:27], s72 offen offset:2048 lds\n"
"v_mfma_f32_16x16x32_bf16 v[132:135], v[88:91], v[216:219], v[132:135]\n"
"buffer_load_dwordx4 v11, s[24:27], s72 offen offset:3072 lds\n"
"v_mfma_f32_16x16x32_bf16 v[136:139], v[88:91], v[232:235], v[136:139]\n"
"s_add_u32 m0, s69, 81920\n"
"ds_read_b128 a[224:227], v21\n"
"s_add_u32 s72, s56, 32768\n"
"v_mfma_f32_16x16x32_bf16 v[140:143], v[88:91], v[248:251], v[140:143]\n"
"ds_read_b128 a[228:231], v21 offset:16384\n"
"ds_read_b64_tr_b16 a[184:185], v93 offset:49152\n"
"v_mfma_f32_16x16x32_bf16 v[144:147], v[88:91], v[72:75], v[144:147]\n"
"ds_read_b64_tr_b16 a[186:187], v93 offset:49664\n"
"ds_read_b128 a[232:235], v21 offset:1024\n"
"v_mfma_f32_16x16x32_bf16 v[128:131], v[168:171], v[204:207], v[128:131]\n"
"ds_read_b128 a[236:239], v21 offset:17408\n"
"ds_read_b64_tr_b16 a[188:189], v94\n"
"v_mfma_f32_16x16x32_bf16 v[132:135], v[168:171], v[220:223], v[132:135]\n"
"ds_read_b64_tr_b16 a[190:191], v94 offset:512\n"
"ds_read_b128 a[240:243], v21 offset:2048\n"
"v_mfma_f32_16x16x32_bf16 v[136:139], v[168:171], v[236:239], v[136:139]\n"
"ds_read_b128 a[244:247], v21 offset:18432\n"
"ds_read_b64_tr_b16 a[192:193], v94 offset:16384\n"
"v_mfma_f32_16x16x32_bf16 v[140:143], v[168:171], v[252:255], v[140:143]\n"
"ds_read_b64_tr_b16 a[194:195], v94 offset:16896\n"
"ds_read_b128 a[248:251], v21 offset:3072\n"
"v_mfma_f32_16x16x32_bf16 v[144:147], v[168:171], v[76:79], v[144:147]\n"
"ds_read_b128 a[252:255], v21 offset:19456\n"
"v_add_u32 v93, 16384, v93\n"
"v_mfma_f32_16x16x32_bf16 v[128:131], v[172:175], a[64:67], v[128:131]\n"
"ds_read_b128 a[0:3], v22\n"
"v_mfma_f32_16x16x32_bf16 v[132:135], v[172:175], a[68:71], v[132:135]\n"
"ds_read_b128 a[4:7], v22 offset:16384\n"
"v_mfma_f32_16x16x32_bf16 v[136:139], v[172:175], a[160:163], v[136:139]\n"
"s_waitcnt lgkmcnt(14)\n"
"ds_read_b128 a[8:11], v22 offset:1024\n"
"v_mfma_f32_16x16x32_bf16 v[140:143], v[172:175], a[224:227], v[140:143]\n"
"ds_read_b128 a[12:15], v22 offset:17408\n"
"v_mfma_f32_16x16x32_bf16 v[144:147], v[172:175], a[228:231], v[144:147]\n"
"ds_read_b128 a[16:19], v22 offset:2048\n"
"v_mfma_f32_16x16x32_bf16 v[128:131], v[176:179], a[88:91], v[128:131]\n"
"ds_read_b128 a[20:23], v22 offset:18432\n"
"v_mfma_f32_16x16x32_bf16 v[132:135], v[176:179], a[92:95], v[132:135]\n"
"ds_read_b128 a[24:27], v22 offset:3072\n"
"v_mfma_f32_16x16x32_bf16 v[136:139], v[176:179], a[164:167], v[136:139]\n"
"s_waitcnt lgkmcnt(14)\n"
"ds_read_b128 a[28:31], v22 offset:19456\n"
"v_mfma_f32_16x16x32_bf16 v[140:143], v[176:179], a[232:235], v[140:143]\n"
"ds_read_b128 a[32:35], v23\n"
"v_mfma_f32_16x16x32_bf16 v[144:147], v[176:179], a[236:239], v[144:147]\n"
"ds_read_b128 a[36:39], v23 offset:16384\n"
"v_mfma_f32_16x16x32_bf16 v[128:131], v[180:183], a[112:115], v[128:131]\n"
"ds_read_b128 a[40:43], v23 offset:1024\n"
"v_mfma_f32_16x16x32_bf16 v[132:135], v[180:183], a[116:119], v[132:135]\n"
"ds_read_b128 a[44:47], v23 offset:17408\n"
"v_mfma_f32_16x16x32_bf16 v[136:139], v[180:183], a[168:171], v[136:139]\n"
"s_waitcnt lgkmcnt(11)\n"
"ds_read_b128 a[48:51], v23 offset:2048\n"
"v_mfma_f32_16x16x32_bf16 v[140:143], v[180:183], a[240:243], v[140:143]\n"
"ds_read_b128 a[52:55], v23 offset:18432\n"
"v_mfma_f32_16x16x32_bf16 v[144:147], v[180:183], a[244:247], v[144:147]\n"
"ds_read_b128 a[56:59], v23 offset:3072\n"
"v_mfma_f32_16x16x32_bf16 v[128:131], a[184:187], a[136:139], v[128:131]\n"
"ds_read_b128 a[60:63], v23 offset:19456\n"
"ds_read_b64_tr_b16 a[196:197], v94 offset:32768\n"
"v_mfma_f32_16x16x32_bf16 v[132:135], a[184:187], a[140:143], v[132:135]\n"
"ds_read_b64_tr_b16 a[198:199], v94 offset:33280\n"
"ds_read_b64_tr_b16 v[80:81], v94 offset:49152\n"
"v_mfma_f32_16x16x32_bf16 v[136:139], a[184:187], a[172:175], v[136:139]\n"
"ds_read_b64_tr_b16 v[82:83], v94 offset:49664\n"
"ds_read_b64_tr_b16 v[84:85], v95\n"
"v_mfma_f32_16x16x32_bf16 v[140:143], a[184:187], a[248:251], v[140:143]\n"
"ds_read_b64_tr_b16 v[86:87], v95 offset:512\n"
"ds_read_b64_tr_b16 v[88:89], v95 offset:16384\n"
"v_mfma_f32_16x16x32_bf16 v[144:147], a[184:187], a[252:255], v[144:147]\n"
"ds_read_b64_tr_b16 v[90:91], v95 offset:16896\n"
"ds_read_b64_tr_b16 v[168:169], v95 offset:32768\n"
"v_mfma_f32_16x16x32_bf16 v[128:131], a[188:191], a[72:75], v[128:131]\n"
"ds_read_b64_tr_b16 v[170:171], v95 offset:33280\n"
"ds_read_b64_tr_b16 v[172:173], v95 offset:49152\n"
"v_mfma_f32_16x16x32_bf16 v[132:135], a[188:191], a[76:79], v[132:135]\n"
"ds_read_b64_tr_b16 v[174:175], v95 offset:49664\n"
"s_waitcnt lgkmcnt(0)\n"
"s_barrier\n"
"v_mfma_f32_16x16x32_bf16 v[136:139], a[188:191], a[176:179], v[136:139]\n"
"buffer_load_dwordx4 v8, s[24:27], s56 offen lds\n"
"v_mfma_f32_16x16x32_bf16 v[140:143], a[188:191], a[0:3], v[140:143]\n"
"buffer_load_dwordx4 v9, s[24:27], s56 offen offset:1024 lds\n"
"v_mfma_f32_16x16x32_bf16 v[144:147], a[188:191], a[4:7], v[144:147]\n"
"buffer_load_dwordx4 v10, s[24:27], s56 offen offset:2048 lds\n"
"v_mfma_f32_16x16x32_bf16 v[128:131], a[192:195], a[96:99], v[128:131]\n"
"buffer_load_dwordx4 v11, s[24:27], s56 offen offset:3072 lds\n"
"v_mfma_f32_16x16x32_bf16 v[132:135], a[192:195], a[100:103], v[132:135]\n"
"s_add_u32 m0, s69, 98304\n"
"v_add_u32 v94, 16384, v94\n"
"v_add_u32 v95, 16384, v95\n"
"v_mfma_f32_16x16x32_bf16 v[136:139], a[192:195], a[180:183], v[136:139]\n"
"buffer_load_dwordx4 v8, s[24:27], s72 offen lds\n"
"v_mfma_f32_16x16x32_bf16 v[140:143], a[192:195], a[8:11], v[140:143]\n"
"buffer_load_dwordx4 v9, s[24:27], s72 offen offset:1024 lds\n"
"v_mfma_f32_16x16x32_bf16 v[144:147], a[192:195], a[12:15], v[144:147]\n"
"buffer_load_dwordx4 v10, s[24:27], s72 offen offset:2048 lds\n"
"v_mfma_f32_16x16x32_bf16 v[128:131], a[196:199], a[120:123], v[128:131]\n"
"v_cmp_eq_u32 vcc, 0, v187\n"
"s_nop 1\n"
"v_cndmask_b32 v184, 0, v186, vcc\n"
"v_mfma_f32_16x16x32_bf16 v[132:135], a[196:199], a[124:127], v[132:135]\n"
"v_cmp_eq_u32 vcc, 1, v187\n"
"s_nop 1\n"
"v_cndmask_b32 v185, 0, v186, vcc\n"
"v_mfma_f32_16x16x32_bf16 v[136:139], a[196:199], a[200:203], v[136:139]\n"
"s_waitcnt vmcnt(7)\n"
"s_barrier\n"
"ds_read_b64_tr_b16 v[176:177], v92 offset:49152\n"
"ds_read_b64_tr_b16 v[178:179], v92 offset:49664\n"
"v_mfma_f32_16x16x32_bf16 v[140:143], a[196:199], a[16:19], v[140:143]\n"
"ds_read_b64_tr_b16 v[180:181], v93 offset:49152\n"
"ds_read_b64_tr_b16 v[182:183], v93 offset:49664\n"
"v_mfma_f32_16x16x32_bf16 v[144:147], a[196:199], a[20:23], v[144:147]\n"
"ds_read_b64_tr_b16 a[184:185], v94 offset:49152\n"
"ds_read_b64_tr_b16 a[186:187], v94 offset:49664\n"
"v_mfma_f32_16x16x32_bf16 v[128:131], v[80:83], a[144:147], v[128:131]\n"
"ds_read_b64_tr_b16 a[188:189], v95 offset:49152\n"
"ds_read_b64_tr_b16 a[190:191], v95 offset:49664\n"
"v_mfma_f32_16x16x32_bf16 v[132:135], v[80:83], a[148:151], v[132:135]\n"
"v_add_u32 v92, 81920, v92\n"
"ds_read_b64_tr_b16 a[192:193], v92 offset:32768\n"
"v_mfma_f32_16x16x32_bf16 v[136:139], v[80:83], a[204:207], v[136:139]\n"
"ds_read_b64_tr_b16 a[194:195], v92 offset:33280\n"
"v_add_u32 v93, 81920, v93\n"
"v_mfma_f32_16x16x32_bf16 v[140:143], v[80:83], a[24:27], v[140:143]\n"
"ds_read_b64_tr_b16 a[196:197], v93 offset:32768\n"
"v_add_u32 v94, 81920, v94\n"
"v_mfma_f32_16x16x32_bf16 v[144:147], v[80:83], a[28:31], v[144:147]\n"
"ds_read_b64_tr_b16 a[198:199], v93 offset:33280\n"
"ds_read_b64_tr_b16 v[80:81], v94 offset:32768\n"
"v_mfma_f32_16x16x32_bf16 v[128:131], v[84:87], a[80:83], v[128:131]\n"
"v_add_u32 v95, 81920, v95\n"
"ds_read_b64_tr_b16 v[82:83], v94 offset:33280\n"
"v_mfma_f32_16x16x32_bf16 v[132:135], v[84:87], a[84:87], v[132:135]\n"
"buffer_load_dwordx4 v11, s[24:27], s72 offen offset:3072 lds\n"
"v_mfma_f32_16x16x32_bf16 v[136:139], v[84:87], a[208:211], v[136:139]\n"
"s_add_u32 m0, s69, 0\n"
"s_add_u32 s72, s56, 65536\n"
"v_mfma_f32_16x16x32_bf16 v[140:143], v[84:87], a[32:35], v[140:143]\n"
"v_mfma_f32_16x16x32_bf16 v[144:147], v[84:87], a[36:39], v[144:147]\n"
"ds_read_b64_tr_b16 v[84:85], v95 offset:32768\n"
"ds_read_b64_tr_b16 v[86:87], v95 offset:33280\n"
"v_mfma_f32_16x16x32_bf16 v[128:131], v[88:91], a[104:107], v[128:131]\n"
"buffer_load_dwordx4 v8, s[24:27], s72 offen lds\n"
"v_mfma_f32_16x16x32_bf16 v[132:135], v[88:91], a[108:111], v[132:135]\n"
"v_mfma_f32_16x16x32_bf16 v[136:139], v[88:91], a[212:215], v[136:139]\n"
"v_mfma_f32_16x16x32_bf16 v[140:143], v[88:91], a[40:43], v[140:143]\n"
"v_mfma_f32_16x16x32_bf16 v[144:147], v[88:91], a[44:47], v[144:147]\n"
"buffer_load_dwordx4 v9, s[24:27], s72 offen offset:1024 lds\n"
"v_mfma_f32_16x16x32_bf16 v[128:131], v[168:171], a[128:131], v[128:131]\n"
"ds_read_b64_tr_b16 v[88:89], v92 offset:49152\n"
"ds_read_b64_tr_b16 v[90:91], v92 offset:49664\n"
"v_mfma_f32_16x16x32_bf16 v[132:135], v[168:171], a[132:135], v[132:135]\n"
"v_mfma_f32_16x16x32_bf16 v[136:139], v[168:171], a[216:219], v[136:139]\n"
"v_mfma_f32_16x16x32_bf16 v[140:143], v[168:171], a[48:51], v[140:143]\n"
"buffer_load_dwordx4 v10, s[24:27], s72 offen offset:2048 lds\n"
"v_mfma_f32_16x16x32_bf16 v[144:147], v[168:171], a[52:55], v[144:147]\n"
"ds_read_b64_tr_b16 v[168:169], v93 offset:49152\n"
"ds_read_b64_tr_b16 v[170:171], v93 offset:49664\n"
"v_mfma_f32_16x16x32_bf16 v[128:131], v[172:175], a[152:155], v[128:131]\n"
"v_mfma_f32_16x16x32_bf16 v[132:135], v[172:175], a[156:159], v[132:135]\n"
"v_mfma_f32_16x16x32_bf16 v[136:139], v[172:175], a[220:223], v[136:139]\n"
"buffer_load_dwordx4 v11, s[24:27], s72 offen offset:3072 lds\n"
"v_mfma_f32_16x16x32_bf16 v[140:143], v[172:175], a[56:59], v[140:143]\n"
"s_add_u32 m0, s69, 16384\n"
"s_add_u32 s72, s56, 98304\n"
"v_mfma_f32_16x16x32_bf16 v[144:147], v[172:175], a[60:63], v[144:147]\n"
"ds_read_b64_tr_b16 v[172:173], v94 offset:49152\n"
"v_cvt_pk_bf16_f32 v96, v128, v129\n"
"s_waitcnt lgkmcnt(5)\n"
"v_mfma_f32_16x16x32_bf16 v[148:151], v[176:179], v[192:195], 0\n"
"v_cvt_pk_bf16_f32 v97, v130, v131\n"
"v_cvt_pk_bf16_f32 v98, v132, v133\n"
"v_mfma_f32_16x16x32_bf16 v[152:155], v[176:179], v[208:211], 0\n"
"buffer_load_dwordx4 v8, s[24:27], s72 offen lds\n"
"v_mfma_f32_16x16x32_bf16 v[156:159], v[176:179], v[224:227], 0\n"
"v_cvt_pk_bf16_f32 v104, v144, v145\n"
"v_cvt_pk_bf16_f32 v105, v146, v147\n"
"v_mfma_f32_16x16x32_bf16 v[160:163], v[176:179], v[240:243], 0\n"
"v_cvt_pk_bf16_f32 v99, v134, v135\n"
"v_cvt_pk_bf16_f32 v100, v136, v137\n"
"v_mfma_f32_16x16x32_bf16 v[164:167], v[176:179], v[64:67], 0\n"
"v_cvt_pk_bf16_f32 v101, v138, v139\n"
"v_cvt_pk_bf16_f32 v102, v140, v141\n"
"v_mfma_f32_16x16x32_bf16 v[148:151], v[180:183], a[64:67], v[148:151]\n"
"buffer_load_dwordx4 v9, s[24:27], s72 offen offset:1024 lds\n"
"v_mfma_f32_16x16x32_bf16 v[152:155], v[180:183], a[68:71], v[152:155]\n"
"v_cvt_pk_bf16_f32 v103, v142, v143\n"
"ds_read_b64_tr_b16 v[174:175], v94 offset:49664\n"
"v_mfma_f32_16x16x32_bf16 v[156:159], v[180:183], a[160:163], v[156:159]\n"
"v_permlane32_swap_b32 v104, v106\n"
"v_mfma_f32_16x16x32_bf16 v[160:163], v[180:183], a[224:227], v[160:163]\n"
"v_permlane32_swap_b32 v105, v107\n"
"v_mfma_f32_16x16x32_bf16 v[164:167], v[180:183], a[228:231], v[164:167]\n"
"ds_read_b64_tr_b16 v[176:177], v95 offset:49152\n"
"ds_read_b64_tr_b16 v[178:179], v95 offset:49664\n"
"v_mfma_f32_16x16x32_bf16 v[148:151], a[184:187], a[72:75], v[148:151]\n"
"buffer_load_dwordx4 v10, s[24:27], s72 offen offset:2048 lds\n"
"v_mfma_f32_16x16x32_bf16 v[152:155], a[184:187], a[76:79], v[152:155]\n"
"ds_read_b64_tr_b16 v[180:181], v92 offset:16384\n"
"ds_read_b64_tr_b16 v[182:183], v92 offset:16896\n"
"v_mfma_f32_16x16x32_bf16 v[156:159], a[184:187], a[176:179], v[156:159]\n"
"v_add_u32 v92, 4294918144, v92\n"
"v_mfma_f32_16x16x32_bf16 v[160:163], a[184:187], a[0:3], v[160:163]\n"
"v_mfma_f32_16x16x32_bf16 v[164:167], a[184:187], a[4:7], v[164:167]\n"
"buffer_load_dwordx4 v11, s[24:27], s72 offen offset:3072 lds\n"
"v_mfma_f32_16x16x32_bf16 v[148:151], a[188:191], a[80:83], v[148:151]\n"
"s_add_u32 m0, s69, 32768\n"
"s_add_u32 s72, s57, 32768\n"
"ds_read_b64_tr_b16 a[184:185], v93 offset:16384\n"
"v_mfma_f32_16x16x32_bf16 v[152:155], a[188:191], a[84:87], v[152:155]\n"
"ds_read_b64_tr_b16 a[186:187], v93 offset:16896\n"
"v_add_u32 v93, 4294918144, v93\n"
"v_mfma_f32_16x16x32_bf16 v[156:159], a[188:191], a[208:211], v[156:159]\n"
"v_mfma_f32_16x16x32_bf16 v[160:163], a[188:191], a[32:35], v[160:163]\n"
"v_mfma_f32_16x16x32_bf16 v[164:167], a[188:191], a[36:39], v[164:167]\n"
"ds_read_b64_tr_b16 a[188:189], v94 offset:16384\n"
"ds_read_b64_tr_b16 a[190:191], v94 offset:16896\n"
"v_mfma_f32_16x16x32_bf16 v[148:151], a[192:195], v[196:199], v[148:151]\n"
"v_add_u32 v94, 4294918144, v94\n"
"v_mfma_f32_16x16x32_bf16 v[152:155], a[192:195], v[212:215], v[152:155]\n"
"v_mfma_f32_16x16x32_bf16 v[156:159], a[192:195], v[228:231], v[156:159]\n"
"buffer_store_dwordx4 v[96:99], v0, s[28:31], s62 offen sc1\n"
"v_mfma_f32_16x16x32_bf16 v[160:163], a[192:195], v[244:247], v[160:163]\n"
"v_mfma_f32_16x16x32_bf16 v[164:167], a[192:195], v[68:71], v[164:167]\n"
"ds_read_b64_tr_b16 a[192:193], v95 offset:16384\n"
"ds_read_b64_tr_b16 a[194:195], v95 offset:16896\n"
"v_mfma_f32_16x16x32_bf16 v[148:151], a[196:199], a[88:91], v[148:151]\n"
"v_add_u32 v95, 4294918144, v95\n"
"v_mfma_f32_16x16x32_bf16 v[152:155], a[196:199], a[92:95], v[152:155]\n"
"v_mfma_f32_16x16x32_bf16 v[156:159], a[196:199], a[164:167], v[156:159]\n"
"v_mfma_f32_16x16x32_bf16 v[160:163], a[196:199], a[232:235], v[160:163]\n"
"v_mfma_f32_16x16x32_bf16 v[164:167], a[196:199], a[236:239], v[164:167]\n"
"buffer_store_dwordx4 v[100:103], v0, s[28:31], s62 offen offset:1024 sc1\n"
"v_mfma_f32_16x16x32_bf16 v[148:151], v[80:83], a[96:99], v[148:151]\n"
"v_mfma_f32_16x16x32_bf16 v[152:155], v[80:83], a[100:103], v[152:155]\n"
"v_mfma_f32_16x16x32_bf16 v[156:159], v[80:83], a[180:183], v[156:159]\n"
"v_mfma_f32_16x16x32_bf16 v[160:163], v[80:83], a[8:11], v[160:163]\n"
"buffer_store_dwordx4 v[104:107], v1, s[28:31], s62 offen offset:2048 sc1\n"
"v_mfma_f32_16x16x32_bf16 v[164:167], v[80:83], a[12:15], v[164:167]\n"
"v_mfma_f32_16x16x32_bf16 v[148:151], v[84:87], a[104:107], v[148:151]\n"
"v_mfma_f32_16x16x32_bf16 v[152:155], v[84:87], a[108:111], v[152:155]\n"
"buffer_load_dwordx4 v8, s[24:27], s57 offen lds\n"
"v_mfma_f32_16x16x32_bf16 v[156:159], v[84:87], a[212:215], v[156:159]\n"
"v_mfma_f32_16x16x32_bf16 v[160:163], v[84:87], a[40:43], v[160:163]\n"
"v_mfma_f32_16x16x32_bf16 v[164:167], v[84:87], a[44:47], v[164:167]\n"
"s_waitcnt lgkmcnt(0)\n"
"v_mfma_f32_16x16x32_bf16 v[148:151], v[88:91], v[200:203], v[148:151]\n"
"buffer_load_dwordx4 v9, s[24:27], s57 offen offset:1024 lds\n"
"v_mfma_f32_16x16x32_bf16 v[152:155], v[88:91], v[216:219], v[152:155]\n"
"v_mfma_f32_16x16x32_bf16 v[156:159], v[88:91], v[232:235], v[156:159]\n"
"v_mfma_f32_16x16x32_bf16 v[160:163], v[88:91], v[248:251], v[160:163]\n"
"v_mfma_f32_16x16x32_bf16 v[164:167], v[88:91], v[72:75], v[164:167]\n"
"v_mfma_f32_16x16x32_bf16 v[148:151], v[168:171], a[112:115], v[148:151]\n"
"v_mfma_f32_16x16x32_bf16 v[152:155], v[168:171], a[116:119], v[152:155]\n"
"v_mfma_f32_16x16x32_bf16 v[156:159], v[168:171], a[168:171], v[156:159]\n"
"v_mfma_f32_16x16x32_bf16 v[160:163], v[168:171], a[240:243], v[160:163]\n"
"v_mfma_f32_16x16x32_bf16 v[164:167], v[168:171], a[244:247], v[164:167]\n"
"buffer_load_dwordx4 v10, s[24:27], s57 offen offset:2048 lds\n"
"v_mfma_f32_16x16x32_bf16 v[148:151], v[172:175], a[120:123], v[148:151]\n"
"buffer_load_dwordx4 v11, s[24:27], s57 offen offset:3072 lds\n"
"v_mfma_f32_16x16x32_bf16 v[152:155], v[172:175], a[124:127], v[152:155]\n"
"s_add_u32 m0, s69, 49152\n"
"v_mfma_f32_16x16x32_bf16 v[156:159], v[172:175], a[200:203], v[156:159]\n"
"v_mfma_f32_16x16x32_bf16 v[160:163], v[172:175], a[16:19], v[160:163]\n"
"s_waitcnt vmcnt(7)\n"
"s_barrier\n"
"ds_read_b64_tr_b16 a[196:197], v92 offset:32768\n"
"ds_read_b64_tr_b16 a[198:199], v92 offset:33280\n"
"v_mfma_f32_16x16x32_bf16 v[164:167], v[172:175], a[20:23], v[164:167]\n"
"ds_read_b64_tr_b16 v[80:81], v93 offset:32768\n"
"ds_read_b64_tr_b16 v[82:83], v93 offset:33280\n"
"v_mfma_f32_16x16x32_bf16 v[148:151], v[176:179], a[128:131], v[148:151]\n"
"ds_read_b64_tr_b16 v[84:85], v94 offset:32768\n"
"ds_read_b64_tr_b16 v[86:87], v94 offset:33280\n"
"v_mfma_f32_16x16x32_bf16 v[152:155], v[176:179], a[132:135], v[152:155]\n"
"ds_read_b64_tr_b16 v[88:89], v95 offset:32768\n"
"ds_read_b64_tr_b16 v[90:91], v95 offset:33280\n"
"v_mfma_f32_16x16x32_bf16 v[156:159], v[176:179], a[216:219], v[156:159]\n"
"buffer_load_dwordx4 v8, s[24:27], s72 offen lds\n"
"v_mfma_f32_16x16x32_bf16 v[160:163], v[176:179], a[48:51], v[160:163]\n"
"buffer_load_dwordx4 v9, s[24:27], s72 offen offset:1024 lds\n"
"v_mfma_f32_16x16x32_bf16 v[164:167], v[176:179], a[52:55], v[164:167]\n"
"ds_read_b64_tr_b16 v[168:169], v92 offset:49152\n"
"ds_read_b64_tr_b16 v[170:171], v92 offset:49664\n"
"v_mfma_f32_16x16x32_bf16 v[148:151], v[180:183], v[204:207], v[148:151]\n"
"ds_read_b64_tr_b16 v[172:173], v93 offset:49152\n"
"ds_read_b64_tr_b16 v[174:175], v93 offset:49664\n"
"v_mfma_f32_16x16x32_bf16 v[152:155], v[180:183], v[220:223], v[152:155]\n"
"buffer_load_dwordx4 v10, s[24:27], s72 offen offset:2048 lds\n"
"v_mfma_f32_16x16x32_bf16 v[156:159], v[180:183], v[236:239], v[156:159]\n"
"ds_read_b64_tr_b16 v[176:177], v94 offset:49152\n"
"ds_read_b64_tr_b16 v[178:179], v94 offset:49664\n"
"v_mfma_f32_16x16x32_bf16 v[160:163], v[180:183], v[252:255], v[160:163]\n"
"v_add_u32 v92, 4294918144, v92\n"
"v_add_u32 v93, 4294918144, v93\n"
"v_mfma_f32_16x16x32_bf16 v[164:167], v[180:183], v[76:79], v[164:167]\n"
"ds_read_b64_tr_b16 v[180:181], v95 offset:49152\n"
"ds_read_b64_tr_b16 v[182:183], v95 offset:49664\n"
"v_mfma_f32_16x16x32_bf16 v[148:151], a[184:187], a[136:139], v[148:151]\n"
"buffer_load_dwordx4 v11, s[24:27], s72 offen offset:3072 lds\n"
"v_mfma_f32_16x16x32_bf16 v[152:155], a[184:187], a[140:143], v[152:155]\n"
"s_add_u32 m0, s69, 65536\n"
"s_add_u32 s72, s57, 65536\n"
"v_add_u32 v94, 4294918144, v94\n"
"v_mfma_f32_16x16x32_bf16 v[156:159], a[184:187], a[172:175], v[156:159]\n"
"v_add_u32 v95, 4294918144, v95\n"
"v_mfma_f32_16x16x32_bf16 v[160:163], a[184:187], a[248:251], v[160:163]\n"
"v_mfma_f32_16x16x32_bf16 v[164:167], a[184:187], a[252:255], v[164:167]\n"
"buffer_load_dwordx4 v8, s[24:27], s72 offen lds\n"
"v_mfma_f32_16x16x32_bf16 v[148:151], a[188:191], a[144:147], v[148:151]\n"
"ds_read_b64_tr_b16 a[184:185], v92\n"
"ds_read_b64_tr_b16 a[186:187], v92 offset:512\n"
"v_mfma_f32_16x16x32_bf16 v[152:155], a[188:191], a[148:151], v[152:155]\n"
"v_mfma_f32_16x16x32_bf16 v[156:159], a[188:191], a[204:207], v[156:159]\n"
"v_mfma_f32_16x16x32_bf16 v[160:163], a[188:191], a[24:27], v[160:163]\n"
"buffer_load_dwordx4 v9, s[24:27], s72 offen offset:1024 lds\n"
"v_mfma_f32_16x16x32_bf16 v[164:167], a[188:191], a[28:31], v[164:167]\n"
"ds_read_b64_tr_b16 a[188:189], v93\n"
"ds_read_b64_tr_b16 a[190:191], v93 offset:512\n"
"v_mfma_f32_16x16x32_bf16 v[148:151], a[192:195], a[152:155], v[148:151]\n"
"v_mfma_f32_16x16x32_bf16 v[152:155], a[192:195], a[156:159], v[152:155]\n"
"v_mfma_f32_16x16x32_bf16 v[156:159], a[192:195], a[220:223], v[156:159]\n"
"buffer_load_dwordx4 v10, s[24:27], s72 offen offset:2048 lds\n"
"v_mfma_f32_16x16x32_bf16 v[160:163], a[192:195], a[56:59], v[160:163]\n"
"v_mfma_f32_16x16x32_bf16 v[164:167], a[192:195], a[60:63], v[164:167]\n"
"ds_read_b64_tr_b16 a[192:193], v94\n"
"ds_read_b64_tr_b16 a[194:195], v94 offset:512\n"
"s_waitcnt lgkmcnt(6)\n"
"v_mfma_f32_16x16x32_bf16 v[128:131], a[196:199], v[192:195], 0\n"
"v_cvt_pk_bf16_f32 v96, v148, v149\n"
"v_cvt_pk_bf16_f32 v97, v150, v151\n"
"v_mfma_f32_16x16x32_bf16 v[132:135], a[196:199], v[208:211], 0\n"
"v_cvt_pk_bf16_f32 v98, v152, v153\n"
"v_cvt_pk_bf16_f32 v99, v154, v155\n"
"v_mfma_f32_16x16x32_bf16 v[136:139], a[196:199], v[224:227], 0\n"
"buffer_load_dwordx4 v11, s[24:27], s72 offen offset:3072 lds\n"
"v_mfma_f32_16x16x32_bf16 v[140:143], a[196:199], v[240:243], 0\n"
"s_add_u32 m0, s69, 131072\n"
"s_add_u32 s72, s57, 98304\n"
"v_cvt_pk_bf16_f32 v104, v164, v165\n"
"v_mfma_f32_16x16x32_bf16 v[144:147], a[196:199], v[64:67], 0\n"
"v_cvt_pk_bf16_f32 v105, v166, v167\n"
"v_cvt_pk_bf16_f32 v100, v156, v157\n"
"v_mfma_f32_16x16x32_bf16 v[128:131], v[80:83], a[64:67], v[128:131]\n"
"v_cvt_pk_bf16_f32 v101, v158, v159\n"
"v_cvt_pk_bf16_f32 v102, v160, v161\n"
"v_mfma_f32_16x16x32_bf16 v[132:135], v[80:83], a[68:71], v[132:135]\n"
"buffer_load_dwordx4 v8, s[24:27], s72 offen lds\n"
"v_mfma_f32_16x16x32_bf16 v[136:139], v[80:83], a[160:163], v[136:139]\n"
"v_cvt_pk_bf16_f32 v103, v162, v163\n"
"ds_read_b64_tr_b16 a[196:197], v95\n"
"v_mfma_f32_16x16x32_bf16 v[140:143], v[80:83], a[224:227], v[140:143]\n"
"v_permlane32_swap_b32 v104, v106\n"
"v_mfma_f32_16x16x32_bf16 v[144:147], v[80:83], a[228:231], v[144:147]\n"
"v_permlane32_swap_b32 v105, v107\n"
"v_mfma_f32_16x16x32_bf16 v[128:131], v[84:87], a[72:75], v[128:131]\n"
"buffer_load_dwordx4 v9, s[24:27], s72 offen offset:1024 lds\n"
"v_mfma_f32_16x16x32_bf16 v[132:135], v[84:87], a[76:79], v[132:135]\n"
"ds_read_b64_tr_b16 a[198:199], v95 offset:512\n"
"ds_read_b64_tr_b16 v[80:81], v92 offset:16384\n"
"v_mfma_f32_16x16x32_bf16 v[136:139], v[84:87], a[176:179], v[136:139]\n"
"ds_read_b64_tr_b16 v[82:83], v92 offset:16896\n"
"v_mfma_f32_16x16x32_bf16 v[140:143], v[84:87], a[0:3], v[140:143]\n"
"v_mfma_f32_16x16x32_bf16 v[144:147], v[84:87], a[4:7], v[144:147]\n"
"buffer_load_dwordx4 v10, s[24:27], s72 offen offset:2048 lds\n"
"v_mfma_f32_16x16x32_bf16 v[128:131], v[88:91], a[80:83], v[128:131]\n"
"ds_read_b64_tr_b16 v[84:85], v93 offset:16384\n"
"ds_read_b64_tr_b16 v[86:87], v93 offset:16896\n"
"v_mfma_f32_16x16x32_bf16 v[132:135], v[88:91], a[84:87], v[132:135]\n"
"v_mfma_f32_16x16x32_bf16 v[136:139], v[88:91], a[208:211], v[136:139]\n"
"v_mfma_f32_16x16x32_bf16 v[140:143], v[88:91], a[32:35], v[140:143]\n"
"buffer_load_dwordx4 v11, s[24:27], s72 offen offset:3072 lds\n"
"v_mfma_f32_16x16x32_bf16 v[144:147], v[88:91], a[36:39], v[144:147]\n"
"s_add_u32 m0, s69, 147456\n"
"s_add_u32 s72, s58, 32768\n"
"ds_read_b64_tr_b16 v[88:89], v94 offset:16384\n"
"v_mfma_f32_16x16x32_bf16 v[128:131], v[168:171], v[196:199], v[128:131]\n"
"ds_read_b64_tr_b16 v[90:91], v94 offset:16896\n"
"v_mfma_f32_16x16x32_bf16 v[132:135], v[168:171], v[212:215], v[132:135]\n"
"v_mfma_f32_16x16x32_bf16 v[136:139], v[168:171], v[228:231], v[136:139]\n"
"v_mfma_f32_16x16x32_bf16 v[140:143], v[168:171], v[244:247], v[140:143]\n"
"v_mfma_f32_16x16x32_bf16 v[144:147], v[168:171], v[68:71], v[144:147]\n"
"ds_read_b64_tr_b16 v[168:169], v95 offset:16384\n"
"ds_read_b64_tr_b16 v[170:171], v95 offset:16896\n"
"v_mfma_f32_16x16x32_bf16 v[128:131], v[172:175], a[88:91], v[128:131]\n"
"v_mfma_f32_16x16x32_bf16 v[132:135], v[172:175], a[92:95], v[132:135]\n"
"buffer_store_dwordx4 v[96:99], v0, s[28:31], s63 offen sc1\n"
"v_mfma_f32_16x16x32_bf16 v[136:139], v[172:175], a[164:167], v[136:139]\n"
"v_mfma_f32_16x16x32_bf16 v[140:143], v[172:175], a[232:235], v[140:143]\n"
"v_mfma_f32_16x16x32_bf16 v[144:147], v[172:175], a[236:239], v[144:147]\n"
"v_mfma_f32_16x16x32_bf16 v[128:131], v[176:179], a[96:99], v[128:131]\n"
"v_mfma_f32_16x16x32_bf16 v[132:135], v[176:179], a[100:103], v[132:135]\n"
"v_mfma_f32_16x16x32_bf16 v[136:139], v[176:179], a[180:183], v[136:139]\n"
"v_mfma_f32_16x16x32_bf16 v[140:143], v[176:179], a[8:11], v[140:143]\n"
"v_mfma_f32_16x16x32_bf16 v[144:147], v[176:179], a[12:15], v[144:147]\n"
"buffer_store_dwordx4 v[100:103], v0, s[28:31], s63 offen offset:1024 sc1\n"
"v_mfma_f32_16x16x32_bf16 v[128:131], v[180:183], a[104:107], v[128:131]\n"
"v_mfma_f32_16x16x32_bf16 v[132:135], v[180:183], a[108:111], v[132:135]\n"
"v_mfma_f32_16x16x32_bf16 v[136:139], v[180:183], a[212:215], v[136:139]\n"
"buffer_store_dwordx4 v[104:107], v1, s[28:31], s63 offen offset:2048 sc1\n"
"v_mfma_f32_16x16x32_bf16 v[140:143], v[180:183], a[40:43], v[140:143]\n"
"s_waitcnt vmcnt(19) lgkmcnt(0)\n"
"buffer_store_dword v31, v191, s[36:39], s71 offen offset:128 sc1\n"
"v_mfma_f32_16x16x32_bf16 v[144:147], v[180:183], a[44:47], v[144:147]\n"
"s_lshl_b32 s71, s55, 1\n"
"v_mfma_f32_16x16x32_bf16 v[128:131], a[184:187], v[200:203], v[128:131]\n"
"v_mfma_f32_16x16x32_bf16 v[132:135], a[184:187], v[216:219], v[132:135]\n"
"buffer_load_dwordx4 v8, s[24:27], s58 offen lds\n"
"v_mfma_f32_16x16x32_bf16 v[136:139], a[184:187], v[232:235], v[136:139]\n"
"v_mfma_f32_16x16x32_bf16 v[140:143], a[184:187], v[248:251], v[140:143]\n"
"v_mfma_f32_16x16x32_bf16 v[144:147], a[184:187], v[72:75], v[144:147]\n"
"v_mfma_f32_16x16x32_bf16 v[128:131], a[188:191], a[112:115], v[128:131]\n"
"buffer_load_dwordx4 v9, s[24:27], s58 offen offset:1024 lds\n"
"v_mfma_f32_16x16x32_bf16 v[132:135], a[188:191], a[116:119], v[132:135]\n"
"v_mfma_f32_16x16x32_bf16 v[136:139], a[188:191], a[168:171], v[136:139]\n"
"v_mfma_f32_16x16x32_bf16 v[140:143], a[188:191], a[240:243], v[140:143]\n"
"v_mfma_f32_16x16x32_bf16 v[144:147], a[188:191], a[244:247], v[144:147]\n"
"v_mfma_f32_16x16x32_bf16 v[128:131], a[192:195], a[120:123], v[128:131]\n"
"v_mfma_f32_16x16x32_bf16 v[132:135], a[192:195], a[124:127], v[132:135]\n"
"v_mfma_f32_16x16x32_bf16 v[136:139], a[192:195], a[200:203], v[136:139]\n"
"v_mfma_f32_16x16x32_bf16 v[140:143], a[192:195], a[16:19], v[140:143]\n"
"buffer_load_dwordx4 v10, s[24:27], s58 offen offset:2048 lds\n"
"v_mfma_f32_16x16x32_bf16 v[144:147], a[192:195], a[20:23], v[144:147]\n"
"buffer_load_dwordx4 v11, s[24:27], s58 offen offset:3072 lds\n"
"v_mfma_f32_16x16x32_bf16 v[128:131], a[196:199], a[128:131], v[128:131]\n"
"s_add_u32 m0, s69, 114688\n"
"v_mfma_f32_16x16x32_bf16 v[132:135], a[196:199], a[132:135], v[132:135]\n"
"v_mfma_f32_16x16x32_bf16 v[136:139], a[196:199], a[216:219], v[136:139]\n"
"s_waitcnt vmcnt(8)\n"
"s_barrier\n"
"ds_read_b64_tr_b16 v[172:173], v92 offset:32768\n"
"ds_read_b64_tr_b16 v[174:175], v92 offset:33280\n"
"v_mfma_f32_16x16x32_bf16 v[140:143], a[196:199], a[48:51], v[140:143]\n"
"ds_read_b64_tr_b16 v[176:177], v93 offset:32768\n"
"ds_read_b64_tr_b16 v[178:179], v93 offset:33280\n"
"v_mfma_f32_16x16x32_bf16 v[144:147], a[196:199], a[52:55], v[144:147]\n"
"ds_read_b64_tr_b16 v[180:181], v94 offset:32768\n"
"ds_read_b64_tr_b16 v[182:183], v94 offset:33280\n"
"v_mfma_f32_16x16x32_bf16 v[128:131], v[80:83], v[204:207], v[128:131]\n"
"ds_read_b64_tr_b16 a[184:185], v95 offset:32768\n"
"ds_read_b64_tr_b16 a[186:187], v95 offset:33280\n"
"v_mfma_f32_16x16x32_bf16 v[132:135], v[80:83], v[220:223], v[132:135]\n"
"buffer_load_dwordx4 v8, s[24:27], s72 offen lds\n"
"v_mfma_f32_16x16x32_bf16 v[136:139], v[80:83], v[236:239], v[136:139]\n"
"buffer_load_dwordx4 v9, s[24:27], s72 offen offset:1024 lds\n"
"v_mfma_f32_16x16x32_bf16 v[140:143], v[80:83], v[252:255], v[140:143]\n"
"ds_read_b64_tr_b16 a[188:189], v92 offset:49152\n"
"ds_read_b64_tr_b16 a[190:191], v92 offset:49664\n"
"v_mfma_f32_16x16x32_bf16 v[144:147], v[80:83], v[76:79], v[144:147]\n"
"ds_read_b64_tr_b16 a[192:193], v93 offset:49152\n"
"ds_read_b64_tr_b16 a[194:195], v93 offset:49664\n"
"v_mfma_f32_16x16x32_bf16 v[128:131], v[84:87], a[136:139], v[128:131]\n"
"buffer_load_dwordx4 v10, s[24:27], s72 offen offset:2048 lds\n"
"v_mfma_f32_16x16x32_bf16 v[132:135], v[84:87], a[140:143], v[132:135]\n"
"ds_read_b64_tr_b16 a[196:197], v94 offset:49152\n"
"ds_read_b64_tr_b16 a[198:199], v94 offset:49664\n"
"v_mfma_f32_16x16x32_bf16 v[136:139], v[84:87], a[172:175], v[136:139]\n"
"ds_read_b64_tr_b16 v[80:81], v95 offset:49152\n"
"ds_read_b64_tr_b16 v[82:83], v95 offset:49664\n"
"v_mfma_f32_16x16x32_bf16 v[140:143], v[84:87], a[248:251], v[140:143]\n"
"v_add_u32 v92, 16384, v92\n"
"v_add_u32 v93, 16384, v93\n"
"v_mfma_f32_16x16x32_bf16 v[144:147], v[84:87], a[252:255], v[144:147]\n"
"ds_read_b64_tr_b16 v[84:85], v92 offset:49152\n"
"ds_read_b64_tr_b16 v[86:87], v92 offset:49664\n"
"v_mfma_f32_16x16x32_bf16 v[128:131], v[88:91], a[144:147], v[128:131]\n"
"buffer_load_dwordx4 v11, s[24:27], s72 offen offset:3072 lds\n"
"v_mfma_f32_16x16x32_bf16 v[132:135], v[88:91], a[148:151], v[132:135]\n"
"s_add_u32 m0, s69, 81920\n"
"s_add_u32 s72, s58, 65536\n"
"v_add_u32 v94, 16384, v94\n"
"v_mfma_f32_16x16x32_bf16 v[136:139], v[88:91], a[204:207], v[136:139]\n"
"v_add_u32 v95, 16384, v95\n"
"v_add_u32 v92, 81920, v92\n"
"v_mfma_f32_16x16x32_bf16 v[140:143], v[88:91], a[24:27], v[140:143]\n"
"v_mfma_f32_16x16x32_bf16 v[144:147], v[88:91], a[28:31], v[144:147]\n"
"buffer_load_dwordx4 v8, s[24:27], s72 offen lds\n"
"v_mfma_f32_16x16x32_bf16 v[128:131], v[168:171], a[152:155], v[128:131]\n"
"ds_read_b64_tr_b16 v[88:89], v93 offset:49152\n"
"ds_read_b64_tr_b16 v[90:91], v93 offset:49664\n"
"v_mfma_f32_16x16x32_bf16 v[132:135], v[168:171], a[156:159], v[132:135]\n"
"v_add_u32 v93, 81920, v93\n"
"v_mfma_f32_16x16x32_bf16 v[136:139], v[168:171], a[220:223], v[136:139]\n"
"v_mfma_f32_16x16x32_bf16 v[140:143], v[168:171], a[56:59], v[140:143]\n"
"buffer_load_dwordx4 v9, s[24:27], s72 offen offset:1024 lds\n"
"v_mfma_f32_16x16x32_bf16 v[144:147], v[168:171], a[60:63], v[144:147]\n"
"s_waitcnt lgkmcnt(5)\n"
"v_cvt_pk_bf16_f32 v96, v128, v129\n"
"v_cvt_pk_bf16_f32 v97, v130, v131\n"
"v_mfma_f32_16x16x32_bf16 v[148:151], v[172:175], v[192:195], 0\n"
"v_cvt_pk_bf16_f32 v98, v132, v133\n"
"v_cvt_pk_bf16_f32 v99, v134, v135\n"
"v_mfma_f32_16x16x32_bf16 v[152:155], v[172:175], v[208:211], 0\n"
"v_cvt_pk_bf16_f32 v100, v136, v137\n"
"v_cvt_pk_bf16_f32 v101, v138, v139\n"
"v_mfma_f32_16x16x32_bf16 v[156:159], v[172:175], v[224:227], 0\n"
"buffer_load_dwordx4 v10, s[24:27], s72 offen offset:2048 lds\n"
"v_mfma_f32_16x16x32_bf16 v[160:163], v[172:175], v[240:243], 0\n"
"v_cvt_pk_bf16_f32 v104, v144, v145\n"
"v_cvt_pk_bf16_f32 v105, v146, v147\n"
"v_mfma_f32_16x16x32_bf16 v[164:167], v[172:175], v[64:67], 0\n"
"v_cvt_pk_bf16_f32 v102, v140, v141\n"
"v_cvt_pk_bf16_f32 v103, v142, v143\n"
"v_mfma_f32_16x16x32_bf16 v[148:151], v[176:179], a[64:67], v[148:151]\n"
"v_permlane32_swap_b32 v104, v106\n"
"v_mfma_f32_16x16x32_bf16 v[152:155], v[176:179], a[68:71], v[152:155]\n"
"buffer_load_dwordx4 v11, s[24:27], s72 offen offset:3072 lds\n"
"v_mfma_f32_16x16x32_bf16 v[156:159], v[176:179], a[160:163], v[156:159]\n"
"s_add_u32 m0, s69, 98304\n"
"s_add_u32 s72, s58, 98304\n"
"ds_read_b64_tr_b16 v[168:169], v94 offset:49152\n"
"v_mfma_f32_16x16x32_bf16 v[160:163], v[176:179], a[224:227], v[160:163]\n"
"v_permlane32_swap_b32 v105, v107\n"
"v_mfma_f32_16x16x32_bf16 v[164:167], v[176:179], a[228:231], v[164:167]\n"
"ds_read_b64_tr_b16 v[170:171], v94 offset:49664\n"
"ds_read_b64_tr_b16 v[172:173], v95 offset:49152\n"
"v_mfma_f32_16x16x32_bf16 v[148:151], v[180:183], a[72:75], v[148:151]\n"
"ds_read_b64_tr_b16 v[174:175], v95 offset:49664\n"
"ds_read_b64_tr_b16 v[176:177], v92 offset:32768\n"
"v_mfma_f32_16x16x32_bf16 v[152:155], v[180:183], a[76:79], v[152:155]\n"
"buffer_load_dwordx4 v8, s[24:27], s72 offen lds\n"
"v_mfma_f32_16x16x32_bf16 v[156:159], v[180:183], a[176:179], v[156:159]\n"
"ds_read_b64_tr_b16 v[178:179], v92 offset:33280\n"
"v_add_u32 v94, 81920, v94\n"
"v_mfma_f32_16x16x32_bf16 v[160:163], v[180:183], a[0:3], v[160:163]\n"
"v_add_u32 v95, 81920, v95\n"
"v_mfma_f32_16x16x32_bf16 v[164:167], v[180:183], a[4:7], v[164:167]\n"
"ds_read_b64_tr_b16 v[180:181], v93 offset:32768\n"
"ds_read_b64_tr_b16 v[182:183], v93 offset:33280\n"
"v_mfma_f32_16x16x32_bf16 v[148:151], a[184:187], a[80:83], v[148:151]\n"
"buffer_load_dwordx4 v9, s[24:27], s72 offen offset:1024 lds\n"
"v_mfma_f32_16x16x32_bf16 v[152:155], a[184:187], a[84:87], v[152:155]\n"
"v_mfma_f32_16x16x32_bf16 v[156:159], a[184:187], a[208:211], v[156:159]\n"
"v_mfma_f32_16x16x32_bf16 v[160:163], a[184:187], a[32:35], v[160:163]\n"
"v_mfma_f32_16x16x32_bf16 v[164:167], a[184:187], a[36:39], v[164:167]\n"
"buffer_load_dwordx4 v10, s[24:27], s72 offen offset:2048 lds\n"
"v_mfma_f32_16x16x32_bf16 v[148:151], a[188:191], v[196:199], v[148:151]\n"
"ds_read_b64_tr_b16 a[184:185], v94 offset:32768\n"
"ds_read_b64_tr_b16 a[186:187], v94 offset:33280\n"
"v_mfma_f32_16x16x32_bf16 v[152:155], a[188:191], v[212:215], v[152:155]\n"
"v_mfma_f32_16x16x32_bf16 v[156:159], a[188:191], v[228:231], v[156:159]\n"
"v_mfma_f32_16x16x32_bf16 v[160:163], a[188:191], v[244:247], v[160:163]\n"
"buffer_load_dwordx4 v11, s[24:27], s72 offen offset:3072 lds\n"
"v_mfma_f32_16x16x32_bf16 v[164:167], a[188:191], v[68:71], v[164:167]\n"
"s_add_u32 m0, s69, 0\n"
"s_add_u32 s72, s59, 32768\n"
"ds_read_b64_tr_b16 a[188:189], v95 offset:32768\n"
"v_mfma_f32_16x16x32_bf16 v[148:151], a[192:195], a[88:91], v[148:151]\n"
"ds_read_b64_tr_b16 a[190:191], v95 offset:33280\n"
"v_mfma_f32_16x16x32_bf16 v[152:155], a[192:195], a[92:95], v[152:155]\n"
"v_mfma_f32_16x16x32_bf16 v[156:159], a[192:195], a[164:167], v[156:159]\n"
"v_mfma_f32_16x16x32_bf16 v[160:163], a[192:195], a[232:235], v[160:163]\n"
"v_mfma_f32_16x16x32_bf16 v[164:167], a[192:195], a[236:239], v[164:167]\n"
"v_mfma_f32_16x16x32_bf16 v[148:151], a[196:199], a[96:99], v[148:151]\n"
"v_mfma_f32_16x16x32_bf16 v[152:155], a[196:199], a[100:103], v[152:155]\n"
"buffer_store_dwordx4 v[96:99], v0, s[28:31], s64 offen sc1\n"
"v_mfma_f32_16x16x32_bf16 v[156:159], a[196:199], a[180:183], v[156:159]\n"
"v_mfma_f32_16x16x32_bf16 v[160:163], a[196:199], a[8:11], v[160:163]\n"
"v_mfma_f32_16x16x32_bf16 v[164:167], a[196:199], a[12:15], v[164:167]\n"
"s_waitcnt lgkmcnt(2)\n"
"v_mfma_f32_16x16x32_bf16 v[148:151], v[80:83], a[104:107], v[148:151]\n"
"v_mfma_f32_16x16x32_bf16 v[152:155], v[80:83], a[108:111], v[152:155]\n"
"v_mfma_f32_16x16x32_bf16 v[156:159], v[80:83], a[212:215], v[156:159]\n"
"v_mfma_f32_16x16x32_bf16 v[160:163], v[80:83], a[40:43], v[160:163]\n"
"v_mfma_f32_16x16x32_bf16 v[164:167], v[80:83], a[44:47], v[164:167]\n"
"buffer_store_dwordx4 v[100:103], v0, s[28:31], s64 offen offset:1024 sc1\n"
"v_mfma_f32_16x16x32_bf16 v[148:151], v[84:87], v[200:203], v[148:151]\n"
"v_mfma_f32_16x16x32_bf16 v[152:155], v[84:87], v[216:219], v[152:155]\n"
"v_mfma_f32_16x16x32_bf16 v[156:159], v[84:87], v[232:235], v[156:159]\n"
"buffer_store_dwordx4 v[104:107], v1, s[28:31], s64 offen offset:2048 sc1\n"
"v_mfma_f32_16x16x32_bf16 v[160:163], v[84:87], v[248:251], v[160:163]\n"
"s_waitcnt vmcnt(20) lgkmcnt(0)\n"
"buffer_store_dword v31, v191, s[36:39], s71 offen offset:132 sc1\n"
"v_mfma_f32_16x16x32_bf16 v[164:167], v[84:87], v[72:75], v[164:167]\n"
"s_lshl_b32 s71, s56, 1\n"
"v_mfma_f32_16x16x32_bf16 v[148:151], v[88:91], a[112:115], v[148:151]\n"
"buffer_load_dwordx4 v8, s[24:27], s59 offen lds\n"
"v_mfma_f32_16x16x32_bf16 v[152:155], v[88:91], a[116:119], v[152:155]\n"
"v_mfma_f32_16x16x32_bf16 v[156:159], v[88:91], a[168:171], v[156:159]\n"
"v_mfma_f32_16x16x32_bf16 v[160:163], v[88:91], a[240:243], v[160:163]\n"
"v_mfma_f32_16x16x32_bf16 v[164:167], v[88:91], a[244:247], v[164:167]\n"
"v_mfma_f32_16x16x32_bf16 v[148:151], v[168:171], a[120:123], v[148:151]\n"
"buffer_load_dwordx4 v9, s[24:27], s59 offen offset:1024 lds\n"
"v_mfma_f32_16x16x32_bf16 v[152:155], v[168:171], a[124:127], v[152:155]\n"
"v_mfma_f32_16x16x32_bf16 v[156:159], v[168:171], a[200:203], v[156:159]\n"
"v_mfma_f32_16x16x32_bf16 v[160:163], v[168:171], a[16:19], v[160:163]\n"
"v_mfma_f32_16x16x32_bf16 v[164:167], v[168:171], a[20:23], v[164:167]\n"
"v_mfma_f32_16x16x32_bf16 v[148:151], v[172:175], a[128:131], v[148:151]\n"
"v_mfma_f32_16x16x32_bf16 v[152:155], v[172:175], a[132:135], v[152:155]\n"
"v_mfma_f32_16x16x32_bf16 v[156:159], v[172:175], a[216:219], v[156:159]\n"
"v_mfma_f32_16x16x32_bf16 v[160:163], v[172:175], a[48:51], v[160:163]\n"
"buffer_load_dwordx4 v10, s[24:27], s59 offen offset:2048 lds\n"
"v_mfma_f32_16x16x32_bf16 v[164:167], v[172:175], a[52:55], v[164:167]\n"
"buffer_load_dwordx4 v11, s[24:27], s59 offen offset:3072 lds\n"
"v_mfma_f32_16x16x32_bf16 v[148:151], v[176:179], v[204:207], v[148:151]\n"
"s_add_u32 m0, s69, 16384\n"
"v_mfma_f32_16x16x32_bf16 v[152:155], v[176:179], v[220:223], v[152:155]\n"
"v_mfma_f32_16x16x32_bf16 v[156:159], v[176:179], v[236:239], v[156:159]\n"
"s_waitcnt vmcnt(8)\n"
"s_barrier\n"
"ds_read_b64_tr_b16 a[192:193], v92 offset:49152\n"
"ds_read_b64_tr_b16 a[194:195], v92 offset:49664\n"
"v_mfma_f32_16x16x32_bf16 v[160:163], v[176:179], v[252:255], v[160:163]\n"
"ds_read_b64_tr_b16 a[196:197], v93 offset:49152\n"
"ds_read_b64_tr_b16 a[198:199], v93 offset:49664\n"
"v_mfma_f32_16x16x32_bf16 v[164:167], v[176:179], v[76:79], v[164:167]\n"
"ds_read_b64_tr_b16 v[80:81], v94 offset:49152\n"
"ds_read_b64_tr_b16 v[82:83], v94 offset:49664\n"
"v_mfma_f32_16x16x32_bf16 v[148:151], v[180:183], a[136:139], v[148:151]\n"
"ds_read_b64_tr_b16 v[84:85], v95 offset:49152\n"
"ds_read_b64_tr_b16 v[86:87], v95 offset:49664\n"
"v_mfma_f32_16x16x32_bf16 v[152:155], v[180:183], a[140:143], v[152:155]\n"
"buffer_load_dwordx4 v8, s[24:27], s72 offen lds\n"
"v_mfma_f32_16x16x32_bf16 v[156:159], v[180:183], a[172:175], v[156:159]\n"
"buffer_load_dwordx4 v9, s[24:27], s72 offen offset:1024 lds\n"
"v_mfma_f32_16x16x32_bf16 v[160:163], v[180:183], a[248:251], v[160:163]\n"
"ds_read_b64_tr_b16 v[88:89], v92 offset:16384\n"
"ds_read_b64_tr_b16 v[90:91], v92 offset:16896\n"
"v_mfma_f32_16x16x32_bf16 v[164:167], v[180:183], a[252:255], v[164:167]\n"
"ds_read_b64_tr_b16 v[168:169], v93 offset:16384\n"
"ds_read_b64_tr_b16 v[170:171], v93 offset:16896\n"
"v_mfma_f32_16x16x32_bf16 v[148:151], a[184:187], a[144:147], v[148:151]\n"
"buffer_load_dwordx4 v10, s[24:27], s72 offen offset:2048 lds\n"
"v_mfma_f32_16x16x32_bf16 v[152:155], a[184:187], a[148:151], v[152:155]\n"
"ds_read_b64_tr_b16 v[172:173], v94 offset:16384\n"
"ds_read_b64_tr_b16 v[174:175], v94 offset:16896\n"
"v_mfma_f32_16x16x32_bf16 v[156:159], a[184:187], a[204:207], v[156:159]\n"
"ds_read_b64_tr_b16 v[176:177], v95 offset:16384\n"
"ds_read_b64_tr_b16 v[178:179], v95 offset:16896\n"
"v_mfma_f32_16x16x32_bf16 v[160:163], a[184:187], a[24:27], v[160:163]\n"
"v_add_u32 v92, 4294918144, v92\n"
"ds_read_b64_tr_b16 v[180:181], v92 offset:32768\n"
"v_mfma_f32_16x16x32_bf16 v[164:167], a[184:187], a[28:31], v[164:167]\n"
"v_add_u32 v93, 4294918144, v93\n"
"ds_read_b64_tr_b16 v[182:183], v92 offset:33280\n"
"v_mfma_f32_16x16x32_bf16 v[148:151], a[188:191], a[152:155], v[148:151]\n"
"buffer_load_dwordx4 v11, s[24:27], s72 offen offset:3072 lds\n"
"v_mfma_f32_16x16x32_bf16 v[152:155], a[188:191], a[156:159], v[152:155]\n"
"s_add_u32 m0, s69, 32768\n"
"s_add_u32 s72, s59, 65536\n"
"ds_read_b64_tr_b16 a[184:185], v93 offset:32768\n"
"v_mfma_f32_16x16x32_bf16 v[156:159], a[188:191], a[220:223], v[156:159]\n"
"v_add_u32 v94, 4294918144, v94\n"
"ds_read_b64_tr_b16 a[186:187], v93 offset:33280\n"
"v_mfma_f32_16x16x32_bf16 v[160:163], a[188:191], a[56:59], v[160:163]\n"
"v_cvt_pk_bf16_f32 v96, v148, v149\n"
"v_cvt_pk_bf16_f32 v97, v150, v151\n"
"v_mfma_f32_16x16x32_bf16 v[164:167], a[188:191], a[60:63], v[164:167]\n"
"s_waitcnt lgkmcnt(12)\n"
"buffer_load_dwordx4 v8, s[24:27], s72 offen lds\n"
"v_mfma_f32_16x16x32_bf16 v[128:131], a[192:195], v[192:195], 0\n"
"v_cvt_pk_bf16_f32 v98, v152, v153\n"
"v_cvt_pk_bf16_f32 v99, v154, v155\n"
"v_mfma_f32_16x16x32_bf16 v[132:135], a[192:195], v[208:211], 0\n"
"v_cvt_pk_bf16_f32 v100, v156, v157\n"
"v_cvt_pk_bf16_f32 v101, v158, v159\n"
"v_mfma_f32_16x16x32_bf16 v[136:139], a[192:195], v[224:227], 0\n"
"v_cvt_pk_bf16_f32 v104, v164, v165\n"
"v_cvt_pk_bf16_f32 v105, v166, v167\n"
"v_mfma_f32_16x16x32_bf16 v[140:143], a[192:195], v[240:243], 0\n"
"buffer_load_dwordx4 v9, s[24:27], s72 offen offset:1024 lds\n"
"v_mfma_f32_16x16x32_bf16 v[144:147], a[192:195], v[64:67], 0\n"
"v_cvt_pk_bf16_f32 v102, v160, v161\n"
"v_cvt_pk_bf16_f32 v103, v162, v163\n"
"v_mfma_f32_16x16x32_bf16 v[128:131], a[196:199], a[64:67], v[128:131]\n"
"v_permlane32_swap_b32 v104, v106\n"
"v_mfma_f32_16x16x32_bf16 v[132:135], a[196:199], a[68:71], v[132:135]\n"
"v_permlane32_swap_b32 v105, v107\n"
"v_mfma_f32_16x16x32_bf16 v[136:139], a[196:199], a[160:163], v[136:139]\n"
"buffer_load_dwordx4 v10, s[24:27], s72 offen offset:2048 lds\n"
"v_mfma_f32_16x16x32_bf16 v[140:143], a[196:199], a[224:227], v[140:143]\n"
"ds_read_b64_tr_b16 a[188:189], v94 offset:32768\n"
"v_add_u32 v95, 4294918144, v95\n"
"v_mfma_f32_16x16x32_bf16 v[144:147], a[196:199], a[228:231], v[144:147]\n"
"ds_read_b64_tr_b16 a[190:191], v94 offset:33280\n"
"ds_read_b64_tr_b16 a[192:193], v95 offset:32768\n"
"v_mfma_f32_16x16x32_bf16 v[128:131], v[80:83], a[72:75], v[128:131]\n"
"ds_read_b64_tr_b16 a[194:195], v95 offset:33280\n"
"ds_read_b64_tr_b16 a[196:197], v92 offset:49152\n"
"v_mfma_f32_16x16x32_bf16 v[132:135], v[80:83], a[76:79], v[132:135]\n"
"buffer_load_dwordx4 v11, s[24:27], s72 offen offset:3072 lds\n"
"v_mfma_f32_16x16x32_bf16 v[136:139], v[80:83], a[176:179], v[136:139]\n"
"s_add_u32 m0, s69, 49152\n"
"s_add_u32 s72, s59, 98304\n"
"ds_read_b64_tr_b16 a[198:199], v92 offset:49664\n"
"v_mfma_f32_16x16x32_bf16 v[140:143], v[80:83], a[0:3], v[140:143]\n"
"v_add_u32 v92, 4294918144, v92\n"
"v_mfma_f32_16x16x32_bf16 v[144:147], v[80:83], a[4:7], v[144:147]\n"
"ds_read_b64_tr_b16 v[80:81], v93 offset:49152\n"
"ds_read_b64_tr_b16 v[82:83], v93 offset:49664\n"
"v_mfma_f32_16x16x32_bf16 v[128:131], v[84:87], a[80:83], v[128:131]\n"
"buffer_load_dwordx4 v8, s[24:27], s72 offen lds\n"
"v_mfma_f32_16x16x32_bf16 v[132:135], v[84:87], a[84:87], v[132:135]\n"
"v_add_u32 v93, 4294918144, v93\n"
"v_mfma_f32_16x16x32_bf16 v[136:139], v[84:87], a[208:211], v[136:139]\n"
"v_mfma_f32_16x16x32_bf16 v[140:143], v[84:87], a[32:35], v[140:143]\n"
"v_mfma_f32_16x16x32_bf16 v[144:147], v[84:87], a[36:39], v[144:147]\n"
"s_waitcnt lgkmcnt(8)\n"
"ds_read_b64_tr_b16 v[84:85], v94 offset:49152\n"
"ds_read_b64_tr_b16 v[86:87], v94 offset:49664\n"
"v_mfma_f32_16x16x32_bf16 v[128:131], v[88:91], v[196:199], v[128:131]\n"
"buffer_load_dwordx4 v9, s[24:27], s72 offen offset:1024 lds\n"
"v_mfma_f32_16x16x32_bf16 v[132:135], v[88:91], v[212:215], v[132:135]\n"
"v_add_u32 v94, 4294918144, v94\n"
"v_mfma_f32_16x16x32_bf16 v[136:139], v[88:91], v[228:231], v[136:139]\n"
"v_mfma_f32_16x16x32_bf16 v[140:143], v[88:91], v[244:247], v[140:143]\n"
"v_mfma_f32_16x16x32_bf16 v[144:147], v[88:91], v[68:71], v[144:147]\n"
"buffer_load_dwordx4 v10, s[24:27], s72 offen offset:2048 lds\n"
"v_mfma_f32_16x16x32_bf16 v[128:131], v[168:171], a[88:91], v[128:131]\n"
"ds_read_b64_tr_b16 v[88:89], v95 offset:49152\n"
"ds_read_b64_tr_b16 v[90:91], v95 offset:49664\n"
"v_mfma_f32_16x16x32_bf16 v[132:135], v[168:171], a[92:95], v[132:135]\n"
"v_add_u32 v95, 4294918144, v95\n"
"v_mfma_f32_16x16x32_bf16 v[136:139], v[168:171], a[164:167], v[136:139]\n"
"v_mfma_f32_16x16x32_bf16 v[140:143], v[168:171], a[232:235], v[140:143]\n"
"buffer_load_dwordx4 v11, s[24:27], s72 offen offset:3072 lds\n"
"v_mfma_f32_16x16x32_bf16 v[144:147], v[168:171], a[236:239], v[144:147]\n"
"s_add_u32 m0, s69, 65536\n"
"s_add_u32 s72, s60, 32768\n"
"v_mfma_f32_16x16x32_bf16 v[128:131], v[172:175], a[96:99], v[128:131]\n"
"v_mfma_f32_16x16x32_bf16 v[132:135], v[172:175], a[100:103], v[132:135]\n"
"v_mfma_f32_16x16x32_bf16 v[136:139], v[172:175], a[180:183], v[136:139]\n"
"v_mfma_f32_16x16x32_bf16 v[140:143], v[172:175], a[8:11], v[140:143]\n"
"v_mfma_f32_16x16x32_bf16 v[144:147], v[172:175], a[12:15], v[144:147]\n"
"v_mfma_f32_16x16x32_bf16 v[128:131], v[176:179], a[104:107], v[128:131]\n"
"v_mfma_f32_16x16x32_bf16 v[132:135], v[176:179], a[108:111], v[132:135]\n"
"buffer_store_dwordx4 v[96:99], v0, s[28:31], s65 offen sc1\n"
"v_mfma_f32_16x16x32_bf16 v[136:139], v[176:179], a[212:215], v[136:139]\n"
"v_mfma_f32_16x16x32_bf16 v[140:143], v[176:179], a[40:43], v[140:143]\n"
"v_mfma_f32_16x16x32_bf16 v[144:147], v[176:179], a[44:47], v[144:147]\n"
"v_mfma_f32_16x16x32_bf16 v[128:131], v[180:183], v[200:203], v[128:131]\n"
"v_mfma_f32_16x16x32_bf16 v[132:135], v[180:183], v[216:219], v[132:135]\n"
"v_mfma_f32_16x16x32_bf16 v[136:139], v[180:183], v[232:235], v[136:139]\n"
"v_mfma_f32_16x16x32_bf16 v[140:143], v[180:183], v[248:251], v[140:143]\n"
"v_mfma_f32_16x16x32_bf16 v[144:147], v[180:183], v[72:75], v[144:147]\n"
"buffer_store_dwordx4 v[100:103], v0, s[28:31], s65 offen offset:1024 sc1\n"
"v_mfma_f32_16x16x32_bf16 v[128:131], a[184:187], a[112:115], v[128:131]\n"
"v_mfma_f32_16x16x32_bf16 v[132:135], a[184:187], a[116:119], v[132:135]\n"
"v_mfma_f32_16x16x32_bf16 v[136:139], a[184:187], a[168:171], v[136:139]\n"
"buffer_store_dwordx4 v[104:107], v1, s[28:31], s65 offen offset:2048 sc1\n"
"v_mfma_f32_16x16x32_bf16 v[140:143], a[184:187], a[240:243], v[140:143]\n"
"s_waitcnt vmcnt(20) lgkmcnt(0)\n"
"buffer_store_dword v31, v191, s[36:39], s71 offen offset:136 sc1\n"
"v_mfma_f32_16x16x32_bf16 v[144:147], a[184:187], a[244:247], v[144:147]\n"
"s_lshl_b32 s71, s57, 1\n"
"v_mfma_f32_16x16x32_bf16 v[128:131], a[188:191], a[120:123], v[128:131]\n"
"buffer_load_dwordx4 v8, s[24:27], s60 offen lds\n"
"v_mfma_f32_16x16x32_bf16 v[132:135], a[188:191], a[124:127], v[132:135]\n"
"v_mfma_f32_16x16x32_bf16 v[136:139], a[188:191], a[200:203], v[136:139]\n"
"v_mfma_f32_16x16x32_bf16 v[140:143], a[188:191], a[16:19], v[140:143]\n"
"v_mfma_f32_16x16x32_bf16 v[144:147], a[188:191], a[20:23], v[144:147]\n"
"buffer_load_dwordx4 v9, s[24:27], s60 offen offset:1024 lds\n"
"v_mfma_f32_16x16x32_bf16 v[128:131], a[192:195], a[128:131], v[128:131]\n"
"v_mfma_f32_16x16x32_bf16 v[132:135], a[192:195], a[132:135], v[132:135]\n"
"v_mfma_f32_16x16x32_bf16 v[136:139], a[192:195], a[216:219], v[136:139]\n"
"v_mfma_f32_16x16x32_bf16 v[140:143], a[192:195], a[48:51], v[140:143]\n"
"v_mfma_f32_16x16x32_bf16 v[144:147], a[192:195], a[52:55], v[144:147]\n"
"v_mfma_f32_16x16x32_bf16 v[128:131], a[196:199], v[204:207], v[128:131]\n"
"v_mfma_f32_16x16x32_bf16 v[132:135], a[196:199], v[220:223], v[132:135]\n"
"v_mfma_f32_16x16x32_bf16 v[136:139], a[196:199], v[236:239], v[136:139]\n"
"v_mfma_f32_16x16x32_bf16 v[140:143], a[196:199], v[252:255], v[140:143]\n"
"buffer_load_dwordx4 v10, s[24:27], s60 offen offset:2048 lds\n"
"v_mfma_f32_16x16x32_bf16 v[144:147], a[196:199], v[76:79], v[144:147]\n"
"buffer_load_dwordx4 v11, s[24:27], s60 offen offset:3072 lds\n"
"v_mfma_f32_16x16x32_bf16 v[128:131], v[80:83], a[136:139], v[128:131]\n"
"s_add_u32 m0, s69, 131072\n"
"v_mfma_f32_16x16x32_bf16 v[132:135], v[80:83], a[140:143], v[132:135]\n"
"v_mfma_f32_16x16x32_bf16 v[136:139], v[80:83], a[172:175], v[136:139]\n"
"s_waitcnt vmcnt(8)\n"
"s_barrier\n"
"ds_read_b64_tr_b16 v[168:169], v92\n"
"ds_read_b64_tr_b16 v[170:171], v92 offset:512\n"
"v_mfma_f32_16x16x32_bf16 v[140:143], v[80:83], a[248:251], v[140:143]\n"
"ds_read_b64_tr_b16 v[172:173], v93\n"
"ds_read_b64_tr_b16 v[174:175], v93 offset:512\n"
"v_mfma_f32_16x16x32_bf16 v[144:147], v[80:83], a[252:255], v[144:147]\n"
"ds_read_b64_tr_b16 v[176:177], v94\n"
"ds_read_b64_tr_b16 v[178:179], v94 offset:512\n"
"v_mfma_f32_16x16x32_bf16 v[128:131], v[84:87], a[144:147], v[128:131]\n"
"ds_read_b64_tr_b16 v[180:181], v95\n"
"ds_read_b64_tr_b16 v[182:183], v95 offset:512\n"
"v_mfma_f32_16x16x32_bf16 v[132:135], v[84:87], a[148:151], v[132:135]\n"
"buffer_load_dwordx4 v8, s[24:27], s72 offen lds\n"
"v_mfma_f32_16x16x32_bf16 v[136:139], v[84:87], a[204:207], v[136:139]\n"
"buffer_load_dwordx4 v9, s[24:27], s72 offen offset:1024 lds\n"
"v_mfma_f32_16x16x32_bf16 v[140:143], v[84:87], a[24:27], v[140:143]\n"
"ds_read_b64_tr_b16 a[184:185], v92 offset:16384\n"
"ds_read_b64_tr_b16 a[186:187], v92 offset:16896\n"
"v_mfma_f32_16x16x32_bf16 v[144:147], v[84:87], a[28:31], v[144:147]\n"
"ds_read_b64_tr_b16 a[188:189], v93 offset:16384\n"
"ds_read_b64_tr_b16 a[190:191], v93 offset:16896\n"
"v_mfma_f32_16x16x32_bf16 v[128:131], v[88:91], a[152:155], v[128:131]\n"
"buffer_load_dwordx4 v10, s[24:27], s72 offen offset:2048 lds\n"
"v_mfma_f32_16x16x32_bf16 v[132:135], v[88:91], a[156:159], v[132:135]\n"
"ds_read_b64_tr_b16 a[192:193], v94 offset:16384\n"
"ds_read_b64_tr_b16 a[194:195], v94 offset:16896\n"
"v_mfma_f32_16x16x32_bf16 v[136:139], v[88:91], a[220:223], v[136:139]\n"
"ds_read_b64_tr_b16 a[196:197], v95 offset:16384\n"
"ds_read_b64_tr_b16 a[198:199], v95 offset:16896\n"
"v_mfma_f32_16x16x32_bf16 v[140:143], v[88:91], a[56:59], v[140:143]\n"
"v_cvt_pk_bf16_f32 v96, v128, v129\n"
"v_cvt_pk_bf16_f32 v97, v130, v131\n"
"v_mfma_f32_16x16x32_bf16 v[144:147], v[88:91], a[60:63], v[144:147]\n"
"s_waitcnt lgkmcnt(14)\n"
"v_cvt_pk_bf16_f32 v98, v132, v133\n"
"v_cvt_pk_bf16_f32 v99, v134, v135\n"
"v_mfma_f32_16x16x32_bf16 v[148:151], v[168:171], v[192:195], 0\n"
"buffer_load_dwordx4 v11, s[24:27], s72 offen offset:3072 lds\n"
"v_mfma_f32_16x16x32_bf16 v[152:155], v[168:171], v[208:211], 0\n"
"s_add_u32 m0, s69, 147456\n"
"s_add_u32 s72, s60, 65536\n"
"v_cvt_pk_bf16_f32 v100, v136, v137\n"
"v_mfma_f32_16x16x32_bf16 v[156:159], v[168:171], v[224:227], 0\n"
"v_cvt_pk_bf16_f32 v104, v144, v145\n"
"v_cvt_pk_bf16_f32 v105, v146, v147\n"
"v_mfma_f32_16x16x32_bf16 v[160:163], v[168:171], v[240:243], 0\n"
"v_cvt_pk_bf16_f32 v101, v138, v139\n"
"v_cvt_pk_bf16_f32 v102, v140, v141\n"
"v_mfma_f32_16x16x32_bf16 v[164:167], v[168:171], v[64:67], 0\n"
"s_waitcnt lgkmcnt(8)\n"
"buffer_load_dwordx4 v8, s[24:27], s72 offen lds\n"
"v_mfma_f32_16x16x32_bf16 v[148:151], v[172:175], a[64:67], v[148:151]\n"
"v_cvt_pk_bf16_f32 v103, v142, v143\n"
"ds_read_b64_tr_b16 v[80:81], v92 offset:32768\n"
"v_mfma_f32_16x16x32_bf16 v[152:155], v[172:175], a[68:71], v[152:155]\n"
"v_permlane32_swap_b32 v104, v106\n"
"v_mfma_f32_16x16x32_bf16 v[156:159], v[172:175], a[160:163], v[156:159]\n"
"v_permlane32_swap_b32 v105, v107\n"
"v_mfma_f32_16x16x32_bf16 v[160:163], v[172:175], a[224:227], v[160:163]\n"
"buffer_load_dwordx4 v9, s[24:27], s72 offen offset:1024 lds\n"
"v_mfma_f32_16x16x32_bf16 v[164:167], v[172:175], a[228:231], v[164:167]\n"
"ds_read_b64_tr_b16 v[82:83], v92 offset:33280\n"
"ds_read_b64_tr_b16 v[84:85], v93 offset:32768\n"
"v_mfma_f32_16x16x32_bf16 v[148:151], v[176:179], a[72:75], v[148:151]\n"
"ds_read_b64_tr_b16 v[86:87], v93 offset:33280\n"
"ds_read_b64_tr_b16 v[88:89], v94 offset:32768\n"
"v_mfma_f32_16x16x32_bf16 v[152:155], v[176:179], a[76:79], v[152:155]\n"
"ds_read_b64_tr_b16 v[90:91], v94 offset:33280\n"
"ds_read_b64_tr_b16 v[168:169], v95 offset:32768\n"
"v_mfma_f32_16x16x32_bf16 v[156:159], v[176:179], a[176:179], v[156:159]\n"
"buffer_load_dwordx4 v10, s[24:27], s72 offen offset:2048 lds\n"
"v_mfma_f32_16x16x32_bf16 v[160:163], v[176:179], a[0:3], v[160:163]\n"
"ds_read_b64_tr_b16 v[170:171], v95 offset:33280\n"
"ds_read_b64_tr_b16 v[172:173], v92 offset:49152\n"
"v_mfma_f32_16x16x32_bf16 v[164:167], v[176:179], a[4:7], v[164:167]\n"
"ds_read_b64_tr_b16 v[174:175], v92 offset:49664\n"
"ds_read_b64_tr_b16 v[176:177], v93 offset:49152\n"
"v_mfma_f32_16x16x32_bf16 v[148:151], v[180:183], a[80:83], v[148:151]\n"
"ds_read_b64_tr_b16 v[178:179], v93 offset:49664\n"
"v_add_u32 v92, 16384, v92\n"
"v_mfma_f32_16x16x32_bf16 v[152:155], v[180:183], a[84:87], v[152:155]\n"
"buffer_load_dwordx4 v11, s[24:27], s72 offen offset:3072 lds\n"
"v_mfma_f32_16x16x32_bf16 v[156:159], v[180:183], a[208:211], v[156:159]\n"
"s_add_u32 m0, s69, 114688\n"
"s_add_u32 s72, s60, 98304\n"
"v_add_u32 v93, 16384, v93\n"
"v_mfma_f32_16x16x32_bf16 v[160:163], v[180:183], a[32:35], v[160:163]\n"
"v_mfma_f32_16x16x32_bf16 v[164:167], v[180:183], a[36:39], v[164:167]\n"
"s_waitcnt lgkmcnt(11)\n"
"ds_read_b64_tr_b16 v[180:181], v94 offset:49152\n"
"ds_read_b64_tr_b16 v[182:183], v94 offset:49664\n"
"v_mfma_f32_16x16x32_bf16 v[148:151], a[184:187], v[196:199], v[148:151]\n"
"buffer_load_dwordx4 v8, s[24:27], s72 offen lds\n"
"v_mfma_f32_16x16x32_bf16 v[152:155], a[184:187], v[212:215], v[152:155]\n"
"v_add_u32 v94, 16384, v94\n"
"v_mfma_f32_16x16x32_bf16 v[156:159], a[184:187], v[228:231], v[156:159]\n"
"v_mfma_f32_16x16x32_bf16 v[160:163], a[184:187], v[244:247], v[160:163]\n"
"v_mfma_f32_16x16x32_bf16 v[164:167], a[184:187], v[68:71], v[164:167]\n"
"buffer_load_dwordx4 v9, s[24:27], s72 offen offset:1024 lds\n"
"v_mfma_f32_16x16x32_bf16 v[148:151], a[188:191], a[88:91], v[148:151]\n"
"ds_read_b64_tr_b16 a[184:185], v95 offset:49152\n"
"ds_read_b64_tr_b16 a[186:187], v95 offset:49664\n"
"v_mfma_f32_16x16x32_bf16 v[152:155], a[188:191], a[92:95], v[152:155]\n"
"v_add_u32 v95, 16384, v95\n"
"v_mfma_f32_16x16x32_bf16 v[156:159], a[188:191], a[164:167], v[156:159]\n"
"v_mfma_f32_16x16x32_bf16 v[160:163], a[188:191], a[232:235], v[160:163]\n"
"v_mfma_f32_16x16x32_bf16 v[164:167], a[188:191], a[236:239], v[164:167]\n"
"buffer_load_dwordx4 v10, s[24:27], s72 offen offset:2048 lds\n"
"v_mfma_f32_16x16x32_bf16 v[148:151], a[192:195], a[96:99], v[148:151]\n"
"v_mfma_f32_16x16x32_bf16 v[152:155], a[192:195], a[100:103], v[152:155]\n"
"v_mfma_f32_16x16x32_bf16 v[156:159], a[192:195], a[180:183], v[156:159]\n"
"v_mfma_f32_16x16x32_bf16 v[160:163], a[192:195], a[8:11], v[160:163]\n"
"buffer_load_dwordx4 v11, s[24:27], s72 offen offset:3072 lds\n"
"v_mfma_f32_16x16x32_bf16 v[164:167], a[192:195], a[12:15], v[164:167]\n"
"s_add_u32 m0, s69, 81920\n"
"s_add_u32 s72, s61, 32768\n"
"v_mfma_f32_16x16x32_bf16 v[148:151], a[196:199], a[104:107], v[148:151]\n"
"v_mfma_f32_16x16x32_bf16 v[152:155], a[196:199], a[108:111], v[152:155]\n"
"v_mfma_f32_16x16x32_bf16 v[156:159], a[196:199], a[212:215], v[156:159]\n"
"v_mfma_f32_16x16x32_bf16 v[160:163], a[196:199], a[40:43], v[160:163]\n"
"v_mfma_f32_16x16x32_bf16 v[164:167], a[196:199], a[44:47], v[164:167]\n"
"s_waitcnt lgkmcnt(0)\n"
"v_mfma_f32_16x16x32_bf16 v[148:151], v[80:83], v[200:203], v[148:151]\n"
"v_mfma_f32_16x16x32_bf16 v[152:155], v[80:83], v[216:219], v[152:155]\n"
"buffer_store_dwordx4 v[96:99], v0, s[28:31], s66 offen sc1\n"
"v_mfma_f32_16x16x32_bf16 v[156:159], v[80:83], v[232:235], v[156:159]\n"
"v_mfma_f32_16x16x32_bf16 v[160:163], v[80:83], v[248:251], v[160:163]\n"
"v_mfma_f32_16x16x32_bf16 v[164:167], v[80:83], v[72:75], v[164:167]\n"
"v_mfma_f32_16x16x32_bf16 v[148:151], v[84:87], a[112:115], v[148:151]\n"
"v_mfma_f32_16x16x32_bf16 v[152:155], v[84:87], a[116:119], v[152:155]\n"
"v_mfma_f32_16x16x32_bf16 v[156:159], v[84:87], a[168:171], v[156:159]\n"
"v_mfma_f32_16x16x32_bf16 v[160:163], v[84:87], a[240:243], v[160:163]\n"
"buffer_store_dwordx4 v[100:103], v0, s[28:31], s66 offen offset:1024 sc1\n"
"v_mfma_f32_16x16x32_bf16 v[164:167], v[84:87], a[244:247], v[164:167]\n"
"v_mfma_f32_16x16x32_bf16 v[148:151], v[88:91], a[120:123], v[148:151]\n"
"v_mfma_f32_16x16x32_bf16 v[152:155], v[88:91], a[124:127], v[152:155]\n"
"v_mfma_f32_16x16x32_bf16 v[156:159], v[88:91], a[200:203], v[156:159]\n"
"buffer_store_dwordx4 v[104:107], v1, s[28:31], s66 offen offset:2048 sc1\n"
"v_mfma_f32_16x16x32_bf16 v[160:163], v[88:91], a[16:19], v[160:163]\n"
"s_waitcnt vmcnt(20)\n"
"buffer_store_dword v31, v191, s[36:39], s71 offen offset:140 sc1\n"
"v_mfma_f32_16x16x32_bf16 v[164:167], v[88:91], a[20:23], v[164:167]\n"
"s_lshl_b32 s71, s58, 1\n"
"v_mfma_f32_16x16x32_bf16 v[148:151], v[168:171], a[128:131], v[148:151]\n"
"buffer_load_dwordx4 v8, s[24:27], s61 offen lds\n"
"v_mfma_f32_16x16x32_bf16 v[152:155], v[168:171], a[132:135], v[152:155]\n"
"v_mfma_f32_16x16x32_bf16 v[156:159], v[168:171], a[216:219], v[156:159]\n"
"v_mfma_f32_16x16x32_bf16 v[160:163], v[168:171], a[48:51], v[160:163]\n"
"v_mfma_f32_16x16x32_bf16 v[164:167], v[168:171], a[52:55], v[164:167]\n"
"buffer_load_dwordx4 v9, s[24:27], s61 offen offset:1024 lds\n"
"v_mfma_f32_16x16x32_bf16 v[148:151], v[172:175], v[204:207], v[148:151]\n"
"v_mfma_f32_16x16x32_bf16 v[152:155], v[172:175], v[220:223], v[152:155]\n"
"v_mfma_f32_16x16x32_bf16 v[156:159], v[172:175], v[236:239], v[156:159]\n"
"v_mfma_f32_16x16x32_bf16 v[160:163], v[172:175], v[252:255], v[160:163]\n"
"v_mfma_f32_16x16x32_bf16 v[164:167], v[172:175], v[76:79], v[164:167]\n"
"v_mfma_f32_16x16x32_bf16 v[148:151], v[176:179], a[136:139], v[148:151]\n"
"v_mfma_f32_16x16x32_bf16 v[152:155], v[176:179], a[140:143], v[152:155]\n"
"v_mfma_f32_16x16x32_bf16 v[156:159], v[176:179], a[172:175], v[156:159]\n"
"v_mfma_f32_16x16x32_bf16 v[160:163], v[176:179], a[248:251], v[160:163]\n"
"buffer_load_dwordx4 v10, s[24:27], s61 offen offset:2048 lds\n"
"v_mfma_f32_16x16x32_bf16 v[164:167], v[176:179], a[252:255], v[164:167]\n"
"buffer_load_dwordx4 v11, s[24:27], s61 offen offset:3072 lds\n"
"v_mfma_f32_16x16x32_bf16 v[148:151], v[180:183], a[144:147], v[148:151]\n"
"s_add_u32 m0, s69, 98304\n"
"v_mfma_f32_16x16x32_bf16 v[152:155], v[180:183], a[148:151], v[152:155]\n"
"v_mfma_f32_16x16x32_bf16 v[156:159], v[180:183], a[204:207], v[156:159]\n"
"s_waitcnt vmcnt(8)\n"
"s_barrier\n"
"ds_read_b64_tr_b16 a[188:189], v92 offset:49152\n"
"ds_read_b64_tr_b16 a[190:191], v92 offset:49664\n"
"v_mfma_f32_16x16x32_bf16 v[160:163], v[180:183], a[24:27], v[160:163]\n"
"ds_read_b64_tr_b16 a[192:193], v93 offset:49152\n"
"ds_read_b64_tr_b16 a[194:195], v93 offset:49664\n"
"v_mfma_f32_16x16x32_bf16 v[164:167], v[180:183], a[28:31], v[164:167]\n"
"ds_read_b64_tr_b16 a[196:197], v94 offset:49152\n"
"ds_read_b64_tr_b16 a[198:199], v94 offset:49664\n"
"v_mfma_f32_16x16x32_bf16 v[148:151], a[184:187], a[152:155], v[148:151]\n"
"ds_read_b64_tr_b16 v[80:81], v95 offset:49152\n"
"ds_read_b64_tr_b16 v[82:83], v95 offset:49664\n"
"v_mfma_f32_16x16x32_bf16 v[152:155], a[184:187], a[156:159], v[152:155]\n"
"buffer_load_dwordx4 v8, s[24:27], s72 offen lds\n"
"v_mfma_f32_16x16x32_bf16 v[156:159], a[184:187], a[220:223], v[156:159]\n"
"buffer_load_dwordx4 v9, s[24:27], s72 offen offset:1024 lds\n"
"v_mfma_f32_16x16x32_bf16 v[160:163], a[184:187], a[56:59], v[160:163]\n"
"v_add_u32 v92, 81920, v92\n"
"ds_read_b64_tr_b16 v[84:85], v92 offset:32768\n"
"v_mfma_f32_16x16x32_bf16 v[164:167], a[184:187], a[60:63], v[164:167]\n"
"ds_read_b64_tr_b16 v[86:87], v92 offset:33280\n"
"v_add_u32 v93, 81920, v93\n"
"buffer_load_dwordx4 v10, s[24:27], s72 offen offset:2048 lds\n"
"ds_read_b64_tr_b16 v[88:89], v93 offset:32768\n"
"ds_read_b64_tr_b16 v[90:91], v93 offset:33280\n"
"s_waitcnt lgkmcnt(10)\n"
"v_mfma_f32_16x16x32_bf16 v[128:131], a[188:191], v[192:195], 0\n"
"v_add_u32 v94, 81920, v94\n"
"ds_read_b64_tr_b16 v[168:169], v94 offset:32768\n"
"v_mfma_f32_16x16x32_bf16 v[132:135], a[188:191], v[208:211], 0\n"
"v_cvt_pk_bf16_f32 v104, v164, v165\n"
"v_cvt_pk_bf16_f32 v105, v166, v167\n"
"v_mfma_f32_16x16x32_bf16 v[136:139], a[188:191], v[224:227], 0\n"
"ds_read_b64_tr_b16 v[170:171], v94 offset:33280\n"
"v_add_u32 v95, 81920, v95\n"
"v_mfma_f32_16x16x32_bf16 v[140:143], a[188:191], v[240:243], 0\n"
"buffer_load_dwordx4 v11, s[24:27], s72 offen offset:3072 lds\n"
"v_mfma_f32_16x16x32_bf16 v[144:147], a[188:191], v[64:67], 0\n"
"s_add_u32 m0, s69, 0\n"
"buffer_load_dword v26, v190, s[36:39], s76 offen offset:128 sc1\n"
"s_waitcnt lgkmcnt(10)\n"
"v_mfma_f32_16x16x32_bf16 v[128:131], a[192:195], a[64:67], v[128:131]\n"
"s_add_u32 s72, s61, 65536\n"
"ds_read_b64_tr_b16 v[172:173], v95 offset:32768\n"
"ds_read_b64_tr_b16 v[174:175], v95 offset:33280\n"
"v_mfma_f32_16x16x32_bf16 v[132:135], a[192:195], a[68:71], v[132:135]\n"
"v_cvt_pk_bf16_f32 v96, v148, v149\n"
"v_cvt_pk_bf16_f32 v97, v150, v151\n"
"v_mfma_f32_16x16x32_bf16 v[136:139], a[192:195], a[160:163], v[136:139]\n"
"buffer_load_dwordx4 v8, s[24:27], s72 offen lds\n"
"v_mfma_f32_16x16x32_bf16 v[140:143], a[192:195], a[224:227], v[140:143]\n"
"v_cvt_pk_bf16_f32 v98, v152, v153\n"
"v_cvt_pk_bf16_f32 v99, v154, v155\n"
"v_mfma_f32_16x16x32_bf16 v[144:147], a[192:195], a[228:231], v[144:147]\n"
"s_waitcnt lgkmcnt(7)\n"
"v_cvt_pk_bf16_f32 v100, v156, v157\n"
"v_cvt_pk_bf16_f32 v101, v158, v159\n"
"v_mfma_f32_16x16x32_bf16 v[128:131], a[196:199], a[72:75], v[128:131]\n"
"v_cvt_pk_bf16_f32 v102, v160, v161\n"
"v_cvt_pk_bf16_f32 v103, v162, v163\n"
"v_mfma_f32_16x16x32_bf16 v[132:135], a[196:199], a[76:79], v[132:135]\n"
"buffer_load_dwordx4 v9, s[24:27], s72 offen offset:1024 lds\n"
"v_mfma_f32_16x16x32_bf16 v[136:139], a[196:199], a[176:179], v[136:139]\n"
"v_permlane32_swap_b32 v104, v106\n"
"v_mfma_f32_16x16x32_bf16 v[140:143], a[196:199], a[0:3], v[140:143]\n"
"v_permlane32_swap_b32 v105, v107\n"
"v_mfma_f32_16x16x32_bf16 v[144:147], a[196:199], a[4:7], v[144:147]\n"
"ds_read_b64_tr_b16 v[176:177], v92 offset:49152\n"
"ds_read_b64_tr_b16 v[178:179], v92 offset:49664\n"
"v_mfma_f32_16x16x32_bf16 v[128:131], v[80:83], a[80:83], v[128:131]\n"
"buffer_load_dwordx4 v10, s[24:27], s72 offen offset:2048 lds\n"
"v_mfma_f32_16x16x32_bf16 v[132:135], v[80:83], a[84:87], v[132:135]\n"
"ds_read_b64_tr_b16 v[180:181], v93 offset:49152\n"
"ds_read_b64_tr_b16 v[182:183], v93 offset:49664\n"
"v_mfma_f32_16x16x32_bf16 v[136:139], v[80:83], a[208:211], v[136:139]\n"
"ds_read_b64_tr_b16 a[184:185], v94 offset:49152\n"
"ds_read_b64_tr_b16 a[186:187], v94 offset:49664\n"
"v_mfma_f32_16x16x32_bf16 v[140:143], v[80:83], a[32:35], v[140:143]\n"
"ds_read_b64_tr_b16 a[188:189], v95 offset:49152\n"
"ds_read_b64_tr_b16 a[190:191], v95 offset:49664\n"
"v_mfma_f32_16x16x32_bf16 v[144:147], v[80:83], a[36:39], v[144:147]\n"
"s_waitcnt lgkmcnt(8)\n"
"ds_read_b64_tr_b16 a[192:193], v92 offset:16384\n"
"ds_read_b64_tr_b16 a[194:195], v92 offset:16896\n"
"v_mfma_f32_16x16x32_bf16 v[128:131], v[84:87], v[196:199], v[128:131]\n"
"buffer_load_dwordx4 v11, s[24:27], s72 offen offset:3072 lds\n"
"v_mfma_f32_16x16x32_bf16 v[132:135], v[84:87], v[212:215], v[132:135]\n"
"s_add_u32 m0, s69, 16384\n"
"s_add_u32 s72, s61, 98304\n"
"ds_read_b64_tr_b16 a[196:197], v93 offset:16384\n"
"v_mfma_f32_16x16x32_bf16 v[136:139], v[84:87], v[228:231], v[136:139]\n"
"ds_read_b64_tr_b16 a[198:199], v93 offset:16896\n"
"ds_read_b64_tr_b16 v[80:81], v94 offset:16384\n"
"v_mfma_f32_16x16x32_bf16 v[140:143], v[84:87], v[244:247], v[140:143]\n"
"ds_read_b64_tr_b16 v[82:83], v94 offset:16896\n"
"v_add_u32 v92, 4294918144, v92\n"
"v_mfma_f32_16x16x32_bf16 v[144:147], v[84:87], v[68:71], v[144:147]\n"
"buffer_load_dwordx4 v8, s[24:27], s72 offen lds\n"
"v_mfma_f32_16x16x32_bf16 v[128:131], v[88:91], a[88:91], v[128:131]\n"
"ds_read_b64_tr_b16 v[84:85], v95 offset:16384\n"
"ds_read_b64_tr_b16 v[86:87], v95 offset:16896\n"
"v_mfma_f32_16x16x32_bf16 v[132:135], v[88:91], a[92:95], v[132:135]\n"
"v_add_u32 v93, 4294918144, v93\n"
"v_add_u32 v94, 4294918144, v94\n"
"v_mfma_f32_16x16x32_bf16 v[136:139], v[88:91], a[164:167], v[136:139]\n"
"v_add_u32 v95, 4294918144, v95\n"
"v_mfma_f32_16x16x32_bf16 v[140:143], v[88:91], a[232:235], v[140:143]\n"
"buffer_load_dwordx4 v9, s[24:27], s72 offen offset:1024 lds\n"
"v_mfma_f32_16x16x32_bf16 v[144:147], v[88:91], a[236:239], v[144:147]\n"
"v_mfma_f32_16x16x32_bf16 v[128:131], v[168:171], a[96:99], v[128:131]\n"
"v_mfma_f32_16x16x32_bf16 v[132:135], v[168:171], a[100:103], v[132:135]\n"
"v_mfma_f32_16x16x32_bf16 v[136:139], v[168:171], a[180:183], v[136:139]\n"
"buffer_load_dwordx4 v10, s[24:27], s72 offen offset:2048 lds\n"
"v_mfma_f32_16x16x32_bf16 v[140:143], v[168:171], a[8:11], v[140:143]\n"
"v_mfma_f32_16x16x32_bf16 v[144:147], v[168:171], a[12:15], v[144:147]\n"
"v_mfma_f32_16x16x32_bf16 v[128:131], v[172:175], a[104:107], v[128:131]\n"
"v_mfma_f32_16x16x32_bf16 v[132:135], v[172:175], a[108:111], v[132:135]\n"
"v_mfma_f32_16x16x32_bf16 v[136:139], v[172:175], a[212:215], v[136:139]\n"
"v_mfma_f32_16x16x32_bf16 v[140:143], v[172:175], a[40:43], v[140:143]\n"
"v_mfma_f32_16x16x32_bf16 v[144:147], v[172:175], a[44:47], v[144:147]\n"
"s_waitcnt lgkmcnt(0)\n"
"v_mfma_f32_16x16x32_bf16 v[128:131], v[176:179], v[200:203], v[128:131]\n"
"v_mfma_f32_16x16x32_bf16 v[132:135], v[176:179], v[216:219], v[132:135]\n"
"v_mfma_f32_16x16x32_bf16 v[136:139], v[176:179], v[232:235], v[136:139]\n"
"v_mfma_f32_16x16x32_bf16 v[140:143], v[176:179], v[248:251], v[140:143]\n"
"v_mfma_f32_16x16x32_bf16 v[144:147], v[176:179], v[72:75], v[144:147]\n"
"v_mfma_f32_16x16x32_bf16 v[128:131], v[180:183], a[112:115], v[128:131]\n"
"v_mfma_f32_16x16x32_bf16 v[132:135], v[180:183], a[116:119], v[132:135]\n"
"buffer_load_dwordx4 v11, s[24:27], s72 offen offset:3072 lds\n"
"v_mfma_f32_16x16x32_bf16 v[136:139], v[180:183], a[168:171], v[136:139]\n"
"v_mfma_f32_16x16x32_bf16 v[140:143], v[180:183], a[240:243], v[140:143]\n"
"v_mfma_f32_16x16x32_bf16 v[144:147], v[180:183], a[244:247], v[144:147]\n"
"v_mfma_f32_16x16x32_bf16 v[128:131], a[184:187], a[120:123], v[128:131]\n"
"v_mfma_f32_16x16x32_bf16 v[132:135], a[184:187], a[124:127], v[132:135]\n"
"v_mfma_f32_16x16x32_bf16 v[136:139], a[184:187], a[200:203], v[136:139]\n"
"v_mfma_f32_16x16x32_bf16 v[140:143], a[184:187], a[16:19], v[140:143]\n"
"v_mfma_f32_16x16x32_bf16 v[144:147], a[184:187], a[20:23], v[144:147]\n"
"s_waitcnt vmcnt(8)\n"
"v_cmp_eq_u32 vcc, v26, v31\n"
"s_nop 1\n"
"s_and_b32 s70, vcc_lo, 7\n"
"s_cmp_eq_u32 s70, 7\n"
"s_cbranch_scc0 LS0P1%=\n"
"LB0P1%=:\n"
"v_mfma_f32_16x16x32_bf16 v[128:131], a[188:191], a[128:131], v[128:131]\n"
"buffer_store_dwordx4 v[96:99], v0, s[28:31], s67 offen sc1\n"
"v_mfma_f32_16x16x32_bf16 v[132:135], a[188:191], a[132:135], v[132:135]\n"
"v_mfma_f32_16x16x32_bf16 v[136:139], a[188:191], a[216:219], v[136:139]\n"
"v_mfma_f32_16x16x32_bf16 v[140:143], a[188:191], a[48:51], v[140:143]\n"
"v_mfma_f32_16x16x32_bf16 v[144:147], a[188:191], a[52:55], v[144:147]\n"
"v_mfma_f32_16x16x32_bf16 v[128:131], a[192:195], v[204:207], v[128:131]\n"
"v_mfma_f32_16x16x32_bf16 v[132:135], a[192:195], v[220:223], v[132:135]\n"
"buffer_store_dwordx4 v[100:103], v0, s[28:31], s67 offen offset:1024 sc1\n"
"v_mfma_f32_16x16x32_bf16 v[136:139], a[192:195], v[236:239], v[136:139]\n"
"v_mfma_f32_16x16x32_bf16 v[140:143], a[192:195], v[252:255], v[140:143]\n"
"v_mfma_f32_16x16x32_bf16 v[144:147], a[192:195], v[76:79], v[144:147]\n"
"buffer_store_dwordx4 v[104:107], v1, s[28:31], s67 offen offset:2048 sc1\n"
"v_mfma_f32_16x16x32_bf16 v[128:131], a[196:199], a[136:139], v[128:131]\n"
"buffer_store_dword v31, v191, s[36:39], s71 offen offset:144 sc1\n"
"v_mfma_f32_16x16x32_bf16 v[132:135], a[196:199], a[140:143], v[132:135]\n"
"v_mfma_f32_16x16x32_bf16 v[136:139], a[196:199], a[172:175], v[136:139]\n"
"v_mfma_f32_16x16x32_bf16 v[140:143], a[196:199], a[248:251], v[140:143]\n"
"buffer_load_dwordx4 v[108:111], v0, s[44:47], 0 offen sc1\n"
"v_mfma_f32_16x16x32_bf16 v[144:147], a[196:199], a[252:255], v[144:147]\n"
"v_mfma_f32_16x16x32_bf16 v[128:131], v[80:83], a[144:147], v[128:131]\n"
"v_mfma_f32_16x16x32_bf16 v[132:135], v[80:83], a[148:151], v[132:135]\n"
"v_mfma_f32_16x16x32_bf16 v[136:139], v[80:83], a[204:207], v[136:139]\n"
"buffer_load_dwordx4 v[112:115], v0, s[44:47], 0 offen offset:1024 sc1\n"
"v_mfma_f32_16x16x32_bf16 v[140:143], v[80:83], a[24:27], v[140:143]\n"
"v_mfma_f32_16x16x32_bf16 v[144:147], v[80:83], a[28:31], v[144:147]\n"
"v_mfma_f32_16x16x32_bf16 v[128:131], v[84:87], a[152:155], v[128:131]\n"
"v_mfma_f32_16x16x32_bf16 v[132:135], v[84:87], a[156:159], v[132:135]\n"
"v_mfma_f32_16x16x32_bf16 v[136:139], v[84:87], a[220:223], v[136:139]\n"
"v_mfma_f32_16x16x32_bf16 v[140:143], v[84:87], a[56:59], v[140:143]\n"
"v_mfma_f32_16x16x32_bf16 v[144:147], v[84:87], a[60:63], v[144:147]\n"
"s_nop 3\n"
"v_cvt_pk_bf16_f32 v96, v128, v129\n"
"v_cvt_pk_bf16_f32 v97, v130, v131\n"
"v_cvt_pk_bf16_f32 v98, v132, v133\n"
"buffer_load_dwordx2 v[116:117], v188, s[44:47], 0 offen offset:2048 sc1\n"
"v_cvt_pk_bf16_f32 v99, v134, v135\n"
"v_cvt_pk_bf16_f32 v100, v136, v137\n"
"buffer_load_dwordx4 v[118:121], v0, s[44:47], 0 offen offset:2560 sc1\n"
"v_cvt_pk_bf16_f32 v101, v138, v139\n"
"v_cvt_pk_bf16_f32 v104, v144, v145\n"
"v_cvt_pk_bf16_f32 v105, v146, v147\n"
"v_cvt_pk_bf16_f32 v102, v140, v141\n"
"v_cvt_pk_bf16_f32 v103, v142, v143\n"
"v_permlane32_swap_b32 v104, v106\n"
"s_waitcnt vmcnt(8)\n"
"s_barrier\n"
"ds_read_b64_tr_b16 v[88:89], v92 offset:32768\n"
"ds_read_b64_tr_b16 v[90:91], v92 offset:33280\n"
"ds_read_b64_tr_b16 v[168:169], v93 offset:32768\n"
"ds_read_b64_tr_b16 v[170:171], v93 offset:33280\n"
"v_permlane32_swap_b32 v105, v107\n"
"ds_read_b64_tr_b16 v[172:173], v94 offset:32768\n"
"ds_read_b64_tr_b16 v[174:175], v94 offset:33280\n"
"ds_read_b64_tr_b16 v[176:177], v95 offset:32768\n"
"ds_read_b64_tr_b16 v[178:179], v95 offset:33280\n"
"ds_read_b64_tr_b16 v[180:181], v92 offset:49152\n"
"ds_read_b64_tr_b16 v[182:183], v92 offset:49664\n"
"ds_read_b64_tr_b16 a[184:185], v93 offset:49152\n"
"ds_read_b64_tr_b16 a[186:187], v93 offset:49664\n"
"ds_read_b64_tr_b16 a[188:189], v94 offset:49152\n"
"buffer_load_dwordx4 v[122:125], v0, s[44:47], 0 offen offset:3584 sc1\n"
"ds_read_b64_tr_b16 a[190:191], v94 offset:49664\n"
"ds_read_b64_tr_b16 a[192:193], v95 offset:49152\n"
"buffer_load_dwordx2 v[126:127], v188, s[44:47], s50 offen offset:512 sc1\n"
"ds_read_b64_tr_b16 a[194:195], v95 offset:49664\n"
"v_add_u32 v92, 4294918144, v92\n"
"buffer_load_dwordx4 v[32:35], v0, s[44:47], s50 offen offset:1024 sc1\n"
"ds_read_b64_tr_b16 a[196:197], v92\n"
"ds_read_b64_tr_b16 a[198:199], v92 offset:512\n"
"v_add_u32 v93, 4294918144, v93\n"
"ds_read_b64_tr_b16 v[80:81], v93\n"
"ds_read_b64_tr_b16 v[82:83], v93 offset:512\n"
"buffer_load_dwordx4 v[36:39], v0, s[44:47], s50 offen offset:2048 sc1\n"
"v_add_u32 v94, 4294918144, v94\n"
"ds_read_b64_tr_b16 v[84:85], v94\n"
"s_waitcnt lgkmcnt(15)\n"
"v_mfma_f32_16x16x32_bf16 v[148:151], v[88:91], v[192:195], 0\n"
"buffer_load_dwordx2 v[40:41], v188, s[44:47], s50 offen offset:3072 sc1\n"
"v_mfma_f32_16x16x32_bf16 v[152:155], v[88:91], v[208:211], 0\n"
"ds_read_b64_tr_b16 v[86:87], v94 offset:512\n"
"v_add_u32 v95, 4294918144, v95\n"
"v_mfma_f32_16x16x32_bf16 v[156:159], v[88:91], v[224:227], 0\n"
"v_mfma_f32_16x16x32_bf16 v[160:163], v[88:91], v[240:243], 0\n"
"v_mfma_f32_16x16x32_bf16 v[164:167], v[88:91], v[64:67], 0\n"
"ds_read_b64_tr_b16 v[88:89], v95\n"
"ds_read_b64_tr_b16 v[90:91], v95 offset:512\n"
"v_mfma_f32_16x16x32_bf16 v[148:151], v[168:171], a[64:67], v[148:151]\n"
"v_mfma_f32_16x16x32_bf16 v[152:155], v[168:171], a[68:71], v[152:155]\n"
"v_mfma_f32_16x16x32_bf16 v[156:159], v[168:171], a[160:163], v[156:159]\n"
"v_mfma_f32_16x16x32_bf16 v[160:163], v[168:171], a[224:227], v[160:163]\n"
"v_mfma_f32_16x16x32_bf16 v[164:167], v[168:171], a[228:231], v[164:167]\n"
"ds_read_b64_tr_b16 v[168:169], v92 offset:16384\n"
"ds_read_b64_tr_b16 v[170:171], v92 offset:16896\n"
"v_mfma_f32_16x16x32_bf16 v[148:151], v[172:175], a[72:75], v[148:151]\n"
"v_mfma_f32_16x16x32_bf16 v[152:155], v[172:175], a[76:79], v[152:155]\n"
"v_mfma_f32_16x16x32_bf16 v[156:159], v[172:175], a[176:179], v[156:159]\n"
"v_mfma_f32_16x16x32_bf16 v[160:163], v[172:175], a[0:3], v[160:163]\n"
"v_mfma_f32_16x16x32_bf16 v[164:167], v[172:175], a[4:7], v[164:167]\n"
"s_waitcnt lgkmcnt(4)\n"
"ds_read_b64_tr_b16 v[172:173], v93 offset:16384\n"
"ds_read_b64_tr_b16 v[174:175], v93 offset:16896\n"
"v_mfma_f32_16x16x32_bf16 v[148:151], v[176:179], a[80:83], v[148:151]\n"
"v_mfma_f32_16x16x32_bf16 v[152:155], v[176:179], a[84:87], v[152:155]\n"
"v_mfma_f32_16x16x32_bf16 v[156:159], v[176:179], a[208:211], v[156:159]\n"
"v_mfma_f32_16x16x32_bf16 v[160:163], v[176:179], a[32:35], v[160:163]\n"
"v_mfma_f32_16x16x32_bf16 v[164:167], v[176:179], a[36:39], v[164:167]\n"
"ds_read_b64_tr_b16 v[176:177], v94 offset:16384\n"
"ds_read_b64_tr_b16 v[178:179], v94 offset:16896\n"
"v_mfma_f32_16x16x32_bf16 v[148:151], v[180:183], v[196:199], v[148:151]\n"
"v_mfma_f32_16x16x32_bf16 v[152:155], v[180:183], v[212:215], v[152:155]\n"
"v_mfma_f32_16x16x32_bf16 v[156:159], v[180:183], v[228:231], v[156:159]\n"
"v_mfma_f32_16x16x32_bf16 v[160:163], v[180:183], v[244:247], v[160:163]\n"
"v_mfma_f32_16x16x32_bf16 v[164:167], v[180:183], v[68:71], v[164:167]\n"
"ds_read_b64_tr_b16 v[180:181], v95 offset:16384\n"
"ds_read_b64_tr_b16 v[182:183], v95 offset:16896\n"
"v_mfma_f32_16x16x32_bf16 v[148:151], a[184:187], a[88:91], v[148:151]\n"
"v_mfma_f32_16x16x32_bf16 v[152:155], a[184:187], a[92:95], v[152:155]\n"
"v_mfma_f32_16x16x32_bf16 v[156:159], a[184:187], a[164:167], v[156:159]\n"
"v_mfma_f32_16x16x32_bf16 v[160:163], a[184:187], a[232:235], v[160:163]\n"
"v_mfma_f32_16x16x32_bf16 v[164:167], a[184:187], a[236:239], v[164:167]\n"
"v_mfma_f32_16x16x32_bf16 v[148:151], a[188:191], a[96:99], v[148:151]\n"
"v_mfma_f32_16x16x32_bf16 v[152:155], a[188:191], a[100:103], v[152:155]\n"
"v_mfma_f32_16x16x32_bf16 v[156:159], a[188:191], a[180:183], v[156:159]\n"
"v_mfma_f32_16x16x32_bf16 v[160:163], a[188:191], a[8:11], v[160:163]\n"
"v_mfma_f32_16x16x32_bf16 v[164:167], a[188:191], a[12:15], v[164:167]\n"
"v_mfma_f32_16x16x32_bf16 v[148:151], a[192:195], a[104:107], v[148:151]\n"
"v_mfma_f32_16x16x32_bf16 v[152:155], a[192:195], a[108:111], v[152:155]\n"
"v_mfma_f32_16x16x32_bf16 v[156:159], a[192:195], a[212:215], v[156:159]\n"
"buffer_load_dword v26, v190, s[36:39], s76 offen offset:128 sc1\n"
"v_mfma_f32_16x16x32_bf16 v[160:163], a[192:195], a[40:43], v[160:163]\n"
"v_mfma_f32_16x16x32_bf16 v[164:167], a[192:195], a[44:47], v[164:167]\n"
"v_mfma_f32_16x16x32_bf16 v[148:151], a[196:199], v[200:203], v[148:151]\n"
"v_mfma_f32_16x16x32_bf16 v[152:155], a[196:199], v[216:219], v[152:155]\n"
"v_mfma_f32_16x16x32_bf16 v[156:159], a[196:199], v[232:235], v[156:159]\n"
"v_mfma_f32_16x16x32_bf16 v[160:163], a[196:199], v[248:251], v[160:163]\n"
"v_mfma_f32_16x16x32_bf16 v[164:167], a[196:199], v[72:75], v[164:167]\n"
"v_mfma_f32_16x16x32_bf16 v[148:151], v[80:83], a[112:115], v[148:151]\n"
"v_mfma_f32_16x16x32_bf16 v[152:155], v[80:83], a[116:119], v[152:155]\n"
"v_mfma_f32_16x16x32_bf16 v[156:159], v[80:83], a[168:171], v[156:159]\n"
"v_mfma_f32_16x16x32_bf16 v[160:163], v[80:83], a[240:243], v[160:163]\n"
"v_mfma_f32_16x16x32_bf16 v[164:167], v[80:83], a[244:247], v[164:167]\n"
"buffer_store_dwordx4 v[96:99], v0, s[28:31], s68 offen sc1\n"
"v_mfma_f32_16x16x32_bf16 v[148:151], v[84:87], a[120:123], v[148:151]\n"
"v_mfma_f32_16x16x32_bf16 v[152:155], v[84:87], a[124:127], v[152:155]\n"
"v_mfma_f32_16x16x32_bf16 v[156:159], v[84:87], a[200:203], v[156:159]\n"
"v_mfma_f32_16x16x32_bf16 v[160:163], v[84:87], a[16:19], v[160:163]\n"
"v_mfma_f32_16x16x32_bf16 v[164:167], v[84:87], a[20:23], v[164:167]\n"
"s_waitcnt lgkmcnt(0)\n"
"v_mfma_f32_16x16x32_bf16 v[148:151], v[88:91], a[128:131], v[148:151]\n"
"v_mfma_f32_16x16x32_bf16 v[152:155], v[88:91], a[132:135], v[152:155]\n"
"v_mfma_f32_16x16x32_bf16 v[156:159], v[88:91], a[216:219], v[156:159]\n"
"buffer_store_dwordx4 v[100:103], v0, s[28:31], s68 offen offset:1024 sc1\n"
"v_mfma_f32_16x16x32_bf16 v[160:163], v[88:91], a[48:51], v[160:163]\n"
"v_mfma_f32_16x16x32_bf16 v[164:167], v[88:91], a[52:55], v[164:167]\n"
"v_mfma_f32_16x16x32_bf16 v[148:151], v[168:171], v[204:207], v[148:151]\n"
"v_mfma_f32_16x16x32_bf16 v[152:155], v[168:171], v[220:223], v[152:155]\n"
"v_mfma_f32_16x16x32_bf16 v[156:159], v[168:171], v[236:239], v[156:159]\n"
"buffer_store_dwordx4 v[104:107], v1, s[28:31], s68 offen offset:2048 sc1\n"
"v_mfma_f32_16x16x32_bf16 v[160:163], v[168:171], v[252:255], v[160:163]\n"
"v_mfma_f32_16x16x32_bf16 v[164:167], v[168:171], v[76:79], v[164:167]\n"
"v_mfma_f32_16x16x32_bf16 v[148:151], v[172:175], a[136:139], v[148:151]\n"
"v_mfma_f32_16x16x32_bf16 v[152:155], v[172:175], a[140:143], v[152:155]\n"
"v_mfma_f32_16x16x32_bf16 v[156:159], v[172:175], a[172:175], v[156:159]\n"
"v_mfma_f32_16x16x32_bf16 v[160:163], v[172:175], a[248:251], v[160:163]\n"
"v_mfma_f32_16x16x32_bf16 v[164:167], v[172:175], a[252:255], v[164:167]\n"
"v_mfma_f32_16x16x32_bf16 v[148:151], v[176:179], a[144:147], v[148:151]\n"
"v_mfma_f32_16x16x32_bf16 v[152:155], v[176:179], a[148:151], v[152:155]\n"
"v_mfma_f32_16x16x32_bf16 v[156:159], v[176:179], a[204:207], v[156:159]\n"
"v_mfma_f32_16x16x32_bf16 v[160:163], v[176:179], a[24:27], v[160:163]\n"
"v_mfma_f32_16x16x32_bf16 v[164:167], v[176:179], a[28:31], v[164:167]\n"
"s_mov_b32 s55, 32768\n"
"s_mov_b32 s56, 65536\n"
"s_mov_b32 s57, 98304\n"
"v_mfma_f32_16x16x32_bf16 v[148:151], v[180:183], a[152:155], v[148:151]\n"
"s_mov_b32 s58, 131072\n"
"v_mfma_f32_16x16x32_bf16 v[152:155], v[180:183], a[156:159], v[152:155]\n"
"v_mfma_f32_16x16x32_bf16 v[156:159], v[180:183], a[220:223], v[156:159]\n"
"v_mfma_f32_16x16x32_bf16 v[160:163], v[180:183], a[56:59], v[160:163]\n"
"v_mfma_f32_16x16x32_bf16 v[164:167], v[180:183], a[60:63], v[164:167]\n"
"s_waitcnt vmcnt(12)\n"
"v_mfma_f32_16x16x16_bf16 v[148:151], v[184:185], v[108:109], v[148:151]\n"
"v_mfma_f32_16x16x16_bf16 v[152:155], v[184:185], v[110:111], v[152:155]\n"
"s_waitcnt vmcnt(11)\n"
"v_mfma_f32_16x16x16_bf16 v[156:159], v[184:185], v[112:113], v[156:159]\n"
"v_mfma_f32_16x16x16_bf16 v[160:163], v[184:185], v[114:115], v[160:163]\n"
"s_waitcnt vmcnt(10)\n"
"v_mfma_f32_16x16x16_bf16 v[164:167], v[184:185], v[116:117], v[164:167]\n"
"s_waitcnt vmcnt(9)\n"
"v_mfma_f32_16x16x16_bf16 v[148:151], v[184:185], v[118:119], v[148:151]\n"
"s_waitcnt vmcnt(3)\n"
"v_cmp_eq_u32 vcc, v26, v31\n"
"s_nop 1\n"
"s_and_b32 s70, vcc_lo, 8\n"
"s_cmp_eq_u32 s70, 8\n"
"s_cbranch_scc0 LS1P1%=\n"
"LB1P1%=:\n"
"v_mfma_f32_16x16x16_bf16 v[152:155], v[184:185], v[120:121], v[152:155]\n"
"buffer_load_dwordx4 v[42:45], v0, s[44:47], s50 offen offset:3584 sc1\n"
"v_mfma_f32_16x16x16_bf16 v[156:159], v[184:185], v[122:123], v[156:159]\n"
"buffer_load_dwordx4 v[46:49], v0, s[44:47], s51 offen offset:512 sc1\n"
"v_mfma_f32_16x16x16_bf16 v[160:163], v[184:185], v[124:125], v[160:163]\n"
"buffer_load_dwordx2 v[50:51], v188, s[44:47], s51 offen offset:1536 sc1\n"
"v_mfma_f32_16x16x16_bf16 v[164:167], v[184:185], v[126:127], v[164:167]\n"
"buffer_load_dword v26, v190, s[36:39], s76 offen offset:128 sc1\n"
"v_mfma_f32_16x16x16_bf16 v[148:151], v[184:185], v[32:33], v[148:151]\n"
"buffer_load_dwordx4 v[128:131], v0, s[44:47], s52 offen offset:512 sc1\n"
"v_mfma_f32_16x16x16_bf16 v[152:155], v[184:185], v[34:35], v[152:155]\n"
"buffer_load_dwordx4 v[132:135], v0, s[44:47], s52 offen offset:1536 sc1\n"
"v_mfma_f32_16x16x16_bf16 v[156:159], v[184:185], v[36:37], v[156:159]\n"
"buffer_load_dwordx2 v[136:137], v188, s[44:47], s52 offen offset:2560 sc1\n"
"v_mfma_f32_16x16x16_bf16 v[160:163], v[184:185], v[38:39], v[160:163]\n"
"v_mfma_f32_16x16x16_bf16 v[164:167], v[184:185], v[40:41], v[164:167]\n"
"s_waitcnt vmcnt(6)\n"
"v_mfma_f32_16x16x16_bf16 v[148:151], v[184:185], v[42:43], v[148:151]\n"
"v_mfma_f32_16x16x16_bf16 v[152:155], v[184:185], v[44:45], v[152:155]\n"
"s_waitcnt vmcnt(5)\n"
"v_mfma_f32_16x16x16_bf16 v[156:159], v[184:185], v[46:47], v[156:159]\n"
"v_mfma_f32_16x16x16_bf16 v[160:163], v[184:185], v[48:49], v[160:163]\n"
"s_waitcnt vmcnt(4)\n"
"v_mfma_f32_16x16x16_bf16 v[164:167], v[184:185], v[50:51], v[164:167]\n"
"s_waitcnt vmcnt(3)\n"
"v_cmp_eq_u32 vcc, v26, v31\n"
"s_nop 1\n"
"s_and_b32 s70, vcc_lo, 16\n"
"s_cmp_eq_u32 s70, 16\n"
"s_cbranch_scc0 LS2P1%=\n"
"LB2P1%=:\n"
"buffer_load_dwordx4 v[52:55], v0, s[44:47], s51 offen offset:2048 sc1\n"
"buffer_load_dwordx4 v[56:59], v0, s[44:47], s51 offen offset:3072 sc1\n"
"buffer_load_dwordx2 v[60:61], v188, s[44:47], s52 offen sc1\n"
"s_waitcnt vmcnt(5)\n"
"v_max3_u32 v28, v128, v129, v130\n"
"buffer_load_dwordx4 v[138:141], v0, s[44:47], s52 offen offset:3072 sc1\n"
"buffer_load_dwordx4 v[142:145], v0, s[44:47], s53 offen sc1\n"
"buffer_load_dwordx2 v[146:147], v188, s[44:47], s53 offen offset:1024 sc1\n"
"s_waitcnt vmcnt(7)\n"
"v_max3_u32 v28, v28, v131, v132\n"
"v_max3_u32 v28, v28, v133, v134\n"
"s_waitcnt vmcnt(6)\n"
"v_max3_u32 v28, v28, v135, v136\n"
"v_max_u32 v28, v28, v137\n"
"v_cmp_eq_u32 vcc, -1, v28\n"
"s_nop 1\n"
"s_cbranch_vccnz LS3P1%=\n"
"LB3P1%=:\n"
"s_nop 1\n"
"v_mfma_f32_16x16x16_bf16 v[148:151], v[184:185], v[128:129], v[148:151]\n"
"v_mfma_f32_16x16x16_bf16 v[152:155], v[184:185], v[130:131], v[152:155]\n"
"v_mfma_f32_16x16x16_bf16 v[156:159], v[184:185], v[132:133], v[156:159]\n"
"v_mfma_f32_16x16x16_bf16 v[160:163], v[184:185], v[134:135], v[160:163]\n"
"v_mfma_f32_16x16x16_bf16 v[164:167], v[184:185], v[136:137], v[164:167]\n"
"s_waitcnt vmcnt(5)\n"
"v_mfma_f32_16x16x16_bf16 v[148:151], v[184:185], v[52:53], v[148:151]\n"
"v_mfma_f32_16x16x16_bf16 v[152:155], v[184:185], v[54:55], v[152:155]\n"
"s_waitcnt vmcnt(4)\n"
"v_mfma_f32_16x16x16_bf16 v[156:159], v[184:185], v[56:57], v[156:159]\n"
"v_mfma_f32_16x16x16_bf16 v[160:163], v[184:185], v[58:59], v[160:163]\n"
"s_waitcnt vmcnt(3)\n"
"v_mfma_f32_16x16x16_bf16 v[164:167], v[184:185], v[60:61], v[164:167]\n"
"s_waitcnt vmcnt(2)\n"
"v_max3_u32 v25, v138, v139, v140\n"
"s_waitcnt vmcnt(1)\n"
"v_max3_u32 v25, v25, v141, v142\n"
"v_max3_u32 v25, v25, v143, v144\n"
"s_waitcnt vmcnt(0)\n"
"v_max3_u32 v25, v25, v145, v146\n"
"v_max_u32 v25, v25, v147\n"
"v_cmp_eq_u32 vcc, -1, v25\n"
"s_nop 1\n"
"s_cbranch_vccnz LS4P1%=\n"
"LB4P1%=:\n"
"v_xor_b32 v19, 0x5bd1e995, v31\n"
"v_add_u32 v19, 1, v19\n"
"buffer_store_dword v19, v191, s[36:39], s76 offen sc1\n"
"v_mfma_f32_16x16x16_bf16 v[148:151], v[184:185], v[138:139], v[148:151]\n"
"v_mfma_f32_16x16x16_bf16 v[152:155], v[184:185], v[140:141], v[152:155]\n"
"v_mfma_f32_16x16x16_bf16 v[156:159], v[184:185], v[142:143], v[156:159]\n"
"v_mfma_f32_16x16x16_bf16 v[160:163], v[184:185], v[144:145], v[160:163]\n"
"v_mfma_f32_16x16x16_bf16 v[164:167], v[184:185], v[146:147], v[164:167]\n"
"s_nop 3\n"
"v_cvt_pk_bf16_f32 v96, v148, v149\n"
"v_cvt_pk_bf16_f32 v97, v150, v151\n"
"buffer_store_dwordx2 v[96:97], v189, s[40:43], 0 offen\n"
"v_cvt_pk_bf16_f32 v98, v152, v153\n"
"v_cvt_pk_bf16_f32 v99, v154, v155\n"
"buffer_store_dwordx2 v[98:99], v189, s[40:43], s55 offen\n"
"v_cvt_pk_bf16_f32 v100, v156, v157\n"
"v_cvt_pk_bf16_f32 v101, v158, v159\n"
"buffer_store_dwordx2 v[100:101], v189, s[40:43], s56 offen\n"
"v_cvt_pk_bf16_f32 v102, v160, v161\n"
"v_cvt_pk_bf16_f32 v103, v162, v163\n"
"buffer_store_dwordx2 v[102:103], v189, s[40:43], s57 offen\n"
"v_cvt_pk_bf16_f32 v104, v164, v165\n"
"v_cvt_pk_bf16_f32 v105, v166, v167\n"
"buffer_store_dwordx2 v[104:105], v189, s[40:43], s58 offen\n"
"s_branch LEND%=\n"
"LS0P1%=:\n"
"s_movk_i32 s77, 0x4000\n"
"LL0P1%=:\n"
"buffer_load_dword v26, v190, s[36:39], s76 offen offset:128 sc1\n"
"s_waitcnt vmcnt(0)\n"
"v_cmp_eq_u32 vcc, v26, v31\n"
"s_nop 1\n"
"s_and_b32 s70, vcc_lo, 7\n"
"s_cmp_eq_u32 s70, 7\n"
"s_cbranch_scc1 LB0P1%=\n"
"s_sub_u32 s77, s77, 1\n"
"s_cbranch_scc0 LL0P1%=\n"
"s_branch LB0P1%=\n"
"LS1P1%=:\n"
"s_movk_i32 s77, 0x4000\n"
"LL1P1%=:\n"
"buffer_load_dword v26, v190, s[36:39], s76 offen offset:128 sc1\n"
"s_waitcnt vmcnt(0)\n"
"v_cmp_eq_u32 vcc, v26, v31\n"
"s_nop 1\n"
"s_and_b32 s70, vcc_lo, 8\n"
"s_cmp_eq_u32 s70, 8\n"
"s_cbranch_scc1 LB1P1%=\n"
"s_sub_u32 s77, s77, 1\n"
"s_cbranch_scc0 LL1P1%=\n"
"s_branch LB1P1%=\n"
"LS2P1%=:\n"
"s_movk_i32 s77, 0x4000\n"
"LL2P1%=:\n"
"buffer_load_dword v26, v190, s[36:39], s76 offen offset:128 sc1\n"
"s_waitcnt vmcnt(0)\n"
"v_cmp_eq_u32 vcc, v26, v31\n"
"s_nop 1\n"
"s_and_b32 s70, vcc_lo, 16\n"
"s_cmp_eq_u32 s70, 16\n"
"s_cbranch_scc1 LB2P1%=\n"
"s_sub_u32 s77, s77, 1\n"
"s_cbranch_scc0 LL2P1%=\n"
"s_branch LB2P1%=\n"
"LS3P1%=:\n"
"s_movk_i32 s77, 0x1000\n"
"LL3P1%=:\n"
"buffer_load_dwordx4 v[128:131], v0, s[44:47], s52 offen offset:512 sc1\n"
"buffer_load_dwordx4 v[132:135], v0, s[44:47], s52 offen offset:1536 sc1\n"
"buffer_load_dwordx2 v[136:137], v188, s[44:47], s52 offen offset:2560 sc1\n"
"s_waitcnt vmcnt(0)\n"
"v_max3_u32 v28, v128, v129, v130\n"
"v_max3_u32 v28, v28, v131, v132\n"
"v_max3_u32 v28, v28, v133, v134\n"
"v_max3_u32 v28, v28, v135, v136\n"
"v_max_u32 v28, v28, v137\n"
"v_cmp_eq_u32 vcc, -1, v28\n"
"s_nop 1\n"
"s_cbranch_vccz LB3P1%=\n"
"s_sub_u32 s77, s77, 1\n"
"s_cbranch_scc0 LL3P1%=\n"
"s_branch LB3P1%=\n"
"LS4P1%=:\n"
"s_movk_i32 s77, 0x1000\n"
"LL4P1%=:\n"
"buffer_load_dwordx4 v[138:141], v0, s[44:47], s52 offen offset:3072 sc1\n"
"buffer_load_dwordx4 v[142:145], v0, s[44:47], s53 offen sc1\n"
"buffer_load_dwordx2 v[146:147], v188, s[44:47], s53 offen offset:1024 sc1\n"
"s_waitcnt vmcnt(0)\n"
"v_max3_u32 v25, v138, v139, v140\n"
"v_max3_u32 v25, v25, v141, v142\n"
"v_max3_u32 v25, v25, v143, v144\n"
"v_max3_u32 v25, v25, v145, v146\n"
"v_max_u32 v25, v25, v147\n"
"v_cmp_eq_u32 vcc, -1, v25\n"
"s_nop 1\n"
"s_cbranch_vccz LB4P1%=\n"
"s_sub_u32 s77, s77, 1\n"
"s_cbranch_scc0 LL4P1%=\n"
"s_branch LB4P1%=\n"
"LEND%=:\n"
    :
    : "s"(__builtin_amdgcn_kernarg_segment_ptr()), "s"(blockIdx.x), "s"(w)
    : "v0", "v1", "v2", "v3", "v4", "v5", "v6", "v7", "v8", "v9", "v10", "v11", "v12", "v13", "v14", "v15", "v16", "v17", "v18", "v19", "v20", "v21", "v22", "v23", "v24", "v25", "v26", "v27", "v28", "v29", "v30", "v31", "v32", "v33", "v34", "v35", "v36", "v37", "v38", "v39", "v40", "v41", "v42", "v43", "v44", "v45", "v46", "v47", "v48", "v49", "v50", "v51", "v52", "v53", "v54", "v55", "v56", "v57", "v58", "v59", "v60", "v61", "v62", "v63", "v64", "v65", "v66", "v67", "v68", "v69", "v70", "v71", "v72", "v73", "v74", "v75", "v76", "v77", "v78", "v79", "v80", "v81", "v82", "v83", "v84", "v85", "v86", "v87", "v88", "v89", "v90", "v91", "v92", "v93", "v94", "v95", "v96", "v97", "v98", "v99", "v100", "v101", "v102", "v103", "v104", "v105", "v106", "v107", "v108", "v109", "v110", "v111", "v112", "v113", "v114", "v115", "v116", "v117", "v118", "v119", "v120", "v121", "v122", "v123", "v124", "v125", "v126", "v127", "v128", "v129", "v130", "v131", "v132", "v133", "v134", "v135", "v136", "v137", "v138", "v139", "v140", "v141", "v142", "v143", "v144", "v145", "v146", "v147", "v148", "v149", "v150", "v151", "v152", "v153", "v154", "v155", "v156", "v157", "v158", "v159", "v160", "v161", "v162", "v163", "v164", "v165", "v166", "v167", "v168", "v169", "v170", "v171", "v172", "v173", "v174", "v175", "v176", "v177", "v178", "v179", "v180", "v181", "v182", "v183", "v184", "v185", "v186", "v187", "v188", "v189", "v190", "v191", "v192", "v193", "v194", "v195", "v196", "v197", "v198", "v199", "v200", "v201", "v202", "v203", "v204", "v205", "v206", "v207", "v208", "v209", "v210", "v211", "v212", "v213", "v214", "v215", "v216", "v217", "v218", "v219", "v220", "v221", "v222", "v223", "v224", "v225", "v226", "v227", "v228", "v229", "v230", "v231", "v232", "v233", "v234", "v235", "v236", "v237", "v238", "v239", "v240", "v241", "v242", "v243", "v244", "v245", "v246", "v247", "v248", "v249", "v250", "v251", "v252", "v253", "v254", "v255", "a0", "a1", "a2", "a3", "a4", "a5", "a6", "a7", "a8", "a9", "a10", "a11", "a12", "a13", "a14", "a15", "a16", "a17", "a18", "a19", "a20", "a21", "a22", "a23", "a24", "a25", "a26", "a27", "a28", "a29", "a30", "a31", "a32", "a33", "a34", "a35", "a36", "a37", "a38", "a39", "a40", "a41", "a42", "a43", "a44", "a45", "a46", "a47", "a48", "a49", "a50", "a51", "a52", "a53", "a54", "a55", "a56", "a57", "a58", "a59", "a60", "a61", "a62", "a63", "a64", "a65", "a66", "a67", "a68", "a69", "a70", "a71", "a72", "a73", "a74", "a75", "a76", "a77", "a78", "a79", "a80", "a81", "a82", "a83", "a84", "a85", "a86", "a87", "a88", "a89", "a90", "a91", "a92", "a93", "a94", "a95", "a96", "a97", "a98", "a99", "a100", "a101", "a102", "a103", "a104", "a105", "a106", "a107", "a108", "a109", "a110", "a111", "a112", "a113", "a114", "a115", "a116", "a117", "a118", "a119", "a120", "a121", "a122", "a123", "a124", "a125", "a126", "a127", "a128", "a129", "a130", "a131", "a132", "a133", "a134", "a135", "a136", "a137", "a138", "a139", "a140", "a141", "a142", "a143", "a144", "a145", "a146", "a147", "a148", "a149", "a150", "a151", "a152", "a153", "a154", "a155", "a156", "a157", "a158", "a159", "a160", "a161", "a162", "a163", "a164", "a165", "a166", "a167", "a168", "a169", "a170", "a171", "a172", "a173", "a174", "a175", "a176", "a177", "a178", "a179", "a180", "a181", "a182", "a183", "a184", "a185", "a186", "a187", "a188", "a189", "a190", "a191", "a192", "a193", "a194", "a195", "a196", "a197", "a198", "a199", "a200", "a201", "a202", "a203", "a204", "a205", "a206", "a207", "a208", "a209", "a210", "a211", "a212", "a213", "a214", "a215", "a216", "a217", "a218", "a219", "a220", "a221", "a222", "a223", "a224", "a225", "a226", "a227", "a228", "a229", "a230", "a231", "a232", "a233", "a234", "a235", "a236", "a237", "a238", "a239", "a240", "a241", "a242", "a243", "a244", "a245", "a246", "a247", "a248", "a249", "a250", "a251", "a252", "a253", "a254", "a255", "s6", "s7", "s8", "s9", "s10", "s11", "s12", "s13", "s14", "s15", "s16", "s17", "s18", "s19", "s20", "s21", "s22", "s23", "s24", "s25", "s26", "s27", "s28", "s29", "s30", "s31", "s33", "s34", "s35", "s36", "s37", "s38", "s39", "s40", "s41", "s42", "s43", "s44", "s45", "s46", "s47", "s48", "s49", "s50", "s51", "s52", "s53", "s54", "s55", "s56", "s57", "s58", "s59", "s60", "s61", "s62", "s63", "s64", "s65", "s66", "s67", "s68", "s69", "s70", "s71", "s72", "s73", "s74", "s75", "s76", "s77", "s78", "s79", "s80", "s81", "s82", "s83", "s84", "s85", "s86", "s87", "s88", "s89", "s90", "s91", "s92", "s93", "s94", "s95", "s96", "s97", "s98", "s99", "vcc", "scc", "memory");
}


extern "C" void kernel_launch(void* const* d_in, const int* in_sizes, int n_in,
                              void* d_out, int out_size, void* d_ws, size_t ws_size,
                              hipStream_t stream) {
  (void)hipFuncSetAttribute((const void*)fused_k, hipFuncAttributeMaxDynamicSharedMemorySize, 163840);
  fused_k<<<256, 256, 163840, stream>>>((const char*)d_in[0], (const char*)d_in[1], (const char*)d_in[2], (const char*)d_in[3], (const char*)d_in[4],
                                       (const char*)d_in[5], (const char*)d_in[6], (const char*)d_in[7], (const char*)d_in[8], (const char*)d_in[9],
                                       (char*)d_ws, (char*)d_out);
}
```
